```python
import torch

def _softmax(x, axis=-1):
    return torch.softmax(x, dim=axis)
def _gelu(x):
    return torch.nn.functional.gelu(x, approximate="tanh")
def _silu(x):
    return torch.nn.functional.silu(x)
def _sigmoid(x):
    return torch.sigmoid(x)
def _layernorm(x, axis=-1, eps=1e-5):
    m = x.mean(dim=axis, keepdim=True)
    v = x.var(dim=axis, keepdim=True, unbiased=False)
    return (x - m) / torch.sqrt(v + eps)
def _scatter_add(tgt, idx, src, axis=0):
    return tgt.clone().index_add_(axis, idx, src)
def _segment_sum(src, seg, n):
    out = torch.zeros((n,) + src.shape[1:], dtype=src.dtype)
    return out.index_add_(0, seg, src)
def _pad(x, pw):
    flat = tuple(v for pair in reversed(pw) for v in pair)
    return torch.nn.functional.pad(x, flat)

kernel_name = 'parallel_sum__compressed_kv_cla__dense_activation_silu__prompt'

def setup_inputs():

    _w = (lambda a: (lambda t: t.to(torch.bfloat16) if t.is_floating_point() else t)(torch.as_tensor(a)))
    def _A_setup():
        B, H, Sq, Sk, D, M = 1, 16, 2048, 2048, 256, 448
        Dc = 4 * D
        return {
            "x": _w((lambda *s: torch.randn(*s, dtype=torch.bfloat16) * 0.05)(B, Sq, M)),
            "c_kv": _w((lambda *s: torch.randn(*s, dtype=torch.bfloat16) * 0.4)(B, Sk, Dc)),
            "w_q": _w((lambda *s: torch.randn(*s, dtype=torch.bfloat16) * 3.2)(M, H, D) * (M ** -0.5)),
            "w_uk": _w((lambda *s: torch.randn(*s, dtype=torch.bfloat16) * 3.2)(Dc, H, D) * (Dc ** -0.5)),
            "w_uv": _w((lambda *s: torch.randn(*s, dtype=torch.bfloat16) * 0.4)(Dc, H, D) * (Dc ** -0.5)),
            "w_o": _w((lambda *s: torch.randn(*s, dtype=torch.bfloat16) * 0.4)(H, D, M) * ((H * D) ** -0.5)),
        }
    _A_in = _A_setup()

    def _B_setup():
        B, S, D, F = 1, 2048, 448, 1792
        return {
            "x": _w((lambda *s: torch.randn(*s, dtype=torch.bfloat16) * 0.05)(B, S, D)),
            "w1": _w((lambda *s: torch.randn(*s, dtype=torch.bfloat16) * 0.8)(D, F) * (D ** -0.5)),
            "w2": _w((lambda *s: torch.randn(*s, dtype=torch.bfloat16) * 0.1)(F, D) * (F ** -0.5)),
        }
    _B_in = _B_setup()

    return {
        "A_x": _A_in["x"],
        "A_c_kv": _A_in["c_kv"],
        "A_w_q": _A_in["w_q"],
        "A_w_uk": _A_in["w_uk"],
        "A_w_uv": _A_in["w_uv"],
        "A_w_o": _A_in["w_o"],
        "B_x": _B_in["x"],
        "B_w1": _B_in["w1"],
        "B_w2": _B_in["w2"],
    }

def reference(A_x, A_c_kv, A_w_q, A_w_uk, A_w_uv, A_w_o, B_x, B_w1, B_w2):
    def _A_ref(x, c_kv, w_q, w_uk, w_uv, w_o):
        B, Sq, M = x.shape[0], x.shape[1], x.shape[2]
        Sk = c_kv.shape[1]
        H, D = w_q.shape[1], w_q.shape[2]
        q = torch.einsum("bqm,mhd->bhqd", x, w_q)
        k = torch.einsum("bkc,chd->bhkd", c_kv, w_uk)
        v = torch.einsum("bkc,chd->bhkd", c_kv, w_uv)
        scores = torch.einsum("bhqd,bhkd->bhqk", q, k) * (D ** -0.5)
        off = Sk - Sq
        qi = torch.arange(Sq)[:, None]
        kj = torch.arange(Sk)[None, :]
        cmask = kj <= (qi + off)
        scores = torch.where(cmask[None, None], scores, -1e9)
        p = _softmax(scores, axis=-1)
        ctx = torch.einsum("bhqk,bhkd->bhqd", p, v)
        return torch.einsum("bhqd,hdm->bqm", ctx, w_o)

    def _B_ref(x, w1, w2):
        h = torch.einsum("bsd,df->bsf", x, w1)
        h = _silu(h)
        out = torch.einsum("bsf,fd->bsd", h, w2)
        return out

    x = A_x
    xn = _layernorm(x, axis=-1)
    return x + _A_ref(xn, A_c_kv, A_w_q, A_w_uk, A_w_uv, A_w_o) + _B_ref(xn, B_w1, B_w2)
```

```cpp
#include <hip/hip_runtime.h>
#include <hip/hip_bf16.h>
#include <stdint.h>

__global__ __launch_bounds__(256,1) void k1_proj(const void* x,const void* c,const void* wq,const void* wk,const void* wv,const void* w1,void* ws){
  unsigned tid=threadIdx.x; unsigned bid=blockIdx.x;
  asm volatile(
"LK1B:\n"
"s_mov_b64 s[60:61], %0\n"
"s_mov_b64 s[62:63], %1\n"
"s_mov_b64 s[64:65], %2\n"
"s_mov_b64 s[66:67], %3\n"
"s_mov_b64 s[68:69], %4\n"
"s_mov_b64 s[70:71], %5\n"
"s_mov_b64 s[72:73], %6\n"
"s_mov_b32 s74, %7\n"
"v_lshrrev_b32 v29, 6, v0\n"
"s_nop 0\n"
"v_readfirstlane_b32 s44, v29\n"
"v_and_b32 v29, 63, v0\n"
"s_and_b32 s50, s74, 7\n"
"s_lshr_b32 s49, s74, 3\n"
"s_lshl_b32 s45, s44, 13\n"
"s_mov_b32 s20, s62\n"
"s_and_b32 s21, s63, 0xffff\n"
"s_mov_b32 s22, 0x80000\n"
"s_mov_b32 s23, 0x20000\n"
"s_lshr_b32 s43, s49, 2\n"
"s_lshl_b32 s43, s43, 19\n"
"s_add_u32 s20, s20, s43\n"
"s_addc_u32 s21, s21, 0\n"
"s_bitcmp1_b32 s49, 1\n"
"s_cselect_b32 s43, s68, s66\n"
"s_cselect_b32 s47, s69, s67\n"
"s_mov_b32 s24, s43\n"
"s_and_b32 s25, s47, 0xffff\n"
"s_mov_b32 s26, 0x800000\n"
"s_mov_b32 s27, 0x20000\n"
"s_and_b32 s43, s49, 1\n"
"s_lshl_b32 s47, s50, 1\n"
"s_add_u32 s51, s43, s47\n"
"s_lshl_b32 s43, s51, 9\n"
"s_add_u32 s24, s24, s43\n"
"s_addc_u32 s25, s25, 0\n"
"v_lshrrev_b32 v30, 3, v29\n"
"v_lshrrev_b32 v31, 4, v29\n"
"v_and_b32 v17, 7, v29\n"
"v_xor_b32 v18, v17, v31\n"
"v_lshlrev_b32 v19, 11, v30\n"
"s_lshl_b32 s43, s44, 17\n"
"v_add_u32 v19, s43, v19\n"
"v_lshl_add_u32 v20, v18, 4, v19\n"
"v_xor_b32 v21, 64, v20\n"
"v_add_u32 v1, 0x0, v20\n"
"v_add_u32 v2, 0x3c00, v21\n"
"v_add_u32 v3, 0x7800, v20\n"
"v_add_u32 v4, 0xb400, v21\n"
"v_add_u32 v5, 0x10000, v20\n"
"v_add_u32 v6, 0x13c00, v21\n"
"v_add_u32 v7, 0x17800, v20\n"
"v_add_u32 v8, 0x1b400, v21\n"
"v_lshrrev_b32 v18, 1, v17\n"
"v_and_b32 v19, 1, v31\n"
"v_xor_b32 v18, v18, v19\n"
"v_lshlrev_b32 v18, 5, v18\n"
"v_and_b32 v19, 1, v29\n"
"v_lshl_add_u32 v18, v19, 4, v18\n"
"v_lshl_add_u32 v18, v30, 13, v18\n"
"s_lshl_b32 s43, s44, 7\n"
"v_add_u32 v20, s43, v18\n"
"v_xor_b32 v21, 64, v20\n"
"v_add_u32 v9, 0x0, v20\n"
"v_add_u32 v10, 0xfc00, v21\n"
"v_add_u32 v11, 0x1f800, v20\n"
"v_add_u32 v12, 0x2f400, v21\n"
"v_add_u32 v13, 0x40000, v20\n"
"v_add_u32 v14, 0x4fc00, v21\n"
"v_add_u32 v15, 0x5f800, v20\n"
"v_add_u32 v16, 0x6f400, v21\n"
"s_mov_b32 s36, 0\n"
"s_mov_b32 s37, 0\n"
"s_add_u32 m0, s45, 0\n"
"s_nop 0\n"
"buffer_load_dwordx4 v9, s[24:27], s37 offen offset:0 lds\n"
"buffer_load_dwordx4 v10, s[24:27], s37 offen offset:1024 lds\n"
"buffer_load_dwordx4 v11, s[24:27], s37 offen offset:2048 lds\n"
"buffer_load_dwordx4 v12, s[24:27], s37 offen offset:3072 lds\n"
"s_add_u32 m0, s45, 4096\n"
"s_nop 0\n"
"buffer_load_dwordx4 v13, s[24:27], s37 offen offset:0 lds\n"
"buffer_load_dwordx4 v14, s[24:27], s37 offen offset:1024 lds\n"
"buffer_load_dwordx4 v15, s[24:27], s37 offen offset:2048 lds\n"
"buffer_load_dwordx4 v16, s[24:27], s37 offen offset:3072 lds\n"
"s_add_u32 s37, s37, 524288\n"
"s_add_u32 m0, s45, 32768\n"
"s_nop 0\n"
"buffer_load_dwordx4 v1, s[20:23], s36 offen offset:0 lds\n"
"buffer_load_dwordx4 v2, s[20:23], s36 offen offset:1024 lds\n"
"buffer_load_dwordx4 v3, s[20:23], s36 offen offset:2048 lds\n"
"buffer_load_dwordx4 v4, s[20:23], s36 offen offset:3072 lds\n"
"s_add_u32 m0, s45, 36864\n"
"s_nop 0\n"
"buffer_load_dwordx4 v5, s[20:23], s36 offen offset:0 lds\n"
"buffer_load_dwordx4 v6, s[20:23], s36 offen offset:1024 lds\n"
"buffer_load_dwordx4 v7, s[20:23], s36 offen offset:2048 lds\n"
"buffer_load_dwordx4 v8, s[20:23], s36 offen offset:3072 lds\n"
"s_add_u32 s36, s36, 128\n"
"s_add_u32 m0, s45, 65536\n"
"s_nop 0\n"
"buffer_load_dwordx4 v9, s[24:27], s37 offen offset:0 lds\n"
"buffer_load_dwordx4 v10, s[24:27], s37 offen offset:1024 lds\n"
"buffer_load_dwordx4 v11, s[24:27], s37 offen offset:2048 lds\n"
"buffer_load_dwordx4 v12, s[24:27], s37 offen offset:3072 lds\n"
"s_add_u32 m0, s45, 69632\n"
"s_nop 0\n"
"buffer_load_dwordx4 v13, s[24:27], s37 offen offset:0 lds\n"
"buffer_load_dwordx4 v14, s[24:27], s37 offen offset:1024 lds\n"
"buffer_load_dwordx4 v15, s[24:27], s37 offen offset:2048 lds\n"
"buffer_load_dwordx4 v16, s[24:27], s37 offen offset:3072 lds\n"
"s_add_u32 s37, s37, 524288\n"
"s_add_u32 m0, s45, 98304\n"
"s_nop 0\n"
"buffer_load_dwordx4 v1, s[20:23], s36 offen offset:0 lds\n"
"buffer_load_dwordx4 v2, s[20:23], s36 offen offset:1024 lds\n"
"buffer_load_dwordx4 v3, s[20:23], s36 offen offset:2048 lds\n"
"buffer_load_dwordx4 v4, s[20:23], s36 offen offset:3072 lds\n"
"s_add_u32 m0, s45, 102400\n"
"s_nop 0\n"
"buffer_load_dwordx4 v5, s[20:23], s36 offen offset:0 lds\n"
"buffer_load_dwordx4 v6, s[20:23], s36 offen offset:1024 lds\n"
"buffer_load_dwordx4 v7, s[20:23], s36 offen offset:2048 lds\n"
"buffer_load_dwordx4 v8, s[20:23], s36 offen offset:3072 lds\n"
"s_add_u32 s36, s36, 128\n"
"s_add_u32 m0, s45, 131072\n"
"s_nop 0\n"
"buffer_load_dwordx4 v9, s[24:27], s37 offen offset:0 lds\n"
"buffer_load_dwordx4 v10, s[24:27], s37 offen offset:1024 lds\n"
"buffer_load_dwordx4 v11, s[24:27], s37 offen offset:2048 lds\n"
"buffer_load_dwordx4 v12, s[24:27], s37 offen offset:3072 lds\n"
"s_add_u32 m0, s45, 135168\n"
"s_nop 0\n"
"v_and_b32 v30, 15, v29\n"
"v_lshrrev_b32 v17, 1, v30\n"
"v_xor_b32 v17, v17, v31\n"
"v_lshlrev_b32 v17, 4, v17\n"
"v_lshl_add_u32 v17, v30, 7, v17\n"
"s_lshr_b32 s43, s44, 1\n"
"s_lshl_b32 s43, s43, 14\n"
"v_add_u32 v17, s43, v17\n"
"v_xor_b32 v18, 64, v17\n"
"v_lshrrev_b32 v25, 2, v30\n"
"v_lshl_add_u32 v25, v31, 3, v25\n"
"v_lshlrev_b32 v25, 7, v25\n"
"v_and_b32 v26, 3, v30\n"
"v_lshl_add_u32 v25, v26, 3, v25\n"
"v_lshrrev_b32 v26, 3, v30\n"
"v_and_b32 v27, 1, v31\n"
"v_lshl_add_u32 v26, v27, 1, v26\n"
"v_lshl_add_u32 v25, v26, 5, v25\n"
"s_and_b32 s43, s44, 1\n"
"s_lshl_b32 s43, s43, 14\n"
"v_add_u32 v21, s43, v25\n"
"v_xor_b32 v22, 32, v21\n"
"v_xor_b32 v23, 64, v21\n"
"v_xor_b32 v24, 96, v21\n"
"v_mov_b32 v25, v21\n"
"v_mov_b32 v26, v22\n"
"v_mov_b32 v27, v23\n"
"v_mov_b32 v28, v24\n"
"v_add_u32 v19, 32768, v17\n"
"v_add_u32 v20, 32768, v18\n"
"s_lshl_b32 s43, s74, 2\n"
"s_add_u32 s43, s43, s44\n"
"v_lshlrev_b32 v32, 6, v29\n"
"v_lshlrev_b32 v33, 5, v29\n"
"v_lshlrev_b32 v34, 7, v29\n"
"s_mov_b32 s76, s60\n"
"s_and_b32 s77, s61, 0xffff\n"
"s_mov_b32 s78, 1835008\n"
"s_mov_b32 s79, 0x20000\n"
"s_mul_i32 s47, s43, 1792\n"
"buffer_load_dword a216, v33, s[76:79], s47 offen\n"
"s_mov_b32 s76, s64\n"
"s_and_b32 s77, s65, 0xffff\n"
"s_mov_b32 s78, 3670016\n"
"s_mov_b32 s79, 0x20000\n"
"s_mul_i32 s47, s43, 3584\n"
"buffer_load_dword a217, v32, s[76:79], s47 offen\n"
"s_mov_b32 s76, s70\n"
"s_and_b32 s77, s71, 0xffff\n"
"s_mov_b32 s78, 1605632\n"
"s_mov_b32 s79, 0x20000\n"
"s_mul_i32 s47, s43, 1568\n"
"buffer_load_dword a218, v33, s[76:79], s47 offen\n"
"s_getpc_b64 s[76:77]\n"
"LPC%=:\n"
"s_sub_u32 s76, s76, LPC%=-LK1B\n"
"s_subb_u32 s77, s77, 0\n"
"s_and_b32 s77, s77, 0xffff\n"
"s_mov_b32 s78, LK2E-LK1B\n"
"s_lshl_b32 s47, s49, 2\n"
"s_add_u32 s47, s47, s44\n"
"s_and_b32 s47, s47, 15\n"
"s_lshl_b32 s47, s47, 13\n"
"buffer_load_dword a219, v34, s[76:79], s47 offen\n"
"s_mov_b32 s41, 0x150e0700\n"
"s_mov_b32 s42, 0x2e28221c\n"
"s_cmp_ge_u32 s51, 8\n"
"s_cselect_b32 s41, 0x5003a34, s41\n"
"s_cselect_b32 s42, 0x18140f0a, s42\n"
"s_mov_b32 s86, 0x66667777\n"
"s_cselect_b32 s86, 0x44555566, s86\n"
"s_bitcmp1_b32 s51, 2\n"
"s_cselect_b32 s41, s42, s41\n"
"s_and_b32 s43, s51, 3\n"
"s_lshl_b32 s43, s43, 3\n"
"s_lshr_b32 s52, s41, s43\n"
"s_and_b32 s52, s52, 255\n"
"s_and_b32 s43, s51, 7\n"
"s_lshl_b32 s43, s43, 2\n"
"s_lshr_b32 s86, s86, s43\n"
"s_and_b32 s86, s86, 15\n"
"s_cmp_ge_u32 s51, 10\n"
"s_cselect_b32 s54, 1, 0\n"
"s_cselect_b32 s43, s70, s64\n"
"s_cselect_b32 s47, s71, s65\n"
"s_mov_b32 s55, 3670016\n"
"s_cselect_b32 s55, 1605632, s55\n"
"s_mov_b32 s42, 8192\n"
"s_cselect_b32 s42, 3584, s42\n"
"s_mov_b32 s80, s43\n"
"s_and_b32 s81, s47, 0xffff\n"
"s_mov_b32 s82, s55\n"
"s_mov_b32 s83, 0x20000\n"
"v_lshrrev_b32 v30, 3, v29\n"
"s_mul_i32 s43, s44, 56\n"
"v_add_u32 v30, s43, v30\n"
"v_mul_lo_u32 v30, v30, s42\n"
"v_bfe_u32 v31, v29, 1, 2\n"
"v_bfe_u32 v32, v29, 4, 1\n"
"v_xor_b32 v31, v31, v32\n"
"s_and_b32 s43, s44, 1\n"
"s_lshl_b32 s43, s43, 1\n"
"v_xor_b32 v31, s43, v31\n"
"v_lshlrev_b32 v31, 5, v31\n"
"v_and_b32 v32, 1, v29\n"
"v_lshl_add_u32 v31, v32, 4, v31\n"
"v_xor_b32 v32, 64, v31\n"
"v_add_u32 v31, v31, v30\n"
"v_add_u32 v32, v32, v30\n"
"s_lshl_b32 s43, s42, 3\n"
"s_mov_b32 s47, 0\n"
"s_sub_u32 s41, s47, 0\n"
"v_add_u32 v33, s41, v31\n"
"s_add_u32 s47, s47, s43\n"
"v_accvgpr_write_b32 a144, v33\n"
"s_sub_u32 s41, s47, 1024\n"
"v_add_u32 v33, s41, v32\n"
"s_add_u32 s47, s47, s43\n"
"v_accvgpr_write_b32 a145, v33\n"
"s_sub_u32 s41, s47, 2048\n"
"v_add_u32 v33, s41, v31\n"
"s_add_u32 s47, s47, s43\n"
"v_accvgpr_write_b32 a146, v33\n"
"s_sub_u32 s41, s47, 3072\n"
"v_add_u32 v33, s41, v32\n"
"s_add_u32 s47, s47, s43\n"
"v_accvgpr_write_b32 a147, v33\n"
"s_sub_u32 s41, s47, 0\n"
"v_add_u32 v33, s41, v31\n"
"s_add_u32 s47, s47, s43\n"
"v_accvgpr_write_b32 a148, v33\n"
"s_sub_u32 s41, s47, 1024\n"
"v_add_u32 v33, s41, v32\n"
"s_add_u32 s47, s47, s43\n"
"v_accvgpr_write_b32 a149, v33\n"
"s_sub_u32 s41, s47, 2048\n"
"v_add_u32 v33, s41, v31\n"
"s_add_u32 s47, s47, s43\n"
"v_accvgpr_write_b32 a150, v33\n"
"s_mul_i32 s56, s42, 224\n"
"s_sub_u32 s87, 128, s56\n"
"s_lshl_b32 s84, s52, 7\n"
"s_mul_i32 s85, s44, 7168\n"
"s_mov_b32 s28, s72\n"
"s_and_b32 s29, s73, 0xffff\n"
"s_mov_b32 s30, 0x100000\n"
"s_mov_b32 s31, 0x20000\n"
"s_add_u32 s28, s28, 100663296\n"
"s_addc_u32 s29, s29, 0\n"
"s_lshl_b32 s43, s74, 8\n"
"v_mov_b32 v30, 0\n"
"v_lshlrev_b32 v31, 2, v29\n"
"buffer_store_dword v30, v31, s[28:31], s43 offen\n"
"s_mov_b32 s76, s72\n"
"s_and_b32 s77, s73, 0xffff\n"
"s_mov_b32 s78, 0x100000\n"
"s_mov_b32 s79, 0x20000\n"
"s_lshl_b32 s43, s51, 20\n"
"s_bitcmp1_b32 s49, 1\n"
"s_cselect_b32 s47, 16777216, 0\n"
"s_cselect_b32 s41, 14, 12\n"
"s_add_u32 s43, s43, s47\n"
"s_add_u32 s76, s76, s43\n"
"s_addc_u32 s77, s77, 0\n"
"s_lshr_b32 s43, s49, 2\n"
"s_lshl_b32 s43, s43, 17\n"
"s_lshr_b32 s47, s44, 1\n"
"s_lshl_b32 s47, s47, 16\n"
"s_add_u32 s43, s43, s47\n"
"s_and_b32 s47, s44, 1\n"
"s_lshl_b32 s47, s47, s41\n"
"s_add_u32 s43, s43, s47\n"
"v_lshlrev_b32 v34, 4, v29\n"
"v_add_u32 v33, s43, v34\n"
"s_mov_b32 s57, 8192\n"
"v_accvgpr_write_b32 a151, v33\n"
"s_mov_b32 s75, 32768\n"
"s_mov_b32 s97, 40960\n"
"s_mov_b32 s32, s60\n"
"s_and_b32 s33, s61, 0xffff\n"
"s_mov_b32 s34, 1835008\n"
"s_mov_b32 s35, 0x20000\n"
"s_lshr_b32 s43, s49, 1\n"
"s_lshl_b32 s43, s43, 7\n"
"s_lshl_b32 s47, s44, 5\n"
"s_add_u32 s43, s43, s47\n"
"v_lshrrev_b32 v30, 3, v29\n"
"v_add_u32 v30, s43, v30\n"
"v_mul_u32_u24 v30, 896, v30\n"
"v_lshrrev_b32 v31, 4, v29\n"
"v_and_b32 v32, 7, v29\n"
"v_xor_b32 v31, v31, v32\n"
"v_lshlrev_b32 v31, 4, v31\n"
"v_xor_b32 v32, 64, v31\n"
"v_add_u32 v31, v31, v30\n"
"v_add_u32 v32, v32, v30\n"
"v_add_u32 v33, 0, v31\n"
"v_accvgpr_write_b32 a220, v33\n"
"v_add_u32 v35, 6144, v32\n"
"v_accvgpr_write_b32 a221, v35\n"
"v_add_u32 v33, 12288, v31\n"
"v_accvgpr_write_b32 a222, v33\n"
"v_add_u32 v35, 18432, v32\n"
"v_accvgpr_write_b32 a223, v35\n"
"v_add_u32 v33, 128, v31\n"
"v_accvgpr_write_b32 a224, v33\n"
"v_add_u32 v35, 6272, v32\n"
"v_accvgpr_write_b32 a225, v35\n"
"v_add_u32 v33, 12416, v31\n"
"v_accvgpr_write_b32 a226, v33\n"
"v_add_u32 v35, 18560, v32\n"
"v_accvgpr_write_b32 a227, v35\n"
"s_lshr_b32 s43, s44, 1\n"
"s_lshl_b32 s43, s43, 14\n"
"s_sub_u32 s43, s45, s43\n"
"v_add_u32 v33, s43, v17\n"
"s_mov_b32 s60, 128\n"
"v_accvgpr_write_b32 a152, v33\n"
"s_mov_b32 s61, 524288\n"
"s_mov_b32 s62, 0\n"
"s_nop 0\n"
"s_mov_b32 s28, s72\n"
"s_and_b32 s29, s73, 0xffff\n"
"s_mov_b32 s30, 0\n"
"s_mov_b32 s31, 0x20000\n"
"s_cmp_eq_u32 s54, 1\n"
"s_mov_b32 s43, 33554432\n"
"s_cselect_b32 s43, 50331648, s43\n"
"s_mov_b32 s47, 8192\n"
"s_cselect_b32 s47, 57344, s47\n"
"s_add_u32 s28, s28, s43\n"
"s_addc_u32 s29, s29, 0\n"
"s_lshr_b32 s43, s49, 1\n"
"s_lshl_b32 s43, s43, 2\n"
"s_add_u32 s43, s43, s44\n"
"s_lshl_b32 s43, s43, 1\n"
"s_mul_i32 s43, s43, s47\n"
"v_add_u32 v33, s43, v34\n"
"v_add_u32 v32, s47, v33\n"
"v_accvgpr_write_b32 a153, v33\n"
"v_accvgpr_write_b32 a154, v32\n"
"v_mov_b32 v33, 0x3f803f80\n"
"s_and_b32 s43, s44, 1\n"
"s_lshl_b32 s43, s43, 14\n"
"v_accvgpr_write_b32 a196, v33\n"
"v_accvgpr_write_b32 a197, v33\n"
"v_accvgpr_write_b32 a198, v33\n"
"v_accvgpr_write_b32 a199, v33\n"
"v_subrev_u32 v30, s43, v21\n"
"v_accvgpr_write_b32 a192, v30\n"
"v_subrev_u32 v31, s43, v22\n"
"v_accvgpr_write_b32 a193, v31\n"
"v_subrev_u32 v30, s43, v23\n"
"v_accvgpr_write_b32 a194, v30\n"
"v_subrev_u32 v31, s43, v24\n"
"v_accvgpr_write_b32 a195, v31\n"
"s_getpc_b64 s[88:89]\n"
"LPD%=:\n"
"s_cmp_eq_u32 s54, 1\n"
"s_mov_b32 s43, LSTQ%=-LPD%=\n"
"s_cselect_b32 s43, LSTB%=-LPD%=, s43\n"
"s_add_u32 s88, s88, s43\n"
"s_addc_u32 s89, s89, 0\n"
"v_mov_b32 v32, 0\n"
"v_mov_b32 v33, 0\n"
"v_mov_b32 v34, 0\n"
"v_mov_b32 v35, 0\n"
"v_mov_b32 v36, 0\n"
"v_mov_b32 v37, 0\n"
"v_mov_b32 v38, 0\n"
"v_mov_b32 v39, 0\n"
"v_mov_b32 v40, 0\n"
"v_mov_b32 v41, 0\n"
"v_mov_b32 v42, 0\n"
"v_mov_b32 v43, 0\n"
"v_mov_b32 v44, 0\n"
"v_mov_b32 v45, 0\n"
"v_mov_b32 v46, 0\n"
"v_mov_b32 v47, 0\n"
"v_mov_b32 v48, 0\n"
"v_mov_b32 v49, 0\n"
"v_mov_b32 v50, 0\n"
"v_mov_b32 v51, 0\n"
"v_mov_b32 v52, 0\n"
"v_mov_b32 v53, 0\n"
"v_mov_b32 v54, 0\n"
"v_mov_b32 v55, 0\n"
"v_mov_b32 v56, 0\n"
"v_mov_b32 v57, 0\n"
"v_mov_b32 v58, 0\n"
"v_mov_b32 v59, 0\n"
"v_mov_b32 v60, 0\n"
"v_mov_b32 v61, 0\n"
"v_mov_b32 v62, 0\n"
"v_mov_b32 v63, 0\n"
"v_mov_b32 v64, 0\n"
"v_mov_b32 v65, 0\n"
"v_mov_b32 v66, 0\n"
"v_mov_b32 v67, 0\n"
"v_mov_b32 v68, 0\n"
"v_mov_b32 v69, 0\n"
"v_mov_b32 v70, 0\n"
"v_mov_b32 v71, 0\n"
"v_mov_b32 v72, 0\n"
"v_mov_b32 v73, 0\n"
"v_mov_b32 v74, 0\n"
"v_mov_b32 v75, 0\n"
"v_mov_b32 v76, 0\n"
"v_mov_b32 v77, 0\n"
"v_mov_b32 v78, 0\n"
"v_mov_b32 v79, 0\n"
"v_mov_b32 v80, 0\n"
"v_mov_b32 v81, 0\n"
"v_mov_b32 v82, 0\n"
"v_mov_b32 v83, 0\n"
"v_mov_b32 v84, 0\n"
"v_mov_b32 v85, 0\n"
"v_mov_b32 v86, 0\n"
"v_mov_b32 v87, 0\n"
"v_mov_b32 v88, 0\n"
"v_mov_b32 v89, 0\n"
"v_mov_b32 v90, 0\n"
"v_mov_b32 v91, 0\n"
"v_mov_b32 v92, 0\n"
"v_mov_b32 v93, 0\n"
"v_mov_b32 v94, 0\n"
"v_mov_b32 v95, 0\n"
"v_mov_b32 v96, 0\n"
"v_mov_b32 v97, 0\n"
"v_mov_b32 v98, 0\n"
"v_mov_b32 v99, 0\n"
"v_mov_b32 v100, 0\n"
"v_mov_b32 v101, 0\n"
"v_mov_b32 v102, 0\n"
"v_mov_b32 v103, 0\n"
"v_mov_b32 v104, 0\n"
"v_mov_b32 v105, 0\n"
"v_mov_b32 v106, 0\n"
"v_mov_b32 v107, 0\n"
"v_mov_b32 v108, 0\n"
"v_mov_b32 v109, 0\n"
"v_mov_b32 v110, 0\n"
"v_mov_b32 v111, 0\n"
"v_mov_b32 v112, 0\n"
"v_mov_b32 v113, 0\n"
"v_mov_b32 v114, 0\n"
"v_mov_b32 v115, 0\n"
"v_mov_b32 v116, 0\n"
"v_mov_b32 v117, 0\n"
"v_mov_b32 v118, 0\n"
"v_mov_b32 v119, 0\n"
"v_mov_b32 v120, 0\n"
"v_mov_b32 v121, 0\n"
"v_mov_b32 v122, 0\n"
"v_mov_b32 v123, 0\n"
"v_mov_b32 v124, 0\n"
"v_mov_b32 v125, 0\n"
"v_mov_b32 v126, 0\n"
"v_mov_b32 v127, 0\n"
"v_mov_b32 v128, 0\n"
"v_mov_b32 v129, 0\n"
"v_mov_b32 v130, 0\n"
"v_mov_b32 v131, 0\n"
"v_mov_b32 v132, 0\n"
"v_mov_b32 v133, 0\n"
"v_mov_b32 v134, 0\n"
"v_mov_b32 v135, 0\n"
"v_mov_b32 v136, 0\n"
"v_mov_b32 v137, 0\n"
"v_mov_b32 v138, 0\n"
"v_mov_b32 v139, 0\n"
"v_mov_b32 v140, 0\n"
"v_mov_b32 v141, 0\n"
"v_mov_b32 v142, 0\n"
"v_mov_b32 v143, 0\n"
"v_mov_b32 v144, 0\n"
"v_mov_b32 v145, 0\n"
"v_mov_b32 v146, 0\n"
"v_mov_b32 v147, 0\n"
"v_mov_b32 v148, 0\n"
"v_mov_b32 v149, 0\n"
"v_mov_b32 v150, 0\n"
"v_mov_b32 v151, 0\n"
"v_mov_b32 v152, 0\n"
"v_mov_b32 v153, 0\n"
"v_mov_b32 v154, 0\n"
"v_mov_b32 v155, 0\n"
"v_mov_b32 v156, 0\n"
"v_mov_b32 v157, 0\n"
"v_mov_b32 v158, 0\n"
"v_mov_b32 v159, 0\n"
"v_mov_b32 v160, 0\n"
"v_mov_b32 v161, 0\n"
"v_mov_b32 v162, 0\n"
"v_mov_b32 v163, 0\n"
"v_mov_b32 v164, 0\n"
"v_mov_b32 v165, 0\n"
"v_mov_b32 v166, 0\n"
"v_mov_b32 v167, 0\n"
"v_mov_b32 v168, 0\n"
"v_mov_b32 v169, 0\n"
"v_mov_b32 v170, 0\n"
"v_mov_b32 v171, 0\n"
"v_mov_b32 v172, 0\n"
"v_mov_b32 v173, 0\n"
"v_mov_b32 v174, 0\n"
"v_mov_b32 v175, 0\n"
"v_mov_b32 v176, 0\n"
"v_mov_b32 v177, 0\n"
"v_mov_b32 v178, 0\n"
"v_mov_b32 v179, 0\n"
"v_mov_b32 v180, 0\n"
"v_mov_b32 v181, 0\n"
"v_mov_b32 v182, 0\n"
"v_mov_b32 v183, 0\n"
"v_mov_b32 v184, 0\n"
"v_mov_b32 v185, 0\n"
"v_mov_b32 v186, 0\n"
"v_mov_b32 v187, 0\n"
"v_mov_b32 v188, 0\n"
"v_mov_b32 v189, 0\n"
"v_mov_b32 v190, 0\n"
"v_mov_b32 v191, 0\n"
"v_mov_b32 v192, 0\n"
"v_mov_b32 v193, 0\n"
"v_mov_b32 v194, 0\n"
"v_mov_b32 v195, 0\n"
"v_mov_b32 v196, 0\n"
"v_mov_b32 v197, 0\n"
"v_mov_b32 v198, 0\n"
"v_mov_b32 v199, 0\n"
"v_mov_b32 v200, 0\n"
"v_mov_b32 v201, 0\n"
"v_mov_b32 v202, 0\n"
"v_mov_b32 v203, 0\n"
"v_mov_b32 v204, 0\n"
"v_mov_b32 v205, 0\n"
"v_mov_b32 v206, 0\n"
"v_mov_b32 v207, 0\n"
"v_mov_b32 v208, 0\n"
"v_mov_b32 v209, 0\n"
"v_mov_b32 v210, 0\n"
"v_mov_b32 v211, 0\n"
"v_mov_b32 v212, 0\n"
"v_mov_b32 v213, 0\n"
"v_mov_b32 v214, 0\n"
"v_mov_b32 v215, 0\n"
"v_mov_b32 v216, 0\n"
"v_mov_b32 v217, 0\n"
"v_mov_b32 v218, 0\n"
"v_mov_b32 v219, 0\n"
"v_mov_b32 v220, 0\n"
"v_mov_b32 v221, 0\n"
"v_mov_b32 v222, 0\n"
"v_mov_b32 v223, 0\n"
"v_mov_b32 v224, 0\n"
"v_mov_b32 v225, 0\n"
"v_mov_b32 v226, 0\n"
"v_mov_b32 v227, 0\n"
"v_mov_b32 v228, 0\n"
"v_mov_b32 v229, 0\n"
"v_mov_b32 v230, 0\n"
"v_mov_b32 v231, 0\n"
"v_mov_b32 v232, 0\n"
"v_mov_b32 v233, 0\n"
"v_mov_b32 v234, 0\n"
"v_mov_b32 v235, 0\n"
"v_mov_b32 v236, 0\n"
"v_mov_b32 v237, 0\n"
"v_mov_b32 v238, 0\n"
"v_mov_b32 v239, 0\n"
"v_mov_b32 v240, 0\n"
"v_mov_b32 v241, 0\n"
"v_mov_b32 v242, 0\n"
"v_mov_b32 v243, 0\n"
"v_mov_b32 v244, 0\n"
"v_mov_b32 v245, 0\n"
"v_mov_b32 v246, 0\n"
"v_mov_b32 v247, 0\n"
"v_mov_b32 v248, 0\n"
"v_mov_b32 v249, 0\n"
"v_mov_b32 v250, 0\n"
"v_mov_b32 v251, 0\n"
"v_mov_b32 v252, 0\n"
"v_mov_b32 v253, 0\n"
"v_mov_b32 v254, 0\n"
"v_mov_b32 v255, 0\n"
"v_accvgpr_write_b32 a160, 0\n"
"v_accvgpr_write_b32 a161, 0\n"
"v_accvgpr_write_b32 a162, 0\n"
"v_accvgpr_write_b32 a163, 0\n"
"v_accvgpr_write_b32 a164, 0\n"
"v_accvgpr_write_b32 a165, 0\n"
"v_accvgpr_write_b32 a166, 0\n"
"v_accvgpr_write_b32 a167, 0\n"
"v_accvgpr_write_b32 a168, 0\n"
"v_accvgpr_write_b32 a169, 0\n"
"v_accvgpr_write_b32 a170, 0\n"
"v_accvgpr_write_b32 a171, 0\n"
"v_accvgpr_write_b32 a172, 0\n"
"v_accvgpr_write_b32 a173, 0\n"
"v_accvgpr_write_b32 a174, 0\n"
"v_accvgpr_write_b32 a175, 0\n"
"v_accvgpr_write_b32 a176, 0\n"
"v_accvgpr_write_b32 a177, 0\n"
"v_accvgpr_write_b32 a178, 0\n"
"v_accvgpr_write_b32 a179, 0\n"
"v_accvgpr_write_b32 a180, 0\n"
"v_accvgpr_write_b32 a181, 0\n"
"v_accvgpr_write_b32 a182, 0\n"
"v_accvgpr_write_b32 a183, 0\n"
"v_accvgpr_write_b32 a184, 0\n"
"v_accvgpr_write_b32 a185, 0\n"
"v_accvgpr_write_b32 a186, 0\n"
"v_accvgpr_write_b32 a187, 0\n"
"v_accvgpr_write_b32 a188, 0\n"
"v_accvgpr_write_b32 a189, 0\n"
"v_accvgpr_write_b32 a190, 0\n"
"v_accvgpr_write_b32 a191, 0\n"
"s_mov_b32 s38, 0\n"
"s_mov_b32 s39, 32768\n"
"s_mov_b32 s40, 65536\n"
"s_mov_b32 s46, 0\n"
"s_waitcnt vmcnt(33)\n"
"s_barrier\n"
"ds_read_b64_tr_b16 a[0:1], v25 offset:0\n"
"ds_read_b64_tr_b16 a[2:3], v25 offset:512\n"
"ds_read_b64_tr_b16 a[4:5], v25 offset:4096\n"
"ds_read_b64_tr_b16 a[6:7], v25 offset:4608\n"
"ds_read_b64_tr_b16 a[8:9], v26 offset:0\n"
"ds_read_b64_tr_b16 a[10:11], v26 offset:512\n"
"ds_read_b64_tr_b16 a[12:13], v26 offset:4096\n"
"ds_read_b64_tr_b16 a[14:15], v26 offset:4608\n"
"ds_read_b64_tr_b16 a[16:17], v27 offset:0\n"
"ds_read_b64_tr_b16 a[18:19], v27 offset:512\n"
"ds_read_b64_tr_b16 a[20:21], v27 offset:4096\n"
"ds_read_b64_tr_b16 a[22:23], v27 offset:4608\n"
"ds_read_b64_tr_b16 a[24:25], v28 offset:0\n"
"ds_read_b64_tr_b16 a[26:27], v28 offset:512\n"
"ds_read_b64_tr_b16 a[28:29], v28 offset:4096\n"
"ds_read_b64_tr_b16 a[30:31], v28 offset:4608\n"
"ds_read_b64_tr_b16 a[32:33], v25 offset:8192\n"
"ds_read_b64_tr_b16 a[34:35], v25 offset:8704\n"
"ds_read_b64_tr_b16 a[36:37], v25 offset:12288\n"
"ds_read_b64_tr_b16 a[38:39], v25 offset:12800\n"
"ds_read_b64_tr_b16 a[40:41], v26 offset:8192\n"
"ds_read_b64_tr_b16 a[42:43], v26 offset:8704\n"
"ds_read_b64_tr_b16 a[44:45], v26 offset:12288\n"
"ds_read_b64_tr_b16 a[46:47], v26 offset:12800\n"
"ds_read_b64_tr_b16 a[48:49], v27 offset:8192\n"
"ds_read_b64_tr_b16 a[50:51], v27 offset:8704\n"
"ds_read_b64_tr_b16 a[52:53], v27 offset:12288\n"
"ds_read_b64_tr_b16 a[54:55], v27 offset:12800\n"
"ds_read_b64_tr_b16 a[56:57], v28 offset:8192\n"
"ds_read_b64_tr_b16 a[58:59], v28 offset:8704\n"
"ds_read_b64_tr_b16 a[60:61], v28 offset:12288\n"
"ds_read_b64_tr_b16 a[62:63], v28 offset:12800\n"
"v_add_u32 v25, 65536, v21\n"
"v_add_u32 v26, 65536, v22\n"
"v_add_u32 v27, 65536, v23\n"
"v_add_u32 v28, 65536, v24\n"
"s_waitcnt vmcnt(17) lgkmcnt(0)\n"
"s_barrier\n"
"ds_read_b128 a[128:131], v19 offset:0\n"
"ds_read_b128 a[132:135], v20 offset:0\n"
"s_bitcmp1_b32 s49, 1\n"
"s_cbranch_scc1 LV%=\n"
"LK%=:\n"
"s_waitcnt lgkmcnt(1)\n"
"v_mfma_f32_16x16x32_bf16 v[32:35], a[0:3], a[128:131], v[32:35]\n"
"ds_read_b128 a[136:139], v19 offset:2048\n"
"s_waitcnt lgkmcnt(1)\n"
"v_mfma_f32_16x16x32_bf16 v[32:35], a[4:7], a[132:135], v[32:35]\n"
"ds_read_b128 a[140:143], v20 offset:2048\n"
"v_mfma_f32_16x16x32_bf16 v[36:39], a[8:11], a[128:131], v[36:39]\n"
"s_cmp_lt_u32 s46, 14\n"
"s_cselect_b32 s22, 0x80000, s34\n"
"v_mfma_f32_16x16x32_bf16 v[36:39], a[12:15], a[132:135], v[36:39]\n"
"ds_read_b64_tr_b16 a[64:65], v25 offset:0\n"
"v_mfma_f32_16x16x32_bf16 v[40:43], a[16:19], a[128:131], v[40:43]\n"
"buffer_load_dwordx4 v13, s[24:27], s37 offen offset:0 lds\n"
"v_mfma_f32_16x16x32_bf16 v[40:43], a[20:23], a[132:135], v[40:43]\n"
"ds_read_b64_tr_b16 a[66:67], v25 offset:512\n"
"v_mfma_f32_16x16x32_bf16 v[44:47], a[24:27], a[128:131], v[44:47]\n"
"v_mfma_f32_16x16x32_bf16 v[44:47], a[28:31], a[132:135], v[44:47]\n"
"ds_read_b64_tr_b16 a[68:69], v25 offset:4096\n"
"v_mfma_f32_16x16x32_bf16 v[144:147], a[32:35], a[128:131], v[144:147]\n"
"buffer_load_dwordx4 v14, s[24:27], s37 offen offset:1024 lds\n"
"v_mfma_f32_16x16x32_bf16 v[144:147], a[36:39], a[132:135], v[144:147]\n"
"ds_read_b64_tr_b16 a[70:71], v25 offset:4608\n"
"v_mfma_f32_16x16x32_bf16 v[148:151], a[40:43], a[128:131], v[148:151]\n"
"v_mfma_f32_16x16x32_bf16 v[148:151], a[44:47], a[132:135], v[148:151]\n"
"ds_read_b64_tr_b16 a[72:73], v26 offset:0\n"
"v_mfma_f32_16x16x32_bf16 v[152:155], a[48:51], a[128:131], v[152:155]\n"
"buffer_load_dwordx4 v15, s[24:27], s37 offen offset:2048 lds\n"
"v_mfma_f32_16x16x32_bf16 v[152:155], a[52:55], a[132:135], v[152:155]\n"
"v_mfma_f32_16x16x32_bf16 v[156:159], a[56:59], a[128:131], v[156:159]\n"
"buffer_load_dwordx4 v16, s[24:27], s37 offen offset:3072 lds\n"
"v_mfma_f32_16x16x32_bf16 v[156:159], a[60:63], a[132:135], v[156:159]\n"
"s_add_u32 m0, s38, s45\n"
"s_add_u32 s37, s37, s61\n"
"s_waitcnt lgkmcnt(6)\n"
"v_mfma_f32_16x16x32_bf16 v[160:163], a[0:3], a[136:139], v[160:163]\n"
"ds_read_b128 a[128:131], v19 offset:4096\n"
"s_waitcnt lgkmcnt(6)\n"
"v_mfma_f32_16x16x32_bf16 v[160:163], a[4:7], a[140:143], v[160:163]\n"
"ds_read_b128 a[132:135], v20 offset:4096\n"
"v_mfma_f32_16x16x32_bf16 v[164:167], a[8:11], a[136:139], v[164:167]\n"
"v_mfma_f32_16x16x32_bf16 v[164:167], a[12:15], a[140:143], v[164:167]\n"
"ds_read_b64_tr_b16 a[74:75], v26 offset:512\n"
"v_mfma_f32_16x16x32_bf16 v[168:171], a[16:19], a[136:139], v[168:171]\n"
"v_mfma_f32_16x16x32_bf16 v[168:171], a[20:23], a[140:143], v[168:171]\n"
"ds_read_b64_tr_b16 a[76:77], v26 offset:4096\n"
"v_mfma_f32_16x16x32_bf16 v[172:175], a[24:27], a[136:139], v[172:175]\n"
"buffer_load_dwordx4 v1, s[20:23], s36 offen offset:0 lds\n"
"v_mfma_f32_16x16x32_bf16 v[172:175], a[28:31], a[140:143], v[172:175]\n"
"ds_read_b64_tr_b16 a[78:79], v26 offset:4608\n"
"v_mfma_f32_16x16x32_bf16 v[176:179], a[32:35], a[136:139], v[176:179]\n"
"v_mfma_f32_16x16x32_bf16 v[176:179], a[36:39], a[140:143], v[176:179]\n"
"ds_read_b64_tr_b16 a[80:81], v27 offset:0\n"
"v_mfma_f32_16x16x32_bf16 v[180:183], a[40:43], a[136:139], v[180:183]\n"
"v_mfma_f32_16x16x32_bf16 v[180:183], a[44:47], a[140:143], v[180:183]\n"
"ds_read_b64_tr_b16 a[82:83], v27 offset:512\n"
"v_mfma_f32_16x16x32_bf16 v[184:187], a[48:51], a[136:139], v[184:187]\n"
"v_mfma_f32_16x16x32_bf16 v[184:187], a[52:55], a[140:143], v[184:187]\n"
"v_mfma_f32_16x16x32_bf16 v[188:191], a[56:59], a[136:139], v[188:191]\n"
"buffer_load_dwordx4 v2, s[20:23], s36 offen offset:1024 lds\n"
"v_mfma_f32_16x16x32_bf16 v[188:191], a[60:63], a[140:143], v[188:191]\n"
"s_waitcnt lgkmcnt(6)\n"
"v_mfma_f32_16x16x32_bf16 v[48:51], a[0:3], a[128:131], v[48:51]\n"
"ds_read_b128 a[136:139], v19 offset:6144\n"
"s_waitcnt lgkmcnt(6)\n"
"v_mfma_f32_16x16x32_bf16 v[48:51], a[4:7], a[132:135], v[48:51]\n"
"ds_read_b128 a[140:143], v20 offset:6144\n"
"v_mfma_f32_16x16x32_bf16 v[52:55], a[8:11], a[128:131], v[52:55]\n"
"v_mfma_f32_16x16x32_bf16 v[52:55], a[12:15], a[132:135], v[52:55]\n"
"ds_read_b64_tr_b16 a[84:85], v27 offset:4096\n"
"v_mfma_f32_16x16x32_bf16 v[56:59], a[16:19], a[128:131], v[56:59]\n"
"v_mfma_f32_16x16x32_bf16 v[56:59], a[20:23], a[132:135], v[56:59]\n"
"ds_read_b64_tr_b16 a[86:87], v27 offset:4608\n"
"v_mfma_f32_16x16x32_bf16 v[60:63], a[24:27], a[128:131], v[60:63]\n"
"v_mfma_f32_16x16x32_bf16 v[60:63], a[28:31], a[132:135], v[60:63]\n"
"ds_read_b64_tr_b16 a[88:89], v28 offset:0\n"
"v_mfma_f32_16x16x32_bf16 v[64:67], a[32:35], a[128:131], v[64:67]\n"
"v_mfma_f32_16x16x32_bf16 v[64:67], a[36:39], a[132:135], v[64:67]\n"
"ds_read_b64_tr_b16 a[90:91], v28 offset:512\n"
"v_mfma_f32_16x16x32_bf16 v[68:71], a[40:43], a[128:131], v[68:71]\n"
"v_mfma_f32_16x16x32_bf16 v[68:71], a[44:47], a[132:135], v[68:71]\n"
"ds_read_b64_tr_b16 a[92:93], v28 offset:4096\n"
"v_mfma_f32_16x16x32_bf16 v[72:75], a[48:51], a[128:131], v[72:75]\n"
"buffer_load_dwordx4 v3, s[20:23], s36 offen offset:2048 lds\n"
"v_mfma_f32_16x16x32_bf16 v[72:75], a[52:55], a[132:135], v[72:75]\n"
"v_mfma_f32_16x16x32_bf16 v[76:79], a[56:59], a[128:131], v[76:79]\n"
"v_mfma_f32_16x16x32_bf16 v[76:79], a[60:63], a[132:135], v[76:79]\n"
"s_waitcnt lgkmcnt(6)\n"
"v_mfma_f32_16x16x32_bf16 v[80:83], a[0:3], a[136:139], v[80:83]\n"
"ds_read_b128 a[128:131], v19 offset:8192\n"
"s_waitcnt lgkmcnt(6)\n"
"v_mfma_f32_16x16x32_bf16 v[80:83], a[4:7], a[140:143], v[80:83]\n"
"ds_read_b128 a[132:135], v20 offset:8192\n"
"v_mfma_f32_16x16x32_bf16 v[84:87], a[8:11], a[136:139], v[84:87]\n"
"v_mfma_f32_16x16x32_bf16 v[84:87], a[12:15], a[140:143], v[84:87]\n"
"ds_read_b64_tr_b16 a[94:95], v28 offset:4608\n"
"v_mfma_f32_16x16x32_bf16 v[88:91], a[16:19], a[136:139], v[88:91]\n"
"v_mfma_f32_16x16x32_bf16 v[88:91], a[20:23], a[140:143], v[88:91]\n"
"ds_read_b64_tr_b16 a[96:97], v25 offset:8192\n"
"v_mfma_f32_16x16x32_bf16 v[92:95], a[24:27], a[136:139], v[92:95]\n"
"v_mfma_f32_16x16x32_bf16 v[92:95], a[28:31], a[140:143], v[92:95]\n"
"ds_read_b64_tr_b16 a[98:99], v25 offset:8704\n"
"v_mfma_f32_16x16x32_bf16 v[96:99], a[32:35], a[136:139], v[96:99]\n"
"buffer_load_dwordx4 v4, s[20:23], s36 offen offset:3072 lds\n"
"v_mfma_f32_16x16x32_bf16 v[96:99], a[36:39], a[140:143], v[96:99]\n"
"ds_read_b64_tr_b16 a[100:101], v25 offset:12288\n"
"v_mfma_f32_16x16x32_bf16 v[100:103], a[40:43], a[136:139], v[100:103]\n"
"s_add_u32 m0, m0, 0x1000\n"
"v_mfma_f32_16x16x32_bf16 v[100:103], a[44:47], a[140:143], v[100:103]\n"
"ds_read_b64_tr_b16 a[102:103], v25 offset:12800\n"
"v_mfma_f32_16x16x32_bf16 v[104:107], a[48:51], a[136:139], v[104:107]\n"
"v_mfma_f32_16x16x32_bf16 v[104:107], a[52:55], a[140:143], v[104:107]\n"
"v_mfma_f32_16x16x32_bf16 v[108:111], a[56:59], a[136:139], v[108:111]\n"
"v_mfma_f32_16x16x32_bf16 v[108:111], a[60:63], a[140:143], v[108:111]\n"
"s_waitcnt lgkmcnt(6)\n"
"v_mfma_f32_16x16x32_bf16 v[192:195], a[0:3], a[128:131], v[192:195]\n"
"ds_read_b128 a[136:139], v19 offset:10240\n"
"s_waitcnt lgkmcnt(6)\n"
"v_mfma_f32_16x16x32_bf16 v[192:195], a[4:7], a[132:135], v[192:195]\n"
"ds_read_b128 a[140:143], v20 offset:10240\n"
"v_mfma_f32_16x16x32_bf16 v[196:199], a[8:11], a[128:131], v[196:199]\n"
"v_mfma_f32_16x16x32_bf16 v[196:199], a[12:15], a[132:135], v[196:199]\n"
"ds_read_b64_tr_b16 a[104:105], v26 offset:8192\n"
"v_mfma_f32_16x16x32_bf16 v[200:203], a[16:19], a[128:131], v[200:203]\n"
"buffer_load_dwordx4 v5, s[20:23], s36 offen offset:0 lds\n"
"v_mfma_f32_16x16x32_bf16 v[200:203], a[20:23], a[132:135], v[200:203]\n"
"ds_read_b64_tr_b16 a[106:107], v26 offset:8704\n"
"v_mfma_f32_16x16x32_bf16 v[204:207], a[24:27], a[128:131], v[204:207]\n"
"v_mfma_f32_16x16x32_bf16 v[204:207], a[28:31], a[132:135], v[204:207]\n"
"ds_read_b64_tr_b16 a[108:109], v26 offset:12288\n"
"v_mfma_f32_16x16x32_bf16 v[208:211], a[32:35], a[128:131], v[208:211]\n"
"v_mfma_f32_16x16x32_bf16 v[208:211], a[36:39], a[132:135], v[208:211]\n"
"ds_read_b64_tr_b16 a[110:111], v26 offset:12800\n"
"v_mfma_f32_16x16x32_bf16 v[212:215], a[40:43], a[128:131], v[212:215]\n"
"v_mfma_f32_16x16x32_bf16 v[212:215], a[44:47], a[132:135], v[212:215]\n"
"ds_read_b64_tr_b16 a[112:113], v27 offset:8192\n"
"v_mfma_f32_16x16x32_bf16 v[216:219], a[48:51], a[128:131], v[216:219]\n"
"v_mfma_f32_16x16x32_bf16 v[216:219], a[52:55], a[132:135], v[216:219]\n"
"v_mfma_f32_16x16x32_bf16 v[220:223], a[56:59], a[128:131], v[220:223]\n"
"buffer_load_dwordx4 v6, s[20:23], s36 offen offset:1024 lds\n"
"v_mfma_f32_16x16x32_bf16 v[220:223], a[60:63], a[132:135], v[220:223]\n"
"s_waitcnt lgkmcnt(6)\n"
"v_mfma_f32_16x16x32_bf16 v[224:227], a[0:3], a[136:139], v[224:227]\n"
"ds_read_b128 a[128:131], v19 offset:12288\n"
"s_waitcnt lgkmcnt(6)\n"
"v_mfma_f32_16x16x32_bf16 v[224:227], a[4:7], a[140:143], v[224:227]\n"
"ds_read_b128 a[132:135], v20 offset:12288\n"
"v_mfma_f32_16x16x32_bf16 v[228:231], a[8:11], a[136:139], v[228:231]\n"
"v_mfma_f32_16x16x32_bf16 v[228:231], a[12:15], a[140:143], v[228:231]\n"
"ds_read_b64_tr_b16 a[114:115], v27 offset:8704\n"
"v_mfma_f32_16x16x32_bf16 v[232:235], a[16:19], a[136:139], v[232:235]\n"
"v_mfma_f32_16x16x32_bf16 v[232:235], a[20:23], a[140:143], v[232:235]\n"
"ds_read_b64_tr_b16 a[116:117], v27 offset:12288\n"
"v_mfma_f32_16x16x32_bf16 v[236:239], a[24:27], a[136:139], v[236:239]\n"
"v_mfma_f32_16x16x32_bf16 v[236:239], a[28:31], a[140:143], v[236:239]\n"
"ds_read_b64_tr_b16 a[118:119], v27 offset:12800\n"
"v_mfma_f32_16x16x32_bf16 v[240:243], a[32:35], a[136:139], v[240:243]\n"
"v_mfma_f32_16x16x32_bf16 v[240:243], a[36:39], a[140:143], v[240:243]\n"
"ds_read_b64_tr_b16 a[120:121], v28 offset:8192\n"
"v_mfma_f32_16x16x32_bf16 v[244:247], a[40:43], a[136:139], v[244:247]\n"
"v_mfma_f32_16x16x32_bf16 v[244:247], a[44:47], a[140:143], v[244:247]\n"
"ds_read_b64_tr_b16 a[122:123], v28 offset:8704\n"
"v_mfma_f32_16x16x32_bf16 v[248:251], a[48:51], a[136:139], v[248:251]\n"
"buffer_load_dwordx4 v7, s[20:23], s36 offen offset:2048 lds\n"
"v_mfma_f32_16x16x32_bf16 v[248:251], a[52:55], a[140:143], v[248:251]\n"
"v_mfma_f32_16x16x32_bf16 v[252:255], a[56:59], a[136:139], v[252:255]\n"
"v_mfma_f32_16x16x32_bf16 v[252:255], a[60:63], a[140:143], v[252:255]\n"
"s_waitcnt lgkmcnt(6)\n"
"v_mfma_f32_16x16x32_bf16 v[112:115], a[0:3], a[128:131], v[112:115]\n"
"ds_read_b128 a[136:139], v19 offset:14336\n"
"s_waitcnt lgkmcnt(6)\n"
"v_mfma_f32_16x16x32_bf16 v[112:115], a[4:7], a[132:135], v[112:115]\n"
"ds_read_b128 a[140:143], v20 offset:14336\n"
"v_mfma_f32_16x16x32_bf16 v[116:119], a[8:11], a[128:131], v[116:119]\n"
"s_add_u32 s41, s40, 32768\n"
"s_cmp_eq_u32 s41, 163840\n"
"v_mfma_f32_16x16x32_bf16 v[116:119], a[12:15], a[132:135], v[116:119]\n"
"ds_read_b64_tr_b16 a[124:125], v28 offset:12288\n"
"v_mfma_f32_16x16x32_bf16 v[120:123], a[16:19], a[128:131], v[120:123]\n"
"s_cselect_b32 s41, 0, s41\n"
"v_add_u32 v19, s41, v17\n"
"v_mfma_f32_16x16x32_bf16 v[120:123], a[20:23], a[132:135], v[120:123]\n"
"ds_read_b64_tr_b16 a[126:127], v28 offset:12800\n"
"v_mfma_f32_16x16x32_bf16 v[124:127], a[24:27], a[128:131], v[124:127]\n"
"v_add_u32 v20, s41, v18\n"
"s_add_u32 s42, s41, 32768\n"
"v_mfma_f32_16x16x32_bf16 v[124:127], a[28:31], a[132:135], v[124:127]\n"
"s_cmp_eq_u32 s42, 163840\n"
"s_cselect_b32 s42, 0, s42\n"
"v_mfma_f32_16x16x32_bf16 v[128:131], a[32:35], a[128:131], v[128:131]\n"
"buffer_load_dwordx4 v8, s[20:23], s36 offen offset:3072 lds\n"
"v_mfma_f32_16x16x32_bf16 v[128:131], a[36:39], a[132:135], v[128:131]\n"
"s_add_u32 s36, s36, s60\n"
"v_mfma_f32_16x16x32_bf16 v[132:135], a[40:43], a[128:131], v[132:135]\n"
"v_add_u32 v25, s42, v21\n"
"v_add_u32 v26, s42, v22\n"
"v_mfma_f32_16x16x32_bf16 v[132:135], a[44:47], a[132:135], v[132:135]\n"
"v_add_u32 v27, s42, v23\n"
"v_add_u32 v28, s42, v24\n"
"v_mfma_f32_16x16x32_bf16 v[136:139], a[48:51], a[128:131], v[136:139]\n"
"s_cmp_lt_u32 s46, 13\n"
"s_cselect_b32 s26, 0x800000, s62\n"
"v_mfma_f32_16x16x32_bf16 v[136:139], a[52:55], a[132:135], v[136:139]\n"
"s_add_u32 m0, s39, s45\n"
"s_mov_b32 s38, s40\n"
"v_mfma_f32_16x16x32_bf16 v[140:143], a[56:59], a[128:131], v[140:143]\n"
"s_mov_b32 s39, s41\n"
"s_mov_b32 s40, s42\n"
"v_mfma_f32_16x16x32_bf16 v[140:143], a[60:63], a[132:135], v[140:143]\n"
"s_waitcnt vmcnt(8) lgkmcnt(0)\n"
"s_barrier\n"
"v_mfma_f32_16x16x32_bf16 a[160:163], a[0:3], a[136:139], a[160:163]\n"
"ds_read_b128 a[128:131], v19 offset:0\n"
"v_mfma_f32_16x16x32_bf16 a[160:163], a[4:7], a[140:143], a[160:163]\n"
"ds_read_b128 a[132:135], v20 offset:0\n"
"v_mfma_f32_16x16x32_bf16 a[164:167], a[8:11], a[136:139], a[164:167]\n"
"buffer_load_dwordx4 v9, s[24:27], s37 offen offset:0 lds\n"
"v_mfma_f32_16x16x32_bf16 a[164:167], a[12:15], a[140:143], a[164:167]\n"
"v_mfma_f32_16x16x32_bf16 a[168:171], a[16:19], a[136:139], a[168:171]\n"
"v_mfma_f32_16x16x32_bf16 a[168:171], a[20:23], a[140:143], a[168:171]\n"
"v_mfma_f32_16x16x32_bf16 a[172:175], a[24:27], a[136:139], a[172:175]\n"
"buffer_load_dwordx4 v10, s[24:27], s37 offen offset:1024 lds\n"
"v_mfma_f32_16x16x32_bf16 a[172:175], a[28:31], a[140:143], a[172:175]\n"
"v_mfma_f32_16x16x32_bf16 a[176:179], a[32:35], a[136:139], a[176:179]\n"
"v_mfma_f32_16x16x32_bf16 a[176:179], a[36:39], a[140:143], a[176:179]\n"
"v_mfma_f32_16x16x32_bf16 a[180:183], a[40:43], a[136:139], a[180:183]\n"
"buffer_load_dwordx4 v11, s[24:27], s37 offen offset:2048 lds\n"
"v_mfma_f32_16x16x32_bf16 a[180:183], a[44:47], a[140:143], a[180:183]\n"
"v_mfma_f32_16x16x32_bf16 a[184:187], a[48:51], a[136:139], a[184:187]\n"
"v_mfma_f32_16x16x32_bf16 a[184:187], a[52:55], a[140:143], a[184:187]\n"
"v_mfma_f32_16x16x32_bf16 a[188:191], a[56:59], a[136:139], a[188:191]\n"
"buffer_load_dwordx4 v12, s[24:27], s37 offen offset:3072 lds\n"
"v_mfma_f32_16x16x32_bf16 a[188:191], a[60:63], a[140:143], a[188:191]\n"
"s_add_u32 m0, m0, 0x1000\n"
"s_add_u32 s46, s46, 1\n"
"s_waitcnt lgkmcnt(1)\n"
"v_mfma_f32_16x16x32_bf16 v[32:35], a[64:67], a[128:131], v[32:35]\n"
"ds_read_b128 a[136:139], v19 offset:2048\n"
"s_waitcnt lgkmcnt(1)\n"
"v_mfma_f32_16x16x32_bf16 v[32:35], a[68:71], a[132:135], v[32:35]\n"
"ds_read_b128 a[140:143], v20 offset:2048\n"
"v_mfma_f32_16x16x32_bf16 v[36:39], a[72:75], a[128:131], v[36:39]\n"
"s_cmp_lt_u32 s46, 14\n"
"s_cselect_b32 s22, 0x80000, s34\n"
"v_mfma_f32_16x16x32_bf16 v[36:39], a[76:79], a[132:135], v[36:39]\n"
"ds_read_b64_tr_b16 a[0:1], v25 offset:0\n"
"v_mfma_f32_16x16x32_bf16 v[40:43], a[80:83], a[128:131], v[40:43]\n"
"buffer_load_dwordx4 v13, s[24:27], s37 offen offset:0 lds\n"
"v_mfma_f32_16x16x32_bf16 v[40:43], a[84:87], a[132:135], v[40:43]\n"
"ds_read_b64_tr_b16 a[2:3], v25 offset:512\n"
"v_mfma_f32_16x16x32_bf16 v[44:47], a[88:91], a[128:131], v[44:47]\n"
"v_mfma_f32_16x16x32_bf16 v[44:47], a[92:95], a[132:135], v[44:47]\n"
"ds_read_b64_tr_b16 a[4:5], v25 offset:4096\n"
"v_mfma_f32_16x16x32_bf16 v[144:147], a[96:99], a[128:131], v[144:147]\n"
"buffer_load_dwordx4 v14, s[24:27], s37 offen offset:1024 lds\n"
"v_mfma_f32_16x16x32_bf16 v[144:147], a[100:103], a[132:135], v[144:147]\n"
"ds_read_b64_tr_b16 a[6:7], v25 offset:4608\n"
"v_mfma_f32_16x16x32_bf16 v[148:151], a[104:107], a[128:131], v[148:151]\n"
"v_mfma_f32_16x16x32_bf16 v[148:151], a[108:111], a[132:135], v[148:151]\n"
"ds_read_b64_tr_b16 a[8:9], v26 offset:0\n"
"v_mfma_f32_16x16x32_bf16 v[152:155], a[112:115], a[128:131], v[152:155]\n"
"buffer_load_dwordx4 v15, s[24:27], s37 offen offset:2048 lds\n"
"v_mfma_f32_16x16x32_bf16 v[152:155], a[116:119], a[132:135], v[152:155]\n"
"v_mfma_f32_16x16x32_bf16 v[156:159], a[120:123], a[128:131], v[156:159]\n"
"buffer_load_dwordx4 v16, s[24:27], s37 offen offset:3072 lds\n"
"v_mfma_f32_16x16x32_bf16 v[156:159], a[124:127], a[132:135], v[156:159]\n"
"s_add_u32 m0, s38, s45\n"
"s_add_u32 s37, s37, s61\n"
"s_waitcnt lgkmcnt(6)\n"
"v_mfma_f32_16x16x32_bf16 v[160:163], a[64:67], a[136:139], v[160:163]\n"
"ds_read_b128 a[128:131], v19 offset:4096\n"
"s_waitcnt lgkmcnt(6)\n"
"v_mfma_f32_16x16x32_bf16 v[160:163], a[68:71], a[140:143], v[160:163]\n"
"ds_read_b128 a[132:135], v20 offset:4096\n"
"v_mfma_f32_16x16x32_bf16 v[164:167], a[72:75], a[136:139], v[164:167]\n"
"v_mfma_f32_16x16x32_bf16 v[164:167], a[76:79], a[140:143], v[164:167]\n"
"ds_read_b64_tr_b16 a[10:11], v26 offset:512\n"
"v_mfma_f32_16x16x32_bf16 v[168:171], a[80:83], a[136:139], v[168:171]\n"
"v_mfma_f32_16x16x32_bf16 v[168:171], a[84:87], a[140:143], v[168:171]\n"
"ds_read_b64_tr_b16 a[12:13], v26 offset:4096\n"
"v_mfma_f32_16x16x32_bf16 v[172:175], a[88:91], a[136:139], v[172:175]\n"
"buffer_load_dwordx4 v1, s[20:23], s36 offen offset:0 lds\n"
"v_mfma_f32_16x16x32_bf16 v[172:175], a[92:95], a[140:143], v[172:175]\n"
"ds_read_b64_tr_b16 a[14:15], v26 offset:4608\n"
"v_mfma_f32_16x16x32_bf16 v[176:179], a[96:99], a[136:139], v[176:179]\n"
"v_mfma_f32_16x16x32_bf16 v[176:179], a[100:103], a[140:143], v[176:179]\n"
"ds_read_b64_tr_b16 a[16:17], v27 offset:0\n"
"v_mfma_f32_16x16x32_bf16 v[180:183], a[104:107], a[136:139], v[180:183]\n"
"v_mfma_f32_16x16x32_bf16 v[180:183], a[108:111], a[140:143], v[180:183]\n"
"ds_read_b64_tr_b16 a[18:19], v27 offset:512\n"
"v_mfma_f32_16x16x32_bf16 v[184:187], a[112:115], a[136:139], v[184:187]\n"
"v_mfma_f32_16x16x32_bf16 v[184:187], a[116:119], a[140:143], v[184:187]\n"
"v_mfma_f32_16x16x32_bf16 v[188:191], a[120:123], a[136:139], v[188:191]\n"
"buffer_load_dwordx4 v2, s[20:23], s36 offen offset:1024 lds\n"
"v_mfma_f32_16x16x32_bf16 v[188:191], a[124:127], a[140:143], v[188:191]\n"
"s_waitcnt lgkmcnt(6)\n"
"v_mfma_f32_16x16x32_bf16 v[48:51], a[64:67], a[128:131], v[48:51]\n"
"ds_read_b128 a[136:139], v19 offset:6144\n"
"s_waitcnt lgkmcnt(6)\n"
"v_mfma_f32_16x16x32_bf16 v[48:51], a[68:71], a[132:135], v[48:51]\n"
"ds_read_b128 a[140:143], v20 offset:6144\n"
"v_mfma_f32_16x16x32_bf16 v[52:55], a[72:75], a[128:131], v[52:55]\n"
"v_mfma_f32_16x16x32_bf16 v[52:55], a[76:79], a[132:135], v[52:55]\n"
"ds_read_b64_tr_b16 a[20:21], v27 offset:4096\n"
"v_mfma_f32_16x16x32_bf16 v[56:59], a[80:83], a[128:131], v[56:59]\n"
"v_mfma_f32_16x16x32_bf16 v[56:59], a[84:87], a[132:135], v[56:59]\n"
"ds_read_b64_tr_b16 a[22:23], v27 offset:4608\n"
"v_mfma_f32_16x16x32_bf16 v[60:63], a[88:91], a[128:131], v[60:63]\n"
"v_mfma_f32_16x16x32_bf16 v[60:63], a[92:95], a[132:135], v[60:63]\n"
"ds_read_b64_tr_b16 a[24:25], v28 offset:0\n"
"v_mfma_f32_16x16x32_bf16 v[64:67], a[96:99], a[128:131], v[64:67]\n"
"v_mfma_f32_16x16x32_bf16 v[64:67], a[100:103], a[132:135], v[64:67]\n"
"ds_read_b64_tr_b16 a[26:27], v28 offset:512\n"
"v_mfma_f32_16x16x32_bf16 v[68:71], a[104:107], a[128:131], v[68:71]\n"
"v_mfma_f32_16x16x32_bf16 v[68:71], a[108:111], a[132:135], v[68:71]\n"
"ds_read_b64_tr_b16 a[28:29], v28 offset:4096\n"
"v_mfma_f32_16x16x32_bf16 v[72:75], a[112:115], a[128:131], v[72:75]\n"
"buffer_load_dwordx4 v3, s[20:23], s36 offen offset:2048 lds\n"
"v_mfma_f32_16x16x32_bf16 v[72:75], a[116:119], a[132:135], v[72:75]\n"
"v_mfma_f32_16x16x32_bf16 v[76:79], a[120:123], a[128:131], v[76:79]\n"
"v_mfma_f32_16x16x32_bf16 v[76:79], a[124:127], a[132:135], v[76:79]\n"
"s_waitcnt lgkmcnt(6)\n"
"v_mfma_f32_16x16x32_bf16 v[80:83], a[64:67], a[136:139], v[80:83]\n"
"ds_read_b128 a[128:131], v19 offset:8192\n"
"s_waitcnt lgkmcnt(6)\n"
"v_mfma_f32_16x16x32_bf16 v[80:83], a[68:71], a[140:143], v[80:83]\n"
"ds_read_b128 a[132:135], v20 offset:8192\n"
"v_mfma_f32_16x16x32_bf16 v[84:87], a[72:75], a[136:139], v[84:87]\n"
"v_mfma_f32_16x16x32_bf16 v[84:87], a[76:79], a[140:143], v[84:87]\n"
"ds_read_b64_tr_b16 a[30:31], v28 offset:4608\n"
"v_mfma_f32_16x16x32_bf16 v[88:91], a[80:83], a[136:139], v[88:91]\n"
"v_mfma_f32_16x16x32_bf16 v[88:91], a[84:87], a[140:143], v[88:91]\n"
"ds_read_b64_tr_b16 a[32:33], v25 offset:8192\n"
"v_mfma_f32_16x16x32_bf16 v[92:95], a[88:91], a[136:139], v[92:95]\n"
"v_mfma_f32_16x16x32_bf16 v[92:95], a[92:95], a[140:143], v[92:95]\n"
"ds_read_b64_tr_b16 a[34:35], v25 offset:8704\n"
"v_mfma_f32_16x16x32_bf16 v[96:99], a[96:99], a[136:139], v[96:99]\n"
"buffer_load_dwordx4 v4, s[20:23], s36 offen offset:3072 lds\n"
"v_mfma_f32_16x16x32_bf16 v[96:99], a[100:103], a[140:143], v[96:99]\n"
"ds_read_b64_tr_b16 a[36:37], v25 offset:12288\n"
"v_mfma_f32_16x16x32_bf16 v[100:103], a[104:107], a[136:139], v[100:103]\n"
"s_add_u32 m0, m0, 0x1000\n"
"v_mfma_f32_16x16x32_bf16 v[100:103], a[108:111], a[140:143], v[100:103]\n"
"ds_read_b64_tr_b16 a[38:39], v25 offset:12800\n"
"v_mfma_f32_16x16x32_bf16 v[104:107], a[112:115], a[136:139], v[104:107]\n"
"v_mfma_f32_16x16x32_bf16 v[104:107], a[116:119], a[140:143], v[104:107]\n"
"v_mfma_f32_16x16x32_bf16 v[108:111], a[120:123], a[136:139], v[108:111]\n"
"v_mfma_f32_16x16x32_bf16 v[108:111], a[124:127], a[140:143], v[108:111]\n"
"s_waitcnt lgkmcnt(6)\n"
"v_mfma_f32_16x16x32_bf16 v[192:195], a[64:67], a[128:131], v[192:195]\n"
"ds_read_b128 a[136:139], v19 offset:10240\n"
"s_waitcnt lgkmcnt(6)\n"
"v_mfma_f32_16x16x32_bf16 v[192:195], a[68:71], a[132:135], v[192:195]\n"
"ds_read_b128 a[140:143], v20 offset:10240\n"
"v_mfma_f32_16x16x32_bf16 v[196:199], a[72:75], a[128:131], v[196:199]\n"
"v_mfma_f32_16x16x32_bf16 v[196:199], a[76:79], a[132:135], v[196:199]\n"
"ds_read_b64_tr_b16 a[40:41], v26 offset:8192\n"
"v_mfma_f32_16x16x32_bf16 v[200:203], a[80:83], a[128:131], v[200:203]\n"
"buffer_load_dwordx4 v5, s[20:23], s36 offen offset:0 lds\n"
"v_mfma_f32_16x16x32_bf16 v[200:203], a[84:87], a[132:135], v[200:203]\n"
"ds_read_b64_tr_b16 a[42:43], v26 offset:8704\n"
"v_mfma_f32_16x16x32_bf16 v[204:207], a[88:91], a[128:131], v[204:207]\n"
"v_mfma_f32_16x16x32_bf16 v[204:207], a[92:95], a[132:135], v[204:207]\n"
"ds_read_b64_tr_b16 a[44:45], v26 offset:12288\n"
"v_mfma_f32_16x16x32_bf16 v[208:211], a[96:99], a[128:131], v[208:211]\n"
"v_mfma_f32_16x16x32_bf16 v[208:211], a[100:103], a[132:135], v[208:211]\n"
"ds_read_b64_tr_b16 a[46:47], v26 offset:12800\n"
"v_mfma_f32_16x16x32_bf16 v[212:215], a[104:107], a[128:131], v[212:215]\n"
"v_mfma_f32_16x16x32_bf16 v[212:215], a[108:111], a[132:135], v[212:215]\n"
"ds_read_b64_tr_b16 a[48:49], v27 offset:8192\n"
"v_mfma_f32_16x16x32_bf16 v[216:219], a[112:115], a[128:131], v[216:219]\n"
"v_mfma_f32_16x16x32_bf16 v[216:219], a[116:119], a[132:135], v[216:219]\n"
"v_mfma_f32_16x16x32_bf16 v[220:223], a[120:123], a[128:131], v[220:223]\n"
"buffer_load_dwordx4 v6, s[20:23], s36 offen offset:1024 lds\n"
"v_mfma_f32_16x16x32_bf16 v[220:223], a[124:127], a[132:135], v[220:223]\n"
"s_waitcnt lgkmcnt(6)\n"
"v_mfma_f32_16x16x32_bf16 v[224:227], a[64:67], a[136:139], v[224:227]\n"
"ds_read_b128 a[128:131], v19 offset:12288\n"
"s_waitcnt lgkmcnt(6)\n"
"v_mfma_f32_16x16x32_bf16 v[224:227], a[68:71], a[140:143], v[224:227]\n"
"ds_read_b128 a[132:135], v20 offset:12288\n"
"v_mfma_f32_16x16x32_bf16 v[228:231], a[72:75], a[136:139], v[228:231]\n"
"v_mfma_f32_16x16x32_bf16 v[228:231], a[76:79], a[140:143], v[228:231]\n"
"ds_read_b64_tr_b16 a[50:51], v27 offset:8704\n"
"v_mfma_f32_16x16x32_bf16 v[232:235], a[80:83], a[136:139], v[232:235]\n"
"v_mfma_f32_16x16x32_bf16 v[232:235], a[84:87], a[140:143], v[232:235]\n"
"ds_read_b64_tr_b16 a[52:53], v27 offset:12288\n"
"v_mfma_f32_16x16x32_bf16 v[236:239], a[88:91], a[136:139], v[236:239]\n"
"v_mfma_f32_16x16x32_bf16 v[236:239], a[92:95], a[140:143], v[236:239]\n"
"ds_read_b64_tr_b16 a[54:55], v27 offset:12800\n"
"v_mfma_f32_16x16x32_bf16 v[240:243], a[96:99], a[136:139], v[240:243]\n"
"v_mfma_f32_16x16x32_bf16 v[240:243], a[100:103], a[140:143], v[240:243]\n"
"ds_read_b64_tr_b16 a[56:57], v28 offset:8192\n"
"v_mfma_f32_16x16x32_bf16 v[244:247], a[104:107], a[136:139], v[244:247]\n"
"v_mfma_f32_16x16x32_bf16 v[244:247], a[108:111], a[140:143], v[244:247]\n"
"ds_read_b64_tr_b16 a[58:59], v28 offset:8704\n"
"v_mfma_f32_16x16x32_bf16 v[248:251], a[112:115], a[136:139], v[248:251]\n"
"buffer_load_dwordx4 v7, s[20:23], s36 offen offset:2048 lds\n"
"v_mfma_f32_16x16x32_bf16 v[248:251], a[116:119], a[140:143], v[248:251]\n"
"v_mfma_f32_16x16x32_bf16 v[252:255], a[120:123], a[136:139], v[252:255]\n"
"v_mfma_f32_16x16x32_bf16 v[252:255], a[124:127], a[140:143], v[252:255]\n"
"s_waitcnt lgkmcnt(6)\n"
"v_mfma_f32_16x16x32_bf16 v[112:115], a[64:67], a[128:131], v[112:115]\n"
"ds_read_b128 a[136:139], v19 offset:14336\n"
"s_waitcnt lgkmcnt(6)\n"
"v_mfma_f32_16x16x32_bf16 v[112:115], a[68:71], a[132:135], v[112:115]\n"
"ds_read_b128 a[140:143], v20 offset:14336\n"
"v_mfma_f32_16x16x32_bf16 v[116:119], a[72:75], a[128:131], v[116:119]\n"
"s_add_u32 s41, s40, 32768\n"
"s_cmp_eq_u32 s41, 163840\n"
"v_mfma_f32_16x16x32_bf16 v[116:119], a[76:79], a[132:135], v[116:119]\n"
"ds_read_b64_tr_b16 a[60:61], v28 offset:12288\n"
"v_mfma_f32_16x16x32_bf16 v[120:123], a[80:83], a[128:131], v[120:123]\n"
"s_cselect_b32 s41, 0, s41\n"
"v_add_u32 v19, s41, v17\n"
"v_mfma_f32_16x16x32_bf16 v[120:123], a[84:87], a[132:135], v[120:123]\n"
"ds_read_b64_tr_b16 a[62:63], v28 offset:12800\n"
"v_mfma_f32_16x16x32_bf16 v[124:127], a[88:91], a[128:131], v[124:127]\n"
"v_add_u32 v20, s41, v18\n"
"s_add_u32 s42, s41, 32768\n"
"v_mfma_f32_16x16x32_bf16 v[124:127], a[92:95], a[132:135], v[124:127]\n"
"s_cmp_eq_u32 s42, 163840\n"
"s_cselect_b32 s42, 0, s42\n"
"v_mfma_f32_16x16x32_bf16 v[128:131], a[96:99], a[128:131], v[128:131]\n"
"buffer_load_dwordx4 v8, s[20:23], s36 offen offset:3072 lds\n"
"v_mfma_f32_16x16x32_bf16 v[128:131], a[100:103], a[132:135], v[128:131]\n"
"s_add_u32 s36, s36, s60\n"
"v_mfma_f32_16x16x32_bf16 v[132:135], a[104:107], a[128:131], v[132:135]\n"
"v_add_u32 v25, s42, v21\n"
"v_add_u32 v26, s42, v22\n"
"v_mfma_f32_16x16x32_bf16 v[132:135], a[108:111], a[132:135], v[132:135]\n"
"v_add_u32 v27, s42, v23\n"
"v_add_u32 v28, s42, v24\n"
"v_mfma_f32_16x16x32_bf16 v[136:139], a[112:115], a[128:131], v[136:139]\n"
"s_cmp_lt_u32 s46, 13\n"
"s_cselect_b32 s26, 0x800000, s62\n"
"v_mfma_f32_16x16x32_bf16 v[136:139], a[116:119], a[132:135], v[136:139]\n"
"s_add_u32 m0, s39, s45\n"
"s_mov_b32 s38, s40\n"
"v_mfma_f32_16x16x32_bf16 v[140:143], a[120:123], a[128:131], v[140:143]\n"
"s_mov_b32 s39, s41\n"
"s_mov_b32 s40, s42\n"
"v_mfma_f32_16x16x32_bf16 v[140:143], a[124:127], a[132:135], v[140:143]\n"
"s_waitcnt vmcnt(8) lgkmcnt(0)\n"
"s_barrier\n"
"v_mfma_f32_16x16x32_bf16 a[160:163], a[64:67], a[136:139], a[160:163]\n"
"ds_read_b128 a[128:131], v19 offset:0\n"
"v_mfma_f32_16x16x32_bf16 a[160:163], a[68:71], a[140:143], a[160:163]\n"
"ds_read_b128 a[132:135], v20 offset:0\n"
"v_mfma_f32_16x16x32_bf16 a[164:167], a[72:75], a[136:139], a[164:167]\n"
"buffer_load_dwordx4 v9, s[24:27], s37 offen offset:0 lds\n"
"v_mfma_f32_16x16x32_bf16 a[164:167], a[76:79], a[140:143], a[164:167]\n"
"v_mfma_f32_16x16x32_bf16 a[168:171], a[80:83], a[136:139], a[168:171]\n"
"v_mfma_f32_16x16x32_bf16 a[168:171], a[84:87], a[140:143], a[168:171]\n"
"v_mfma_f32_16x16x32_bf16 a[172:175], a[88:91], a[136:139], a[172:175]\n"
"buffer_load_dwordx4 v10, s[24:27], s37 offen offset:1024 lds\n"
"v_mfma_f32_16x16x32_bf16 a[172:175], a[92:95], a[140:143], a[172:175]\n"
"v_mfma_f32_16x16x32_bf16 a[176:179], a[96:99], a[136:139], a[176:179]\n"
"v_mfma_f32_16x16x32_bf16 a[176:179], a[100:103], a[140:143], a[176:179]\n"
"v_mfma_f32_16x16x32_bf16 a[180:183], a[104:107], a[136:139], a[180:183]\n"
"buffer_load_dwordx4 v11, s[24:27], s37 offen offset:2048 lds\n"
"v_mfma_f32_16x16x32_bf16 a[180:183], a[108:111], a[140:143], a[180:183]\n"
"v_mfma_f32_16x16x32_bf16 a[184:187], a[112:115], a[136:139], a[184:187]\n"
"v_mfma_f32_16x16x32_bf16 a[184:187], a[116:119], a[140:143], a[184:187]\n"
"v_mfma_f32_16x16x32_bf16 a[188:191], a[120:123], a[136:139], a[188:191]\n"
"buffer_load_dwordx4 v12, s[24:27], s37 offen offset:3072 lds\n"
"v_mfma_f32_16x16x32_bf16 a[188:191], a[124:127], a[140:143], a[188:191]\n"
"s_add_u32 m0, m0, 0x1000\n"
"s_add_u32 s46, s46, 1\n"
"s_cmp_lt_u32 s46, 14\n"
"s_cbranch_scc1 LK%=\n"
"s_cmp_eq_u32 s46, 16\n"
"s_cbranch_scc1 LMK%=\n"
"v_accvgpr_read_b32 v1, a220\n"
"v_accvgpr_read_b32 v2, a221\n"
"v_accvgpr_read_b32 v3, a222\n"
"v_accvgpr_read_b32 v4, a223\n"
"v_accvgpr_read_b32 v5, a224\n"
"v_accvgpr_read_b32 v6, a225\n"
"v_accvgpr_read_b32 v7, a226\n"
"v_accvgpr_read_b32 v8, a227\n"
"v_accvgpr_read_b32 v9, a220\n"
"v_accvgpr_read_b32 v10, a221\n"
"v_accvgpr_read_b32 v11, a222\n"
"v_accvgpr_read_b32 v12, a223\n"
"v_accvgpr_read_b32 v13, a224\n"
"v_accvgpr_read_b32 v14, a225\n"
"v_accvgpr_read_b32 v15, a226\n"
"v_accvgpr_read_b32 v16, a227\n"
"s_mov_b64 s[20:21], s[32:33]\n"
"s_mov_b64 s[24:25], s[32:33]\n"
"s_mov_b32 s62, s34\n"
"s_mov_b32 s36, 0\n"
"s_mov_b32 s37, 256\n"
"s_mov_b32 s60, 256\n"
"s_mov_b32 s61, 256\n"
"s_branch LK%=\n"
"LMK%=:\n"
"v_accvgpr_read_b32 v9, a144\n"
"v_accvgpr_read_b32 v10, a145\n"
"v_accvgpr_read_b32 v11, a146\n"
"v_accvgpr_read_b32 v12, a147\n"
"v_accvgpr_read_b32 v13, a148\n"
"v_accvgpr_read_b32 v14, a149\n"
"v_accvgpr_read_b32 v15, a150\n"
"v_accvgpr_read_b32 v20, a152\n"
"v_accvgpr_read_b32 v19, a151\n"
"v_accvgpr_read_b32 v17, a153\n"
"v_accvgpr_read_b32 v18, a154\n"
"v_xor_b32 v21, 64, v20\n"
"v_add_u32 v30, 98304, v20\n"
"v_add_u32 v31, 98304, v21\n"
"s_add_u32 m0, s85, 65536\n"
"s_nop 0\n"
"buffer_load_dwordx4 v9, s[80:83], s84 offen offset:0 lds\n"
"v_cvt_pk_bf16_f32 v32, v32, v33\n"
"v_cvt_pk_bf16_f32 v33, v34, v35\n"
"v_cvt_pk_bf16_f32 v34, v36, v37\n"
"v_cvt_pk_bf16_f32 v35, v38, v39\n"
"s_waitcnt vmcnt(63)\n"
"ds_read_b128 v[36:39], v30 offset:0\n"
"buffer_load_dwordx4 v10, s[80:83], s84 offen offset:1024 lds\n"
"v_cvt_pk_bf16_f32 v144, v144, v145\n"
"v_cvt_pk_bf16_f32 v145, v146, v147\n"
"v_cvt_pk_bf16_f32 v146, v148, v149\n"
"v_cvt_pk_bf16_f32 v147, v150, v151\n"
"ds_read_b128 v[148:151], v30 offset:2048\n"
"buffer_load_dwordx4 v11, s[80:83], s84 offen offset:2048 lds\n"
"v_cvt_pk_bf16_f32 v40, v40, v41\n"
"v_cvt_pk_bf16_f32 v41, v42, v43\n"
"v_cvt_pk_bf16_f32 v42, v44, v45\n"
"v_cvt_pk_bf16_f32 v43, v46, v47\n"
"ds_read_b128 v[44:47], v31 offset:0\n"
"buffer_load_dwordx4 v12, s[80:83], s84 offen offset:3072 lds\n"
"v_cvt_pk_bf16_f32 v152, v152, v153\n"
"v_cvt_pk_bf16_f32 v153, v154, v155\n"
"v_cvt_pk_bf16_f32 v154, v156, v157\n"
"v_cvt_pk_bf16_f32 v155, v158, v159\n"
"ds_read_b128 v[156:159], v31 offset:2048\n"
"s_add_u32 m0, s85, 69632\n"
"s_nop 0\n"
"buffer_load_dwordx4 v13, s[80:83], s84 offen offset:0 lds\n"
"v_cvt_pk_bf16_f32 v48, v48, v49\n"
"v_cvt_pk_bf16_f32 v49, v50, v51\n"
"v_cvt_pk_bf16_f32 v50, v52, v53\n"
"v_cvt_pk_bf16_f32 v51, v54, v55\n"
"ds_read_b128 v[52:55], v30 offset:4096\n"
"buffer_load_dwordx4 v14, s[80:83], s84 offen offset:1024 lds\n"
"v_cvt_pk_bf16_f32 v160, v160, v161\n"
"v_cvt_pk_bf16_f32 v161, v162, v163\n"
"v_cvt_pk_bf16_f32 v162, v164, v165\n"
"v_cvt_pk_bf16_f32 v163, v166, v167\n"
"ds_read_b128 v[164:167], v30 offset:6144\n"
"buffer_load_dwordx4 v15, s[80:83], s84 offen offset:2048 lds\n"
"s_add_u32 s84, s84, s56\n"
"v_cvt_pk_bf16_f32 v56, v56, v57\n"
"v_cvt_pk_bf16_f32 v57, v58, v59\n"
"v_cvt_pk_bf16_f32 v58, v60, v61\n"
"v_cvt_pk_bf16_f32 v59, v62, v63\n"
"ds_read_b128 v[60:63], v31 offset:4096\n"
"v_cvt_pk_bf16_f32 v168, v168, v169\n"
"v_cvt_pk_bf16_f32 v169, v170, v171\n"
"v_cvt_pk_bf16_f32 v170, v172, v173\n"
"v_cvt_pk_bf16_f32 v171, v174, v175\n"
"ds_read_b128 v[172:175], v31 offset:6144\n"
"v_cvt_pk_bf16_f32 v96, v96, v97\n"
"v_cvt_pk_bf16_f32 v97, v98, v99\n"
"v_cvt_pk_bf16_f32 v98, v100, v101\n"
"v_cvt_pk_bf16_f32 v99, v102, v103\n"
"ds_read_b128 v[100:103], v30 offset:32768\n"
"v_cvt_pk_bf16_f32 v208, v208, v209\n"
"v_cvt_pk_bf16_f32 v209, v210, v211\n"
"v_cvt_pk_bf16_f32 v210, v212, v213\n"
"v_cvt_pk_bf16_f32 v211, v214, v215\n"
"ds_read_b128 v[212:215], v30 offset:34816\n"
"v_cvt_pk_bf16_f32 v104, v104, v105\n"
"v_cvt_pk_bf16_f32 v105, v106, v107\n"
"v_cvt_pk_bf16_f32 v106, v108, v109\n"
"v_cvt_pk_bf16_f32 v107, v110, v111\n"
"ds_read_b128 v[108:111], v31 offset:32768\n"
"v_cvt_pk_bf16_f32 v216, v216, v217\n"
"v_cvt_pk_bf16_f32 v217, v218, v219\n"
"v_cvt_pk_bf16_f32 v218, v220, v221\n"
"v_cvt_pk_bf16_f32 v219, v222, v223\n"
"ds_read_b128 v[220:223], v31 offset:34816\n"
"v_cvt_pk_bf16_f32 v112, v112, v113\n"
"v_cvt_pk_bf16_f32 v113, v114, v115\n"
"v_cvt_pk_bf16_f32 v114, v116, v117\n"
"v_cvt_pk_bf16_f32 v115, v118, v119\n"
"ds_read_b128 v[116:119], v30 offset:36864\n"
"v_cvt_pk_bf16_f32 v224, v224, v225\n"
"v_cvt_pk_bf16_f32 v225, v226, v227\n"
"v_cvt_pk_bf16_f32 v226, v228, v229\n"
"v_cvt_pk_bf16_f32 v227, v230, v231\n"
"ds_read_b128 v[228:231], v30 offset:38912\n"
"v_cvt_pk_bf16_f32 v120, v120, v121\n"
"v_cvt_pk_bf16_f32 v121, v122, v123\n"
"v_cvt_pk_bf16_f32 v122, v124, v125\n"
"v_cvt_pk_bf16_f32 v123, v126, v127\n"
"ds_read_b128 v[124:127], v31 offset:36864\n"
"v_cvt_pk_bf16_f32 v232, v232, v233\n"
"v_cvt_pk_bf16_f32 v233, v234, v235\n"
"v_cvt_pk_bf16_f32 v234, v236, v237\n"
"v_cvt_pk_bf16_f32 v235, v238, v239\n"
"ds_read_b128 v[236:239], v31 offset:38912\n"
"v_cvt_pk_bf16_f32 v64, v64, v65\n"
"v_cvt_pk_bf16_f32 v65, v66, v67\n"
"v_cvt_pk_bf16_f32 v66, v68, v69\n"
"v_cvt_pk_bf16_f32 v67, v70, v71\n"
"s_waitcnt vmcnt(11)\n"
"ds_read_b128 v[68:71], v20 offset:0\n"
"v_cvt_pk_bf16_f32 v176, v176, v177\n"
"v_cvt_pk_bf16_f32 v177, v178, v179\n"
"v_cvt_pk_bf16_f32 v178, v180, v181\n"
"v_cvt_pk_bf16_f32 v179, v182, v183\n"
"ds_read_b128 v[180:183], v20 offset:2048\n"
"v_cvt_pk_bf16_f32 v72, v72, v73\n"
"v_cvt_pk_bf16_f32 v73, v74, v75\n"
"v_cvt_pk_bf16_f32 v74, v76, v77\n"
"v_cvt_pk_bf16_f32 v75, v78, v79\n"
"ds_read_b128 v[76:79], v21 offset:0\n"
"v_cvt_pk_bf16_f32 v184, v184, v185\n"
"v_cvt_pk_bf16_f32 v185, v186, v187\n"
"v_cvt_pk_bf16_f32 v186, v188, v189\n"
"v_cvt_pk_bf16_f32 v187, v190, v191\n"
"ds_read_b128 v[188:191], v21 offset:2048\n"
"v_cvt_pk_bf16_f32 v80, v80, v81\n"
"v_cvt_pk_bf16_f32 v81, v82, v83\n"
"v_cvt_pk_bf16_f32 v82, v84, v85\n"
"v_cvt_pk_bf16_f32 v83, v86, v87\n"
"ds_read_b128 v[84:87], v20 offset:4096\n"
"v_cvt_pk_bf16_f32 v192, v192, v193\n"
"v_cvt_pk_bf16_f32 v193, v194, v195\n"
"v_cvt_pk_bf16_f32 v194, v196, v197\n"
"v_cvt_pk_bf16_f32 v195, v198, v199\n"
"ds_read_b128 v[196:199], v20 offset:6144\n"
"v_cvt_pk_bf16_f32 v88, v88, v89\n"
"v_cvt_pk_bf16_f32 v89, v90, v91\n"
"v_cvt_pk_bf16_f32 v90, v92, v93\n"
"v_cvt_pk_bf16_f32 v91, v94, v95\n"
"ds_read_b128 v[92:95], v21 offset:4096\n"
"v_cvt_pk_bf16_f32 v200, v200, v201\n"
"v_cvt_pk_bf16_f32 v201, v202, v203\n"
"v_cvt_pk_bf16_f32 v202, v204, v205\n"
"v_cvt_pk_bf16_f32 v203, v206, v207\n"
"ds_read_b128 v[204:207], v21 offset:6144\n"
"v_cvt_pk_bf16_f32 v128, v128, v129\n"
"v_cvt_pk_bf16_f32 v129, v130, v131\n"
"v_cvt_pk_bf16_f32 v130, v132, v133\n"
"v_cvt_pk_bf16_f32 v131, v134, v135\n"
"v_accvgpr_read_b32 v1, a160\n"
"v_accvgpr_read_b32 v2, a161\n"
"v_accvgpr_read_b32 v3, a162\n"
"v_accvgpr_read_b32 v4, a163\n"
"v_accvgpr_read_b32 v5, a164\n"
"v_accvgpr_read_b32 v6, a165\n"
"v_accvgpr_read_b32 v7, a166\n"
"v_accvgpr_read_b32 v8, a167\n"
"v_cvt_pk_bf16_f32 v2, v1, v2\n"
"v_cvt_pk_bf16_f32 v3, v3, v4\n"
"v_cvt_pk_bf16_f32 v4, v5, v6\n"
"v_cvt_pk_bf16_f32 v5, v7, v8\n"
"s_mov_b32 s43, 57344\n"
"buffer_store_dwordx4 v[2:5], v19, s[76:79], s43 offen offset:0 sc0 sc1\n"
"s_nop 0\n"
"v_accvgpr_read_b32 v1, a168\n"
"v_accvgpr_read_b32 v2, a169\n"
"v_accvgpr_read_b32 v3, a170\n"
"v_accvgpr_read_b32 v4, a171\n"
"v_accvgpr_read_b32 v5, a172\n"
"v_accvgpr_read_b32 v6, a173\n"
"v_accvgpr_read_b32 v7, a174\n"
"v_accvgpr_read_b32 v8, a175\n"
"v_cvt_pk_bf16_f32 v2, v1, v2\n"
"v_cvt_pk_bf16_f32 v3, v3, v4\n"
"v_cvt_pk_bf16_f32 v4, v5, v6\n"
"v_cvt_pk_bf16_f32 v5, v7, v8\n"
"s_mov_b32 s43, 57344\n"
"buffer_store_dwordx4 v[2:5], v19, s[76:79], s43 offen offset:1024 sc0 sc1\n"
"s_nop 0\n"
"v_accvgpr_read_b32 v1, a176\n"
"v_accvgpr_read_b32 v2, a177\n"
"v_accvgpr_read_b32 v3, a178\n"
"v_accvgpr_read_b32 v4, a179\n"
"v_accvgpr_read_b32 v5, a180\n"
"v_accvgpr_read_b32 v6, a181\n"
"v_accvgpr_read_b32 v7, a182\n"
"v_accvgpr_read_b32 v8, a183\n"
"v_cvt_pk_bf16_f32 v2, v1, v2\n"
"v_cvt_pk_bf16_f32 v3, v3, v4\n"
"v_cvt_pk_bf16_f32 v4, v5, v6\n"
"v_cvt_pk_bf16_f32 v5, v7, v8\n"
"s_mov_b32 s43, 57344\n"
"buffer_store_dwordx4 v[2:5], v19, s[76:79], s43 offen offset:2048 sc0 sc1\n"
"s_nop 0\n"
"v_accvgpr_read_b32 v1, a184\n"
"v_accvgpr_read_b32 v2, a185\n"
"v_accvgpr_read_b32 v3, a186\n"
"v_accvgpr_read_b32 v4, a187\n"
"v_accvgpr_read_b32 v5, a188\n"
"v_accvgpr_read_b32 v6, a189\n"
"v_accvgpr_read_b32 v7, a190\n"
"v_accvgpr_read_b32 v8, a191\n"
"v_cvt_pk_bf16_f32 v2, v1, v2\n"
"v_cvt_pk_bf16_f32 v3, v3, v4\n"
"v_cvt_pk_bf16_f32 v4, v5, v6\n"
"v_cvt_pk_bf16_f32 v5, v7, v8\n"
"s_mov_b32 s43, 57344\n"
"buffer_store_dwordx4 v[2:5], v19, s[76:79], s43 offen offset:3072 sc0 sc1\n"
"s_nop 0\n"
"s_waitcnt vmcnt(11)\n"
"ds_read_b128 v[132:135], v20 offset:32768\n"
"v_cvt_pk_bf16_f32 v240, v240, v241\n"
"v_cvt_pk_bf16_f32 v241, v242, v243\n"
"v_cvt_pk_bf16_f32 v242, v244, v245\n"
"v_cvt_pk_bf16_f32 v243, v246, v247\n"
"ds_read_b128 v[244:247], v20 offset:34816\n"
"v_cvt_pk_bf16_f32 v136, v136, v137\n"
"v_cvt_pk_bf16_f32 v137, v138, v139\n"
"v_cvt_pk_bf16_f32 v138, v140, v141\n"
"v_cvt_pk_bf16_f32 v139, v142, v143\n"
"ds_read_b128 v[140:143], v21 offset:32768\n"
"v_cvt_pk_bf16_f32 v248, v248, v249\n"
"v_cvt_pk_bf16_f32 v249, v250, v251\n"
"v_cvt_pk_bf16_f32 v250, v252, v253\n"
"v_cvt_pk_bf16_f32 v251, v254, v255\n"
"ds_read_b128 v[252:255], v21 offset:34816\n"
"v_accvgpr_read_b32 v21, a192\n"
"v_accvgpr_read_b32 v22, a193\n"
"v_accvgpr_read_b32 v23, a194\n"
"v_accvgpr_read_b32 v24, a195\n"
"v_accvgpr_read_b32 v25, a192\n"
"v_accvgpr_read_b32 v26, a193\n"
"v_accvgpr_read_b32 v27, a194\n"
"v_accvgpr_read_b32 v28, a195\n"
"v_add_u32 v25, 65536, v25\n"
"v_add_u32 v26, 65536, v26\n"
"v_add_u32 v27, 65536, v27\n"
"v_add_u32 v28, 65536, v28\n"
"buffer_store_dwordx4 v[32:35], v19, s[76:79], 0 offen offset:0 sc0 sc1\n"
"buffer_store_dwordx4 v[40:43], v19, s[76:79], 0 offen offset:1024 sc0 sc1\n"
"s_waitcnt lgkmcnt(15)\n"
"v_mfma_f32_16x16x32_bf16 a[200:203], a[196:199], v[36:39], 0\n"
"v_mfma_f32_16x16x32_bf16 a[208:211], v[36:39], v[36:39], 0\n"
"v_mfma_f32_16x16x32_bf16 a[204:207], a[196:199], v[148:151], 0\n"
"v_mfma_f32_16x16x32_bf16 a[212:215], v[148:151], v[148:151], 0\n"
"v_mfma_f32_16x16x32_bf16 a[200:203], a[196:199], v[44:47], a[200:203]\n"
"v_mfma_f32_16x16x32_bf16 a[208:211], v[44:47], v[44:47], a[208:211]\n"
"v_mfma_f32_16x16x32_bf16 a[204:207], a[196:199], v[156:159], a[204:207]\n"
"v_mfma_f32_16x16x32_bf16 a[212:215], v[156:159], v[156:159], a[212:215]\n"
"v_mfma_f32_16x16x32_bf16 a[200:203], a[196:199], v[52:55], a[200:203]\n"
"v_mfma_f32_16x16x32_bf16 a[208:211], v[52:55], v[52:55], a[208:211]\n"
"v_mfma_f32_16x16x32_bf16 a[204:207], a[196:199], v[164:167], a[204:207]\n"
"v_mfma_f32_16x16x32_bf16 a[212:215], v[164:167], v[164:167], a[212:215]\n"
"v_mfma_f32_16x16x32_bf16 a[200:203], a[196:199], v[60:63], a[200:203]\n"
"v_mfma_f32_16x16x32_bf16 a[208:211], v[60:63], v[60:63], a[208:211]\n"
"v_mfma_f32_16x16x32_bf16 a[204:207], a[196:199], v[172:175], a[204:207]\n"
"v_mfma_f32_16x16x32_bf16 a[212:215], v[172:175], v[172:175], a[212:215]\n"
"v_mfma_f32_16x16x32_bf16 a[200:203], a[196:199], v[100:103], a[200:203]\n"
"v_mfma_f32_16x16x32_bf16 a[208:211], v[100:103], v[100:103], a[208:211]\n"
"v_mfma_f32_16x16x32_bf16 a[204:207], a[196:199], v[212:215], a[204:207]\n"
"v_mfma_f32_16x16x32_bf16 a[212:215], v[212:215], v[212:215], a[212:215]\n"
"v_mfma_f32_16x16x32_bf16 a[200:203], a[196:199], v[108:111], a[200:203]\n"
"v_mfma_f32_16x16x32_bf16 a[208:211], v[108:111], v[108:111], a[208:211]\n"
"v_mfma_f32_16x16x32_bf16 a[204:207], a[196:199], v[220:223], a[204:207]\n"
"v_mfma_f32_16x16x32_bf16 a[212:215], v[220:223], v[220:223], a[212:215]\n"
"v_mfma_f32_16x16x32_bf16 a[200:203], a[196:199], v[116:119], a[200:203]\n"
"v_mfma_f32_16x16x32_bf16 a[208:211], v[116:119], v[116:119], a[208:211]\n"
"s_waitcnt lgkmcnt(14)\n"
"v_mfma_f32_16x16x32_bf16 a[204:207], a[196:199], v[228:231], a[204:207]\n"
"v_mfma_f32_16x16x32_bf16 a[212:215], v[228:231], v[228:231], a[212:215]\n"
"s_waitcnt lgkmcnt(13)\n"
"v_mfma_f32_16x16x32_bf16 a[200:203], a[196:199], v[124:127], a[200:203]\n"
"v_mfma_f32_16x16x32_bf16 a[208:211], v[124:127], v[124:127], a[208:211]\n"
"s_waitcnt lgkmcnt(12)\n"
"v_mfma_f32_16x16x32_bf16 a[204:207], a[196:199], v[236:239], a[204:207]\n"
"v_mfma_f32_16x16x32_bf16 a[212:215], v[236:239], v[236:239], a[212:215]\n"
"s_waitcnt lgkmcnt(11)\n"
"v_mfma_f32_16x16x32_bf16 a[200:203], a[196:199], v[68:71], a[200:203]\n"
"v_mfma_f32_16x16x32_bf16 a[208:211], v[68:71], v[68:71], a[208:211]\n"
"s_waitcnt lgkmcnt(10)\n"
"v_mfma_f32_16x16x32_bf16 a[204:207], a[196:199], v[180:183], a[204:207]\n"
"v_mfma_f32_16x16x32_bf16 a[212:215], v[180:183], v[180:183], a[212:215]\n"
"s_waitcnt lgkmcnt(9)\n"
"v_mfma_f32_16x16x32_bf16 a[200:203], a[196:199], v[76:79], a[200:203]\n"
"v_mfma_f32_16x16x32_bf16 a[208:211], v[76:79], v[76:79], a[208:211]\n"
"s_waitcnt lgkmcnt(8)\n"
"v_mfma_f32_16x16x32_bf16 a[204:207], a[196:199], v[188:191], a[204:207]\n"
"v_mfma_f32_16x16x32_bf16 a[212:215], v[188:191], v[188:191], a[212:215]\n"
"s_waitcnt lgkmcnt(7)\n"
"v_mfma_f32_16x16x32_bf16 a[200:203], a[196:199], v[84:87], a[200:203]\n"
"v_mfma_f32_16x16x32_bf16 a[208:211], v[84:87], v[84:87], a[208:211]\n"
"s_waitcnt lgkmcnt(6)\n"
"v_mfma_f32_16x16x32_bf16 a[204:207], a[196:199], v[196:199], a[204:207]\n"
"v_mfma_f32_16x16x32_bf16 a[212:215], v[196:199], v[196:199], a[212:215]\n"
"s_waitcnt lgkmcnt(5)\n"
"v_mfma_f32_16x16x32_bf16 a[200:203], a[196:199], v[92:95], a[200:203]\n"
"v_mfma_f32_16x16x32_bf16 a[208:211], v[92:95], v[92:95], a[208:211]\n"
"s_waitcnt lgkmcnt(4)\n"
"v_mfma_f32_16x16x32_bf16 a[204:207], a[196:199], v[204:207], a[204:207]\n"
"v_mfma_f32_16x16x32_bf16 a[212:215], v[204:207], v[204:207], a[212:215]\n"
"s_waitcnt lgkmcnt(3)\n"
"v_mfma_f32_16x16x32_bf16 a[200:203], a[196:199], v[132:135], a[200:203]\n"
"v_mfma_f32_16x16x32_bf16 a[208:211], v[132:135], v[132:135], a[208:211]\n"
"s_waitcnt lgkmcnt(2)\n"
"v_mfma_f32_16x16x32_bf16 a[204:207], a[196:199], v[244:247], a[204:207]\n"
"v_mfma_f32_16x16x32_bf16 a[212:215], v[244:247], v[244:247], a[212:215]\n"
"s_waitcnt lgkmcnt(1)\n"
"v_mfma_f32_16x16x32_bf16 a[200:203], a[196:199], v[140:143], a[200:203]\n"
"v_mfma_f32_16x16x32_bf16 a[208:211], v[140:143], v[140:143], a[208:211]\n"
"s_waitcnt lgkmcnt(0)\n"
"v_mfma_f32_16x16x32_bf16 a[204:207], a[196:199], v[252:255], a[204:207]\n"
"v_mfma_f32_16x16x32_bf16 a[212:215], v[252:255], v[252:255], a[212:215]\n"
"s_waitcnt lgkmcnt(0)\n"
"s_barrier\n"
"v_and_b32 v30, 15, v29\n"
"v_lshrrev_b32 v31, 2, v30\n"
"v_lshl_add_u32 v31, v31, 4, v30\n"
"v_lshlrev_b32 v31, 2, v31\n"
"v_and_b32 v30, 3, v29\n"
"s_nop 3\n"
"v_accvgpr_read_b32 v1, a208\n"
"v_accvgpr_read_b32 v2, a209\n"
"v_accvgpr_read_b32 v3, a210\n"
"v_accvgpr_read_b32 v4, a211\n"
"v_accvgpr_read_b32 v5, a212\n"
"v_accvgpr_read_b32 v6, a213\n"
"v_accvgpr_read_b32 v7, a214\n"
"v_accvgpr_read_b32 v8, a215\n"
"v_accvgpr_read_b32 v40, a200\n"
"v_accvgpr_read_b32 v42, a204\n"
"v_cmp_eq_u32 vcc, 1, v30\n"
"s_nop 1\n"
"v_cndmask_b32 v1, v1, v2, vcc\n"
"v_cndmask_b32 v5, v5, v6, vcc\n"
"v_cmp_eq_u32 vcc, 2, v30\n"
"s_nop 1\n"
"v_cndmask_b32 v1, v1, v3, vcc\n"
"v_cndmask_b32 v5, v5, v7, vcc\n"
"v_cmp_eq_u32 vcc, 3, v30\n"
"s_nop 1\n"
"v_cndmask_b32 v1, v1, v4, vcc\n"
"v_cndmask_b32 v5, v5, v8, vcc\n"
"ds_bpermute_b32 v2, v31, v1\n"
"ds_bpermute_b32 v6, v31, v5\n"
"s_cmp_eq_u32 s54, 1\n"
"s_mov_b32 s43, 0x3db8aa3b\n"
"s_cselect_b32 s43, 0xbfb8aa3b, s43\n"
"v_mul_f32 v40, 0x3b124925, v40\n"
"s_waitcnt lgkmcnt(1)\n"
"v_mul_f32 v2, 0x3b124925, v2\n"
"v_fma_f32 v2, -v40, v40, v2\n"
"v_max_f32 v2, 0, v2\n"
"v_add_f32 v2, 0x3727c5ac, v2\n"
"v_rsq_f32 v2, v2\n"
"s_nop 0\n"
"v_mul_f32 v32, s43, v2\n"
"v_mul_f32 v40, v40, -v32\n"
"v_mul_f32 v42, 0x3b124925, v42\n"
"s_waitcnt lgkmcnt(0)\n"
"v_mul_f32 v6, 0x3b124925, v6\n"
"v_fma_f32 v6, -v42, v42, v6\n"
"v_max_f32 v6, 0, v6\n"
"v_add_f32 v6, 0x3727c5ac, v6\n"
"v_rsq_f32 v6, v6\n"
"s_nop 0\n"
"v_mul_f32 v34, s43, v6\n"
"v_mul_f32 v42, v42, -v34\n"
"v_lshlrev_b32 v2, 16, v36\n"
"v_and_b32 v3, 0xffff0000, v36\n"
"s_add_u32 m0, s85, 98304\n"
"s_nop 0\n"
"buffer_load_dwordx4 v9, s[80:83], s84 offen offset:0 lds\n"
"v_lshlrev_b32 v4, 16, v37\n"
"v_and_b32 v5, 0xffff0000, v37\n"
"v_pk_fma_f32 v[2:3], v[2:3], v[32:33], v[40:41] op_sel_hi:[1,0,0]\n"
"v_lshlrev_b32 v6, 16, v38\n"
"v_and_b32 v7, 0xffff0000, v38\n"
"v_pk_fma_f32 v[4:5], v[4:5], v[32:33], v[40:41] op_sel_hi:[1,0,0]\n"
"v_cvt_pk_bf16_f32 v36, v2, v3\n"
"v_lshlrev_b32 v2, 16, v39\n"
"v_and_b32 v3, 0xffff0000, v39\n"
"v_pk_fma_f32 v[6:7], v[6:7], v[32:33], v[40:41] op_sel_hi:[1,0,0]\n"
"v_cvt_pk_bf16_f32 v37, v4, v5\n"
"v_lshlrev_b32 v4, 16, v148\n"
"v_and_b32 v5, 0xffff0000, v148\n"
"v_pk_fma_f32 v[2:3], v[2:3], v[32:33], v[40:41] op_sel_hi:[1,0,0]\n"
"v_cvt_pk_bf16_f32 v38, v6, v7\n"
"buffer_load_dwordx4 v10, s[80:83], s84 offen offset:1024 lds\n"
"v_lshlrev_b32 v6, 16, v149\n"
"v_and_b32 v7, 0xffff0000, v149\n"
"v_pk_fma_f32 v[4:5], v[4:5], v[34:35], v[42:43] op_sel_hi:[1,0,0]\n"
"v_cvt_pk_bf16_f32 v39, v2, v3\n"
"v_lshlrev_b32 v2, 16, v150\n"
"v_and_b32 v3, 0xffff0000, v150\n"
"v_pk_fma_f32 v[6:7], v[6:7], v[34:35], v[42:43] op_sel_hi:[1,0,0]\n"
"v_cvt_pk_bf16_f32 v148, v4, v5\n"
"v_lshlrev_b32 v4, 16, v151\n"
"v_and_b32 v5, 0xffff0000, v151\n"
"v_pk_fma_f32 v[2:3], v[2:3], v[34:35], v[42:43] op_sel_hi:[1,0,0]\n"
"v_cvt_pk_bf16_f32 v149, v6, v7\n"
"v_lshlrev_b32 v6, 16, v44\n"
"v_and_b32 v7, 0xffff0000, v44\n"
"v_pk_fma_f32 v[4:5], v[4:5], v[34:35], v[42:43] op_sel_hi:[1,0,0]\n"
"v_cvt_pk_bf16_f32 v150, v2, v3\n"
"buffer_load_dwordx4 v11, s[80:83], s84 offen offset:2048 lds\n"
"v_lshlrev_b32 v2, 16, v45\n"
"v_and_b32 v3, 0xffff0000, v45\n"
"v_pk_fma_f32 v[6:7], v[6:7], v[32:33], v[40:41] op_sel_hi:[1,0,0]\n"
"v_cvt_pk_bf16_f32 v151, v4, v5\n"
"v_lshlrev_b32 v4, 16, v46\n"
"v_and_b32 v5, 0xffff0000, v46\n"
"v_pk_fma_f32 v[2:3], v[2:3], v[32:33], v[40:41] op_sel_hi:[1,0,0]\n"
"v_cvt_pk_bf16_f32 v44, v6, v7\n"
"v_lshlrev_b32 v6, 16, v47\n"
"v_and_b32 v7, 0xffff0000, v47\n"
"v_pk_fma_f32 v[4:5], v[4:5], v[32:33], v[40:41] op_sel_hi:[1,0,0]\n"
"v_cvt_pk_bf16_f32 v45, v2, v3\n"
"v_lshlrev_b32 v2, 16, v156\n"
"v_and_b32 v3, 0xffff0000, v156\n"
"v_pk_fma_f32 v[6:7], v[6:7], v[32:33], v[40:41] op_sel_hi:[1,0,0]\n"
"v_cvt_pk_bf16_f32 v46, v4, v5\n"
"buffer_load_dwordx4 v12, s[80:83], s84 offen offset:3072 lds\n"
"v_lshlrev_b32 v4, 16, v157\n"
"v_and_b32 v5, 0xffff0000, v157\n"
"v_pk_fma_f32 v[2:3], v[2:3], v[34:35], v[42:43] op_sel_hi:[1,0,0]\n"
"v_cvt_pk_bf16_f32 v47, v6, v7\n"
"v_lshlrev_b32 v6, 16, v158\n"
"v_and_b32 v7, 0xffff0000, v158\n"
"v_pk_fma_f32 v[4:5], v[4:5], v[34:35], v[42:43] op_sel_hi:[1,0,0]\n"
"v_cvt_pk_bf16_f32 v156, v2, v3\n"
"v_lshlrev_b32 v2, 16, v159\n"
"v_and_b32 v3, 0xffff0000, v159\n"
"v_pk_fma_f32 v[6:7], v[6:7], v[34:35], v[42:43] op_sel_hi:[1,0,0]\n"
"v_cvt_pk_bf16_f32 v157, v4, v5\n"
"v_lshlrev_b32 v4, 16, v52\n"
"v_and_b32 v5, 0xffff0000, v52\n"
"v_pk_fma_f32 v[2:3], v[2:3], v[34:35], v[42:43] op_sel_hi:[1,0,0]\n"
"v_cvt_pk_bf16_f32 v158, v6, v7\n"
"s_add_u32 m0, s85, 102400\n"
"s_nop 0\n"
"buffer_load_dwordx4 v13, s[80:83], s84 offen offset:0 lds\n"
"v_lshlrev_b32 v6, 16, v53\n"
"v_and_b32 v7, 0xffff0000, v53\n"
"v_pk_fma_f32 v[4:5], v[4:5], v[32:33], v[40:41] op_sel_hi:[1,0,0]\n"
"v_cvt_pk_bf16_f32 v159, v2, v3\n"
"v_lshlrev_b32 v2, 16, v54\n"
"v_and_b32 v3, 0xffff0000, v54\n"
"v_pk_fma_f32 v[6:7], v[6:7], v[32:33], v[40:41] op_sel_hi:[1,0,0]\n"
"v_cvt_pk_bf16_f32 v52, v4, v5\n"
"v_lshlrev_b32 v4, 16, v55\n"
"v_and_b32 v5, 0xffff0000, v55\n"
"v_pk_fma_f32 v[2:3], v[2:3], v[32:33], v[40:41] op_sel_hi:[1,0,0]\n"
"v_cvt_pk_bf16_f32 v53, v6, v7\n"
"v_lshlrev_b32 v6, 16, v164\n"
"v_and_b32 v7, 0xffff0000, v164\n"
"v_pk_fma_f32 v[4:5], v[4:5], v[32:33], v[40:41] op_sel_hi:[1,0,0]\n"
"v_cvt_pk_bf16_f32 v54, v2, v3\n"
"buffer_load_dwordx4 v14, s[80:83], s84 offen offset:1024 lds\n"
"v_lshlrev_b32 v2, 16, v165\n"
"v_and_b32 v3, 0xffff0000, v165\n"
"v_pk_fma_f32 v[6:7], v[6:7], v[34:35], v[42:43] op_sel_hi:[1,0,0]\n"
"v_cvt_pk_bf16_f32 v55, v4, v5\n"
"v_lshlrev_b32 v4, 16, v166\n"
"v_and_b32 v5, 0xffff0000, v166\n"
"v_pk_fma_f32 v[2:3], v[2:3], v[34:35], v[42:43] op_sel_hi:[1,0,0]\n"
"v_cvt_pk_bf16_f32 v164, v6, v7\n"
"v_lshlrev_b32 v6, 16, v167\n"
"v_and_b32 v7, 0xffff0000, v167\n"
"v_pk_fma_f32 v[4:5], v[4:5], v[34:35], v[42:43] op_sel_hi:[1,0,0]\n"
"v_cvt_pk_bf16_f32 v165, v2, v3\n"
"v_lshlrev_b32 v2, 16, v60\n"
"v_and_b32 v3, 0xffff0000, v60\n"
"v_pk_fma_f32 v[6:7], v[6:7], v[34:35], v[42:43] op_sel_hi:[1,0,0]\n"
"v_cvt_pk_bf16_f32 v166, v4, v5\n"
"s_mov_b32 s43, 16384\n"
"buffer_store_dwordx4 v[48:51], v19, s[76:79], s43 offen offset:0 sc0 sc1\n"
"v_lshlrev_b32 v4, 16, v61\n"
"v_and_b32 v5, 0xffff0000, v61\n"
"v_pk_fma_f32 v[2:3], v[2:3], v[32:33], v[40:41] op_sel_hi:[1,0,0]\n"
"v_cvt_pk_bf16_f32 v167, v6, v7\n"
"v_lshlrev_b32 v6, 16, v62\n"
"v_and_b32 v7, 0xffff0000, v62\n"
"v_pk_fma_f32 v[4:5], v[4:5], v[32:33], v[40:41] op_sel_hi:[1,0,0]\n"
"v_cvt_pk_bf16_f32 v60, v2, v3\n"
"v_lshlrev_b32 v2, 16, v63\n"
"v_and_b32 v3, 0xffff0000, v63\n"
"v_pk_fma_f32 v[6:7], v[6:7], v[32:33], v[40:41] op_sel_hi:[1,0,0]\n"
"v_cvt_pk_bf16_f32 v61, v4, v5\n"
"v_lshlrev_b32 v4, 16, v172\n"
"v_and_b32 v5, 0xffff0000, v172\n"
"v_pk_fma_f32 v[2:3], v[2:3], v[32:33], v[40:41] op_sel_hi:[1,0,0]\n"
"v_cvt_pk_bf16_f32 v62, v6, v7\n"
"buffer_load_dwordx4 v15, s[80:83], s84 offen offset:2048 lds\n"
"s_add_u32 s84, s84, s87\n"
"v_lshlrev_b32 v6, 16, v173\n"
"v_and_b32 v7, 0xffff0000, v173\n"
"v_pk_fma_f32 v[4:5], v[4:5], v[34:35], v[42:43] op_sel_hi:[1,0,0]\n"
"v_cvt_pk_bf16_f32 v63, v2, v3\n"
"v_lshlrev_b32 v2, 16, v174\n"
"v_and_b32 v3, 0xffff0000, v174\n"
"v_pk_fma_f32 v[6:7], v[6:7], v[34:35], v[42:43] op_sel_hi:[1,0,0]\n"
"v_cvt_pk_bf16_f32 v172, v4, v5\n"
"v_lshlrev_b32 v4, 16, v175\n"
"v_and_b32 v5, 0xffff0000, v175\n"
"v_pk_fma_f32 v[2:3], v[2:3], v[34:35], v[42:43] op_sel_hi:[1,0,0]\n"
"v_cvt_pk_bf16_f32 v173, v6, v7\n"
"v_lshlrev_b32 v6, 16, v68\n"
"v_and_b32 v7, 0xffff0000, v68\n"
"v_pk_fma_f32 v[4:5], v[4:5], v[34:35], v[42:43] op_sel_hi:[1,0,0]\n"
"v_cvt_pk_bf16_f32 v174, v2, v3\n"
"s_mov_b32 s43, 16384\n"
"buffer_store_dwordx4 v[56:59], v19, s[76:79], s43 offen offset:1024 sc0 sc1\n"
"v_lshlrev_b32 v2, 16, v69\n"
"v_and_b32 v3, 0xffff0000, v69\n"
"v_pk_fma_f32 v[6:7], v[6:7], v[32:33], v[40:41] op_sel_hi:[1,0,0]\n"
"v_cvt_pk_bf16_f32 v175, v4, v5\n"
"v_lshlrev_b32 v4, 16, v70\n"
"v_and_b32 v5, 0xffff0000, v70\n"
"v_pk_fma_f32 v[2:3], v[2:3], v[32:33], v[40:41] op_sel_hi:[1,0,0]\n"
"v_cvt_pk_bf16_f32 v68, v6, v7\n"
"v_lshlrev_b32 v6, 16, v71\n"
"v_and_b32 v7, 0xffff0000, v71\n"
"v_pk_fma_f32 v[4:5], v[4:5], v[32:33], v[40:41] op_sel_hi:[1,0,0]\n"
"v_cvt_pk_bf16_f32 v69, v2, v3\n"
"v_lshlrev_b32 v2, 16, v180\n"
"v_and_b32 v3, 0xffff0000, v180\n"
"v_pk_fma_f32 v[6:7], v[6:7], v[32:33], v[40:41] op_sel_hi:[1,0,0]\n"
"v_cvt_pk_bf16_f32 v70, v4, v5\n"
"s_add_u32 m0, s85, 131072\n"
"s_nop 0\n"
"buffer_load_dwordx4 v9, s[80:83], s84 offen offset:0 lds\n"
"v_lshlrev_b32 v4, 16, v181\n"
"v_and_b32 v5, 0xffff0000, v181\n"
"v_pk_fma_f32 v[2:3], v[2:3], v[34:35], v[42:43] op_sel_hi:[1,0,0]\n"
"v_cvt_pk_bf16_f32 v71, v6, v7\n"
"v_lshlrev_b32 v6, 16, v182\n"
"v_and_b32 v7, 0xffff0000, v182\n"
"v_pk_fma_f32 v[4:5], v[4:5], v[34:35], v[42:43] op_sel_hi:[1,0,0]\n"
"v_cvt_pk_bf16_f32 v180, v2, v3\n"
"v_lshlrev_b32 v2, 16, v183\n"
"v_and_b32 v3, 0xffff0000, v183\n"
"v_pk_fma_f32 v[6:7], v[6:7], v[34:35], v[42:43] op_sel_hi:[1,0,0]\n"
"v_cvt_pk_bf16_f32 v181, v4, v5\n"
"v_lshlrev_b32 v4, 16, v76\n"
"v_and_b32 v5, 0xffff0000, v76\n"
"v_pk_fma_f32 v[2:3], v[2:3], v[34:35], v[42:43] op_sel_hi:[1,0,0]\n"
"v_cvt_pk_bf16_f32 v182, v6, v7\n"
"s_mov_b32 s43, 16384\n"
"buffer_store_dwordx4 v[64:67], v19, s[76:79], s43 offen offset:2048 sc0 sc1\n"
"v_lshlrev_b32 v6, 16, v77\n"
"v_and_b32 v7, 0xffff0000, v77\n"
"v_pk_fma_f32 v[4:5], v[4:5], v[32:33], v[40:41] op_sel_hi:[1,0,0]\n"
"v_cvt_pk_bf16_f32 v183, v2, v3\n"
"v_lshlrev_b32 v2, 16, v78\n"
"v_and_b32 v3, 0xffff0000, v78\n"
"v_pk_fma_f32 v[6:7], v[6:7], v[32:33], v[40:41] op_sel_hi:[1,0,0]\n"
"v_cvt_pk_bf16_f32 v76, v4, v5\n"
"v_lshlrev_b32 v4, 16, v79\n"
"v_and_b32 v5, 0xffff0000, v79\n"
"v_pk_fma_f32 v[2:3], v[2:3], v[32:33], v[40:41] op_sel_hi:[1,0,0]\n"
"v_cvt_pk_bf16_f32 v77, v6, v7\n"
"v_lshlrev_b32 v6, 16, v188\n"
"v_and_b32 v7, 0xffff0000, v188\n"
"v_pk_fma_f32 v[4:5], v[4:5], v[32:33], v[40:41] op_sel_hi:[1,0,0]\n"
"v_cvt_pk_bf16_f32 v78, v2, v3\n"
"buffer_load_dwordx4 v10, s[80:83], s84 offen offset:1024 lds\n"
"v_lshlrev_b32 v2, 16, v189\n"
"v_and_b32 v3, 0xffff0000, v189\n"
"v_pk_fma_f32 v[6:7], v[6:7], v[34:35], v[42:43] op_sel_hi:[1,0,0]\n"
"v_cvt_pk_bf16_f32 v79, v4, v5\n"
"v_lshlrev_b32 v4, 16, v190\n"
"v_and_b32 v5, 0xffff0000, v190\n"
"v_pk_fma_f32 v[2:3], v[2:3], v[34:35], v[42:43] op_sel_hi:[1,0,0]\n"
"v_cvt_pk_bf16_f32 v188, v6, v7\n"
"v_lshlrev_b32 v6, 16, v191\n"
"v_and_b32 v7, 0xffff0000, v191\n"
"v_pk_fma_f32 v[4:5], v[4:5], v[34:35], v[42:43] op_sel_hi:[1,0,0]\n"
"v_cvt_pk_bf16_f32 v189, v2, v3\n"
"v_lshlrev_b32 v2, 16, v84\n"
"v_and_b32 v3, 0xffff0000, v84\n"
"v_pk_fma_f32 v[6:7], v[6:7], v[34:35], v[42:43] op_sel_hi:[1,0,0]\n"
"v_cvt_pk_bf16_f32 v190, v4, v5\n"
"s_mov_b32 s43, 16384\n"
"buffer_store_dwordx4 v[72:75], v19, s[76:79], s43 offen offset:3072 sc0 sc1\n"
"v_lshlrev_b32 v4, 16, v85\n"
"v_and_b32 v5, 0xffff0000, v85\n"
"v_pk_fma_f32 v[2:3], v[2:3], v[32:33], v[40:41] op_sel_hi:[1,0,0]\n"
"v_cvt_pk_bf16_f32 v191, v6, v7\n"
"v_lshlrev_b32 v6, 16, v86\n"
"v_and_b32 v7, 0xffff0000, v86\n"
"v_pk_fma_f32 v[4:5], v[4:5], v[32:33], v[40:41] op_sel_hi:[1,0,0]\n"
"v_cvt_pk_bf16_f32 v84, v2, v3\n"
"v_lshlrev_b32 v2, 16, v87\n"
"v_and_b32 v3, 0xffff0000, v87\n"
"v_pk_fma_f32 v[6:7], v[6:7], v[32:33], v[40:41] op_sel_hi:[1,0,0]\n"
"v_cvt_pk_bf16_f32 v85, v4, v5\n"
"v_lshlrev_b32 v4, 16, v196\n"
"v_and_b32 v5, 0xffff0000, v196\n"
"v_pk_fma_f32 v[2:3], v[2:3], v[32:33], v[40:41] op_sel_hi:[1,0,0]\n"
"v_cvt_pk_bf16_f32 v86, v6, v7\n"
"buffer_load_dwordx4 v11, s[80:83], s84 offen offset:2048 lds\n"
"v_lshlrev_b32 v6, 16, v197\n"
"v_and_b32 v7, 0xffff0000, v197\n"
"v_pk_fma_f32 v[4:5], v[4:5], v[34:35], v[42:43] op_sel_hi:[1,0,0]\n"
"v_cvt_pk_bf16_f32 v87, v2, v3\n"
"v_lshlrev_b32 v2, 16, v198\n"
"v_and_b32 v3, 0xffff0000, v198\n"
"v_pk_fma_f32 v[6:7], v[6:7], v[34:35], v[42:43] op_sel_hi:[1,0,0]\n"
"v_cvt_pk_bf16_f32 v196, v4, v5\n"
"v_lshlrev_b32 v4, 16, v199\n"
"v_and_b32 v5, 0xffff0000, v199\n"
"v_pk_fma_f32 v[2:3], v[2:3], v[34:35], v[42:43] op_sel_hi:[1,0,0]\n"
"v_cvt_pk_bf16_f32 v197, v6, v7\n"
"v_lshlrev_b32 v6, 16, v92\n"
"v_and_b32 v7, 0xffff0000, v92\n"
"v_pk_fma_f32 v[4:5], v[4:5], v[34:35], v[42:43] op_sel_hi:[1,0,0]\n"
"v_cvt_pk_bf16_f32 v198, v2, v3\n"
"s_mov_b32 s43, 24576\n"
"buffer_store_dwordx4 v[80:83], v19, s[76:79], s43 offen offset:0 sc0 sc1\n"
"v_lshlrev_b32 v2, 16, v93\n"
"v_and_b32 v3, 0xffff0000, v93\n"
"v_pk_fma_f32 v[6:7], v[6:7], v[32:33], v[40:41] op_sel_hi:[1,0,0]\n"
"v_cvt_pk_bf16_f32 v199, v4, v5\n"
"v_lshlrev_b32 v4, 16, v94\n"
"v_and_b32 v5, 0xffff0000, v94\n"
"v_pk_fma_f32 v[2:3], v[2:3], v[32:33], v[40:41] op_sel_hi:[1,0,0]\n"
"v_cvt_pk_bf16_f32 v92, v6, v7\n"
"v_lshlrev_b32 v6, 16, v95\n"
"v_and_b32 v7, 0xffff0000, v95\n"
"v_pk_fma_f32 v[4:5], v[4:5], v[32:33], v[40:41] op_sel_hi:[1,0,0]\n"
"v_cvt_pk_bf16_f32 v93, v2, v3\n"
"v_lshlrev_b32 v2, 16, v204\n"
"v_and_b32 v3, 0xffff0000, v204\n"
"v_pk_fma_f32 v[6:7], v[6:7], v[32:33], v[40:41] op_sel_hi:[1,0,0]\n"
"v_cvt_pk_bf16_f32 v94, v4, v5\n"
"buffer_load_dwordx4 v12, s[80:83], s84 offen offset:3072 lds\n"
"v_lshlrev_b32 v4, 16, v205\n"
"v_and_b32 v5, 0xffff0000, v205\n"
"v_pk_fma_f32 v[2:3], v[2:3], v[34:35], v[42:43] op_sel_hi:[1,0,0]\n"
"v_cvt_pk_bf16_f32 v95, v6, v7\n"
"v_lshlrev_b32 v6, 16, v206\n"
"v_and_b32 v7, 0xffff0000, v206\n"
"v_pk_fma_f32 v[4:5], v[4:5], v[34:35], v[42:43] op_sel_hi:[1,0,0]\n"
"v_cvt_pk_bf16_f32 v204, v2, v3\n"
"v_lshlrev_b32 v2, 16, v207\n"
"v_and_b32 v3, 0xffff0000, v207\n"
"v_pk_fma_f32 v[6:7], v[6:7], v[34:35], v[42:43] op_sel_hi:[1,0,0]\n"
"v_cvt_pk_bf16_f32 v205, v4, v5\n"
"v_lshlrev_b32 v4, 16, v100\n"
"v_and_b32 v5, 0xffff0000, v100\n"
"v_pk_fma_f32 v[2:3], v[2:3], v[34:35], v[42:43] op_sel_hi:[1,0,0]\n"
"v_cvt_pk_bf16_f32 v206, v6, v7\n"
"s_mov_b32 s43, 24576\n"
"buffer_store_dwordx4 v[88:91], v19, s[76:79], s43 offen offset:1024 sc0 sc1\n"
"v_lshlrev_b32 v6, 16, v101\n"
"v_and_b32 v7, 0xffff0000, v101\n"
"v_pk_fma_f32 v[4:5], v[4:5], v[32:33], v[40:41] op_sel_hi:[1,0,0]\n"
"v_cvt_pk_bf16_f32 v207, v2, v3\n"
"v_lshlrev_b32 v2, 16, v102\n"
"v_and_b32 v3, 0xffff0000, v102\n"
"v_pk_fma_f32 v[6:7], v[6:7], v[32:33], v[40:41] op_sel_hi:[1,0,0]\n"
"v_cvt_pk_bf16_f32 v100, v4, v5\n"
"v_lshlrev_b32 v4, 16, v103\n"
"v_and_b32 v5, 0xffff0000, v103\n"
"v_pk_fma_f32 v[2:3], v[2:3], v[32:33], v[40:41] op_sel_hi:[1,0,0]\n"
"v_cvt_pk_bf16_f32 v101, v6, v7\n"
"v_lshlrev_b32 v6, 16, v212\n"
"v_and_b32 v7, 0xffff0000, v212\n"
"v_pk_fma_f32 v[4:5], v[4:5], v[32:33], v[40:41] op_sel_hi:[1,0,0]\n"
"v_cvt_pk_bf16_f32 v102, v2, v3\n"
"s_add_u32 m0, s85, 135168\n"
"s_nop 0\n"
"buffer_load_dwordx4 v13, s[80:83], s84 offen offset:0 lds\n"
"v_lshlrev_b32 v2, 16, v213\n"
"v_and_b32 v3, 0xffff0000, v213\n"
"v_pk_fma_f32 v[6:7], v[6:7], v[34:35], v[42:43] op_sel_hi:[1,0,0]\n"
"v_cvt_pk_bf16_f32 v103, v4, v5\n"
"v_lshlrev_b32 v4, 16, v214\n"
"v_and_b32 v5, 0xffff0000, v214\n"
"v_pk_fma_f32 v[2:3], v[2:3], v[34:35], v[42:43] op_sel_hi:[1,0,0]\n"
"v_cvt_pk_bf16_f32 v212, v6, v7\n"
"v_lshlrev_b32 v6, 16, v215\n"
"v_and_b32 v7, 0xffff0000, v215\n"
"v_pk_fma_f32 v[4:5], v[4:5], v[34:35], v[42:43] op_sel_hi:[1,0,0]\n"
"v_cvt_pk_bf16_f32 v213, v2, v3\n"
"v_lshlrev_b32 v2, 16, v108\n"
"v_and_b32 v3, 0xffff0000, v108\n"
"v_pk_fma_f32 v[6:7], v[6:7], v[34:35], v[42:43] op_sel_hi:[1,0,0]\n"
"v_cvt_pk_bf16_f32 v214, v4, v5\n"
"s_mov_b32 s43, 24576\n"
"buffer_store_dwordx4 v[96:99], v19, s[76:79], s43 offen offset:2048 sc0 sc1\n"
"v_lshlrev_b32 v4, 16, v109\n"
"v_and_b32 v5, 0xffff0000, v109\n"
"v_pk_fma_f32 v[2:3], v[2:3], v[32:33], v[40:41] op_sel_hi:[1,0,0]\n"
"v_cvt_pk_bf16_f32 v215, v6, v7\n"
"v_lshlrev_b32 v6, 16, v110\n"
"v_and_b32 v7, 0xffff0000, v110\n"
"v_pk_fma_f32 v[4:5], v[4:5], v[32:33], v[40:41] op_sel_hi:[1,0,0]\n"
"v_cvt_pk_bf16_f32 v108, v2, v3\n"
"v_lshlrev_b32 v2, 16, v111\n"
"v_and_b32 v3, 0xffff0000, v111\n"
"v_pk_fma_f32 v[6:7], v[6:7], v[32:33], v[40:41] op_sel_hi:[1,0,0]\n"
"v_cvt_pk_bf16_f32 v109, v4, v5\n"
"v_lshlrev_b32 v4, 16, v220\n"
"v_and_b32 v5, 0xffff0000, v220\n"
"v_pk_fma_f32 v[2:3], v[2:3], v[32:33], v[40:41] op_sel_hi:[1,0,0]\n"
"v_cvt_pk_bf16_f32 v110, v6, v7\n"
"buffer_load_dwordx4 v14, s[80:83], s84 offen offset:1024 lds\n"
"v_lshlrev_b32 v6, 16, v221\n"
"v_and_b32 v7, 0xffff0000, v221\n"
"v_pk_fma_f32 v[4:5], v[4:5], v[34:35], v[42:43] op_sel_hi:[1,0,0]\n"
"v_cvt_pk_bf16_f32 v111, v2, v3\n"
"v_lshlrev_b32 v2, 16, v222\n"
"v_and_b32 v3, 0xffff0000, v222\n"
"v_pk_fma_f32 v[6:7], v[6:7], v[34:35], v[42:43] op_sel_hi:[1,0,0]\n"
"v_cvt_pk_bf16_f32 v220, v4, v5\n"
"v_lshlrev_b32 v4, 16, v223\n"
"v_and_b32 v5, 0xffff0000, v223\n"
"v_pk_fma_f32 v[2:3], v[2:3], v[34:35], v[42:43] op_sel_hi:[1,0,0]\n"
"v_cvt_pk_bf16_f32 v221, v6, v7\n"
"v_lshlrev_b32 v6, 16, v116\n"
"v_and_b32 v7, 0xffff0000, v116\n"
"v_pk_fma_f32 v[4:5], v[4:5], v[34:35], v[42:43] op_sel_hi:[1,0,0]\n"
"v_cvt_pk_bf16_f32 v222, v2, v3\n"
"s_mov_b32 s43, 24576\n"
"buffer_store_dwordx4 v[104:107], v19, s[76:79], s43 offen offset:3072 sc0 sc1\n"
"v_lshlrev_b32 v2, 16, v117\n"
"v_and_b32 v3, 0xffff0000, v117\n"
"v_pk_fma_f32 v[6:7], v[6:7], v[32:33], v[40:41] op_sel_hi:[1,0,0]\n"
"v_cvt_pk_bf16_f32 v223, v4, v5\n"
"v_lshlrev_b32 v4, 16, v118\n"
"v_and_b32 v5, 0xffff0000, v118\n"
"v_pk_fma_f32 v[2:3], v[2:3], v[32:33], v[40:41] op_sel_hi:[1,0,0]\n"
"v_cvt_pk_bf16_f32 v116, v6, v7\n"
"v_lshlrev_b32 v6, 16, v119\n"
"v_and_b32 v7, 0xffff0000, v119\n"
"v_pk_fma_f32 v[4:5], v[4:5], v[32:33], v[40:41] op_sel_hi:[1,0,0]\n"
"v_cvt_pk_bf16_f32 v117, v2, v3\n"
"v_lshlrev_b32 v2, 16, v228\n"
"v_and_b32 v3, 0xffff0000, v228\n"
"v_pk_fma_f32 v[6:7], v[6:7], v[32:33], v[40:41] op_sel_hi:[1,0,0]\n"
"v_cvt_pk_bf16_f32 v118, v4, v5\n"
"buffer_load_dwordx4 v15, s[80:83], s84 offen offset:2048 lds\n"
"s_add_u32 s84, s84, s56\n"
"v_lshlrev_b32 v4, 16, v229\n"
"v_and_b32 v5, 0xffff0000, v229\n"
"v_pk_fma_f32 v[2:3], v[2:3], v[34:35], v[42:43] op_sel_hi:[1,0,0]\n"
"v_cvt_pk_bf16_f32 v119, v6, v7\n"
"v_lshlrev_b32 v6, 16, v230\n"
"v_and_b32 v7, 0xffff0000, v230\n"
"v_pk_fma_f32 v[4:5], v[4:5], v[34:35], v[42:43] op_sel_hi:[1,0,0]\n"
"v_cvt_pk_bf16_f32 v228, v2, v3\n"
"v_lshlrev_b32 v2, 16, v231\n"
"v_and_b32 v3, 0xffff0000, v231\n"
"v_pk_fma_f32 v[6:7], v[6:7], v[34:35], v[42:43] op_sel_hi:[1,0,0]\n"
"v_cvt_pk_bf16_f32 v229, v4, v5\n"
"v_lshlrev_b32 v4, 16, v124\n"
"v_and_b32 v5, 0xffff0000, v124\n"
"v_pk_fma_f32 v[2:3], v[2:3], v[34:35], v[42:43] op_sel_hi:[1,0,0]\n"
"v_cvt_pk_bf16_f32 v230, v6, v7\n"
"s_mov_b32 s43, 49152\n"
"buffer_store_dwordx4 v[112:115], v19, s[76:79], s43 offen offset:0 sc0 sc1\n"
"v_lshlrev_b32 v6, 16, v125\n"
"v_and_b32 v7, 0xffff0000, v125\n"
"v_pk_fma_f32 v[4:5], v[4:5], v[32:33], v[40:41] op_sel_hi:[1,0,0]\n"
"v_cvt_pk_bf16_f32 v231, v2, v3\n"
"v_lshlrev_b32 v2, 16, v126\n"
"v_and_b32 v3, 0xffff0000, v126\n"
"v_pk_fma_f32 v[6:7], v[6:7], v[32:33], v[40:41] op_sel_hi:[1,0,0]\n"
"v_cvt_pk_bf16_f32 v124, v4, v5\n"
"v_lshlrev_b32 v4, 16, v127\n"
"v_and_b32 v5, 0xffff0000, v127\n"
"v_pk_fma_f32 v[2:3], v[2:3], v[32:33], v[40:41] op_sel_hi:[1,0,0]\n"
"v_cvt_pk_bf16_f32 v125, v6, v7\n"
"v_lshlrev_b32 v6, 16, v236\n"
"v_and_b32 v7, 0xffff0000, v236\n"
"v_pk_fma_f32 v[4:5], v[4:5], v[32:33], v[40:41] op_sel_hi:[1,0,0]\n"
"v_cvt_pk_bf16_f32 v126, v2, v3\n"
"s_add_u32 m0, s85, 0\n"
"s_nop 0\n"
"buffer_load_dwordx4 v9, s[80:83], s84 offen offset:0 lds\n"
"v_lshlrev_b32 v2, 16, v237\n"
"v_and_b32 v3, 0xffff0000, v237\n"
"v_pk_fma_f32 v[6:7], v[6:7], v[34:35], v[42:43] op_sel_hi:[1,0,0]\n"
"v_cvt_pk_bf16_f32 v127, v4, v5\n"
"v_lshlrev_b32 v4, 16, v238\n"
"v_and_b32 v5, 0xffff0000, v238\n"
"v_pk_fma_f32 v[2:3], v[2:3], v[34:35], v[42:43] op_sel_hi:[1,0,0]\n"
"v_cvt_pk_bf16_f32 v236, v6, v7\n"
"v_lshlrev_b32 v6, 16, v239\n"
"v_and_b32 v7, 0xffff0000, v239\n"
"v_pk_fma_f32 v[4:5], v[4:5], v[34:35], v[42:43] op_sel_hi:[1,0,0]\n"
"v_cvt_pk_bf16_f32 v237, v2, v3\n"
"v_lshlrev_b32 v2, 16, v132\n"
"v_and_b32 v3, 0xffff0000, v132\n"
"v_pk_fma_f32 v[6:7], v[6:7], v[34:35], v[42:43] op_sel_hi:[1,0,0]\n"
"v_cvt_pk_bf16_f32 v238, v4, v5\n"
"s_mov_b32 s43, 49152\n"
"buffer_store_dwordx4 v[120:123], v19, s[76:79], s43 offen offset:1024 sc0 sc1\n"
"v_lshlrev_b32 v4, 16, v133\n"
"v_and_b32 v5, 0xffff0000, v133\n"
"v_pk_fma_f32 v[2:3], v[2:3], v[32:33], v[40:41] op_sel_hi:[1,0,0]\n"
"v_cvt_pk_bf16_f32 v239, v6, v7\n"
"v_lshlrev_b32 v6, 16, v134\n"
"v_and_b32 v7, 0xffff0000, v134\n"
"v_pk_fma_f32 v[4:5], v[4:5], v[32:33], v[40:41] op_sel_hi:[1,0,0]\n"
"v_cvt_pk_bf16_f32 v132, v2, v3\n"
"v_lshlrev_b32 v2, 16, v135\n"
"v_and_b32 v3, 0xffff0000, v135\n"
"v_pk_fma_f32 v[6:7], v[6:7], v[32:33], v[40:41] op_sel_hi:[1,0,0]\n"
"v_cvt_pk_bf16_f32 v133, v4, v5\n"
"v_lshlrev_b32 v4, 16, v244\n"
"v_and_b32 v5, 0xffff0000, v244\n"
"v_pk_fma_f32 v[2:3], v[2:3], v[32:33], v[40:41] op_sel_hi:[1,0,0]\n"
"v_cvt_pk_bf16_f32 v134, v6, v7\n"
"buffer_load_dwordx4 v10, s[80:83], s84 offen offset:1024 lds\n"
"v_lshlrev_b32 v6, 16, v245\n"
"v_and_b32 v7, 0xffff0000, v245\n"
"v_pk_fma_f32 v[4:5], v[4:5], v[34:35], v[42:43] op_sel_hi:[1,0,0]\n"
"v_cvt_pk_bf16_f32 v135, v2, v3\n"
"v_lshlrev_b32 v2, 16, v246\n"
"v_and_b32 v3, 0xffff0000, v246\n"
"v_pk_fma_f32 v[6:7], v[6:7], v[34:35], v[42:43] op_sel_hi:[1,0,0]\n"
"v_cvt_pk_bf16_f32 v244, v4, v5\n"
"v_lshlrev_b32 v4, 16, v247\n"
"v_and_b32 v5, 0xffff0000, v247\n"
"v_pk_fma_f32 v[2:3], v[2:3], v[34:35], v[42:43] op_sel_hi:[1,0,0]\n"
"v_cvt_pk_bf16_f32 v245, v6, v7\n"
"v_lshlrev_b32 v6, 16, v140\n"
"v_and_b32 v7, 0xffff0000, v140\n"
"v_pk_fma_f32 v[4:5], v[4:5], v[34:35], v[42:43] op_sel_hi:[1,0,0]\n"
"v_cvt_pk_bf16_f32 v246, v2, v3\n"
"s_mov_b32 s43, 49152\n"
"buffer_store_dwordx4 v[128:131], v19, s[76:79], s43 offen offset:2048 sc0 sc1\n"
"v_lshlrev_b32 v2, 16, v141\n"
"v_and_b32 v3, 0xffff0000, v141\n"
"v_pk_fma_f32 v[6:7], v[6:7], v[32:33], v[40:41] op_sel_hi:[1,0,0]\n"
"v_cvt_pk_bf16_f32 v247, v4, v5\n"
"v_lshlrev_b32 v4, 16, v142\n"
"v_and_b32 v5, 0xffff0000, v142\n"
"v_pk_fma_f32 v[2:3], v[2:3], v[32:33], v[40:41] op_sel_hi:[1,0,0]\n"
"v_cvt_pk_bf16_f32 v140, v6, v7\n"
"v_lshlrev_b32 v6, 16, v143\n"
"v_and_b32 v7, 0xffff0000, v143\n"
"v_pk_fma_f32 v[4:5], v[4:5], v[32:33], v[40:41] op_sel_hi:[1,0,0]\n"
"v_cvt_pk_bf16_f32 v141, v2, v3\n"
"v_lshlrev_b32 v2, 16, v252\n"
"v_and_b32 v3, 0xffff0000, v252\n"
"v_pk_fma_f32 v[6:7], v[6:7], v[32:33], v[40:41] op_sel_hi:[1,0,0]\n"
"v_cvt_pk_bf16_f32 v142, v4, v5\n"
"buffer_load_dwordx4 v11, s[80:83], s84 offen offset:2048 lds\n"
"v_lshlrev_b32 v4, 16, v253\n"
"v_and_b32 v5, 0xffff0000, v253\n"
"v_pk_fma_f32 v[2:3], v[2:3], v[34:35], v[42:43] op_sel_hi:[1,0,0]\n"
"v_cvt_pk_bf16_f32 v143, v6, v7\n"
"v_lshlrev_b32 v6, 16, v254\n"
"v_and_b32 v7, 0xffff0000, v254\n"
"v_pk_fma_f32 v[4:5], v[4:5], v[34:35], v[42:43] op_sel_hi:[1,0,0]\n"
"v_cvt_pk_bf16_f32 v252, v2, v3\n"
"v_lshlrev_b32 v2, 16, v255\n"
"v_and_b32 v3, 0xffff0000, v255\n"
"v_pk_fma_f32 v[6:7], v[6:7], v[34:35], v[42:43] op_sel_hi:[1,0,0]\n"
"v_cvt_pk_bf16_f32 v253, v4, v5\n"
"v_pk_fma_f32 v[2:3], v[2:3], v[34:35], v[42:43] op_sel_hi:[1,0,0]\n"
"v_cvt_pk_bf16_f32 v254, v6, v7\n"
"s_mov_b32 s43, 49152\n"
"buffer_store_dwordx4 v[136:139], v19, s[76:79], s43 offen offset:3072 sc0 sc1\n"
"v_cvt_pk_bf16_f32 v255, v2, v3\n"
"s_mov_b32 s38, 65536\n"
"s_mov_b32 s39, 32768\n"
"s_lshl_b32 s41, s86, 1\n"
"s_sub_u32 s41, s41, 4\n"
"s_sub_u32 s46, s86, 1\n"
"s_lshl_b32 s53, s52, 11\n"
"s_sub_u32 s53, s53, 2048\n"
"s_waitcnt vmcnt(35)\n"
"s_barrier\n"
"ds_read_b64_tr_b16 a[0:1], v25 offset:0\n"
"ds_read_b64_tr_b16 a[2:3], v25 offset:512\n"
"ds_read_b64_tr_b16 a[4:5], v25 offset:4096\n"
"ds_read_b64_tr_b16 a[6:7], v25 offset:4608\n"
"ds_read_b64_tr_b16 a[8:9], v25 offset:8192\n"
"ds_read_b64_tr_b16 a[10:11], v25 offset:8704\n"
"ds_read_b64_tr_b16 a[12:13], v25 offset:12288\n"
"ds_read_b64_tr_b16 a[14:15], v25 offset:12800\n"
"ds_read_b64_tr_b16 a[16:17], v25 offset:16384\n"
"ds_read_b64_tr_b16 a[18:19], v25 offset:16896\n"
"ds_read_b64_tr_b16 a[20:21], v25 offset:20480\n"
"ds_read_b64_tr_b16 a[22:23], v25 offset:20992\n"
"ds_read_b64_tr_b16 a[24:25], v25 offset:24576\n"
"ds_read_b64_tr_b16 a[26:27], v25 offset:25088\n"
"ds_read_b64_tr_b16 a[28:29], v26 offset:0\n"
"ds_read_b64_tr_b16 a[30:31], v26 offset:512\n"
"ds_read_b64_tr_b16 a[32:33], v26 offset:4096\n"
"ds_read_b64_tr_b16 a[34:35], v26 offset:4608\n"
"ds_read_b64_tr_b16 a[36:37], v26 offset:8192\n"
"ds_read_b64_tr_b16 a[38:39], v26 offset:8704\n"
"s_mov_b32 s48, 0xbfb8aa3b\n"
"s_branch LP%=\n"
"LV%=:\n"
"s_waitcnt lgkmcnt(1)\n"
"v_mfma_f32_16x16x32_bf16 v[32:35], a[128:131], a[0:3], v[32:35]\n"
"ds_read_b128 a[136:139], v19 offset:2048\n"
"s_waitcnt lgkmcnt(1)\n"
"v_mfma_f32_16x16x32_bf16 v[32:35], a[132:135], a[4:7], v[32:35]\n"
"ds_read_b128 a[140:143], v20 offset:2048\n"
"v_mfma_f32_16x16x32_bf16 v[144:147], a[128:131], a[8:11], v[144:147]\n"
"s_cmp_lt_u32 s46, 14\n"
"s_cselect_b32 s22, 0x80000, s34\n"
"v_mfma_f32_16x16x32_bf16 v[144:147], a[132:135], a[12:15], v[144:147]\n"
"ds_read_b64_tr_b16 a[64:65], v25 offset:0\n"
"v_mfma_f32_16x16x32_bf16 v[40:43], a[128:131], a[16:19], v[40:43]\n"
"buffer_load_dwordx4 v13, s[24:27], s37 offen offset:0 lds\n"
"v_mfma_f32_16x16x32_bf16 v[40:43], a[132:135], a[20:23], v[40:43]\n"
"ds_read_b64_tr_b16 a[66:67], v25 offset:512\n"
"v_mfma_f32_16x16x32_bf16 v[48:51], a[128:131], a[24:27], v[48:51]\n"
"v_mfma_f32_16x16x32_bf16 v[48:51], a[132:135], a[28:31], v[48:51]\n"
"ds_read_b64_tr_b16 a[68:69], v25 offset:4096\n"
"v_mfma_f32_16x16x32_bf16 v[160:163], a[128:131], a[32:35], v[160:163]\n"
"buffer_load_dwordx4 v14, s[24:27], s37 offen offset:1024 lds\n"
"v_mfma_f32_16x16x32_bf16 v[160:163], a[132:135], a[36:39], v[160:163]\n"
"ds_read_b64_tr_b16 a[70:71], v25 offset:4608\n"
"v_mfma_f32_16x16x32_bf16 v[176:179], a[128:131], a[40:43], v[176:179]\n"
"v_mfma_f32_16x16x32_bf16 v[176:179], a[132:135], a[44:47], v[176:179]\n"
"ds_read_b64_tr_b16 a[72:73], v26 offset:0\n"
"v_mfma_f32_16x16x32_bf16 v[56:59], a[128:131], a[48:51], v[56:59]\n"
"buffer_load_dwordx4 v15, s[24:27], s37 offen offset:2048 lds\n"
"v_mfma_f32_16x16x32_bf16 v[56:59], a[132:135], a[52:55], v[56:59]\n"
"v_mfma_f32_16x16x32_bf16 v[64:67], a[128:131], a[56:59], v[64:67]\n"
"buffer_load_dwordx4 v16, s[24:27], s37 offen offset:3072 lds\n"
"v_mfma_f32_16x16x32_bf16 v[64:67], a[132:135], a[60:63], v[64:67]\n"
"s_add_u32 m0, s38, s45\n"
"s_add_u32 s37, s37, s61\n"
"s_waitcnt lgkmcnt(6)\n"
"v_mfma_f32_16x16x32_bf16 v[36:39], a[136:139], a[0:3], v[36:39]\n"
"ds_read_b128 a[128:131], v19 offset:4096\n"
"s_waitcnt lgkmcnt(6)\n"
"v_mfma_f32_16x16x32_bf16 v[36:39], a[140:143], a[4:7], v[36:39]\n"
"ds_read_b128 a[132:135], v20 offset:4096\n"
"v_mfma_f32_16x16x32_bf16 v[148:151], a[136:139], a[8:11], v[148:151]\n"
"v_mfma_f32_16x16x32_bf16 v[148:151], a[140:143], a[12:15], v[148:151]\n"
"ds_read_b64_tr_b16 a[74:75], v26 offset:512\n"
"v_mfma_f32_16x16x32_bf16 v[44:47], a[136:139], a[16:19], v[44:47]\n"
"v_mfma_f32_16x16x32_bf16 v[44:47], a[140:143], a[20:23], v[44:47]\n"
"ds_read_b64_tr_b16 a[76:77], v26 offset:4096\n"
"v_mfma_f32_16x16x32_bf16 v[52:55], a[136:139], a[24:27], v[52:55]\n"
"buffer_load_dwordx4 v1, s[20:23], s36 offen offset:0 lds\n"
"v_mfma_f32_16x16x32_bf16 v[52:55], a[140:143], a[28:31], v[52:55]\n"
"ds_read_b64_tr_b16 a[78:79], v26 offset:4608\n"
"v_mfma_f32_16x16x32_bf16 v[164:167], a[136:139], a[32:35], v[164:167]\n"
"v_mfma_f32_16x16x32_bf16 v[164:167], a[140:143], a[36:39], v[164:167]\n"
"ds_read_b64_tr_b16 a[80:81], v27 offset:0\n"
"v_mfma_f32_16x16x32_bf16 v[180:183], a[136:139], a[40:43], v[180:183]\n"
"v_mfma_f32_16x16x32_bf16 v[180:183], a[140:143], a[44:47], v[180:183]\n"
"ds_read_b64_tr_b16 a[82:83], v27 offset:512\n"
"v_mfma_f32_16x16x32_bf16 v[60:63], a[136:139], a[48:51], v[60:63]\n"
"v_mfma_f32_16x16x32_bf16 v[60:63], a[140:143], a[52:55], v[60:63]\n"
"v_mfma_f32_16x16x32_bf16 v[68:71], a[136:139], a[56:59], v[68:71]\n"
"buffer_load_dwordx4 v2, s[20:23], s36 offen offset:1024 lds\n"
"v_mfma_f32_16x16x32_bf16 v[68:71], a[140:143], a[60:63], v[68:71]\n"
"s_waitcnt lgkmcnt(6)\n"
"v_mfma_f32_16x16x32_bf16 v[72:75], a[128:131], a[0:3], v[72:75]\n"
"ds_read_b128 a[136:139], v19 offset:6144\n"
"s_waitcnt lgkmcnt(6)\n"
"v_mfma_f32_16x16x32_bf16 v[72:75], a[132:135], a[4:7], v[72:75]\n"
"ds_read_b128 a[140:143], v20 offset:6144\n"
"v_mfma_f32_16x16x32_bf16 v[152:155], a[128:131], a[8:11], v[152:155]\n"
"v_mfma_f32_16x16x32_bf16 v[152:155], a[132:135], a[12:15], v[152:155]\n"
"ds_read_b64_tr_b16 a[84:85], v27 offset:4096\n"
"v_mfma_f32_16x16x32_bf16 v[80:83], a[128:131], a[16:19], v[80:83]\n"
"v_mfma_f32_16x16x32_bf16 v[80:83], a[132:135], a[20:23], v[80:83]\n"
"ds_read_b64_tr_b16 a[86:87], v27 offset:4608\n"
"v_mfma_f32_16x16x32_bf16 v[88:91], a[128:131], a[24:27], v[88:91]\n"
"v_mfma_f32_16x16x32_bf16 v[88:91], a[132:135], a[28:31], v[88:91]\n"
"ds_read_b64_tr_b16 a[88:89], v28 offset:0\n"
"v_mfma_f32_16x16x32_bf16 v[168:171], a[128:131], a[32:35], v[168:171]\n"
"v_mfma_f32_16x16x32_bf16 v[168:171], a[132:135], a[36:39], v[168:171]\n"
"ds_read_b64_tr_b16 a[90:91], v28 offset:512\n"
"v_mfma_f32_16x16x32_bf16 v[184:187], a[128:131], a[40:43], v[184:187]\n"
"v_mfma_f32_16x16x32_bf16 v[184:187], a[132:135], a[44:47], v[184:187]\n"
"ds_read_b64_tr_b16 a[92:93], v28 offset:4096\n"
"v_mfma_f32_16x16x32_bf16 v[96:99], a[128:131], a[48:51], v[96:99]\n"
"buffer_load_dwordx4 v3, s[20:23], s36 offen offset:2048 lds\n"
"v_mfma_f32_16x16x32_bf16 v[96:99], a[132:135], a[52:55], v[96:99]\n"
"v_mfma_f32_16x16x32_bf16 v[104:107], a[128:131], a[56:59], v[104:107]\n"
"v_mfma_f32_16x16x32_bf16 v[104:107], a[132:135], a[60:63], v[104:107]\n"
"s_waitcnt lgkmcnt(6)\n"
"v_mfma_f32_16x16x32_bf16 v[76:79], a[136:139], a[0:3], v[76:79]\n"
"ds_read_b128 a[128:131], v19 offset:8192\n"
"s_waitcnt lgkmcnt(6)\n"
"v_mfma_f32_16x16x32_bf16 v[76:79], a[140:143], a[4:7], v[76:79]\n"
"ds_read_b128 a[132:135], v20 offset:8192\n"
"v_mfma_f32_16x16x32_bf16 v[156:159], a[136:139], a[8:11], v[156:159]\n"
"v_mfma_f32_16x16x32_bf16 v[156:159], a[140:143], a[12:15], v[156:159]\n"
"ds_read_b64_tr_b16 a[94:95], v28 offset:4608\n"
"v_mfma_f32_16x16x32_bf16 v[84:87], a[136:139], a[16:19], v[84:87]\n"
"v_mfma_f32_16x16x32_bf16 v[84:87], a[140:143], a[20:23], v[84:87]\n"
"ds_read_b64_tr_b16 a[96:97], v25 offset:8192\n"
"v_mfma_f32_16x16x32_bf16 v[92:95], a[136:139], a[24:27], v[92:95]\n"
"v_mfma_f32_16x16x32_bf16 v[92:95], a[140:143], a[28:31], v[92:95]\n"
"ds_read_b64_tr_b16 a[98:99], v25 offset:8704\n"
"v_mfma_f32_16x16x32_bf16 v[172:175], a[136:139], a[32:35], v[172:175]\n"
"buffer_load_dwordx4 v4, s[20:23], s36 offen offset:3072 lds\n"
"v_mfma_f32_16x16x32_bf16 v[172:175], a[140:143], a[36:39], v[172:175]\n"
"ds_read_b64_tr_b16 a[100:101], v25 offset:12288\n"
"v_mfma_f32_16x16x32_bf16 v[188:191], a[136:139], a[40:43], v[188:191]\n"
"s_add_u32 m0, m0, 0x1000\n"
"v_mfma_f32_16x16x32_bf16 v[188:191], a[140:143], a[44:47], v[188:191]\n"
"ds_read_b64_tr_b16 a[102:103], v25 offset:12800\n"
"v_mfma_f32_16x16x32_bf16 v[100:103], a[136:139], a[48:51], v[100:103]\n"
"v_mfma_f32_16x16x32_bf16 v[100:103], a[140:143], a[52:55], v[100:103]\n"
"v_mfma_f32_16x16x32_bf16 v[108:111], a[136:139], a[56:59], v[108:111]\n"
"v_mfma_f32_16x16x32_bf16 v[108:111], a[140:143], a[60:63], v[108:111]\n"
"s_waitcnt lgkmcnt(6)\n"
"v_mfma_f32_16x16x32_bf16 v[192:195], a[128:131], a[0:3], v[192:195]\n"
"ds_read_b128 a[136:139], v19 offset:10240\n"
"s_waitcnt lgkmcnt(6)\n"
"v_mfma_f32_16x16x32_bf16 v[192:195], a[132:135], a[4:7], v[192:195]\n"
"ds_read_b128 a[140:143], v20 offset:10240\n"
"v_mfma_f32_16x16x32_bf16 v[208:211], a[128:131], a[8:11], v[208:211]\n"
"v_mfma_f32_16x16x32_bf16 v[208:211], a[132:135], a[12:15], v[208:211]\n"
"ds_read_b64_tr_b16 a[104:105], v26 offset:8192\n"
"v_mfma_f32_16x16x32_bf16 v[112:115], a[128:131], a[16:19], v[112:115]\n"
"buffer_load_dwordx4 v5, s[20:23], s36 offen offset:0 lds\n"
"v_mfma_f32_16x16x32_bf16 v[112:115], a[132:135], a[20:23], v[112:115]\n"
"ds_read_b64_tr_b16 a[106:107], v26 offset:8704\n"
"v_mfma_f32_16x16x32_bf16 v[120:123], a[128:131], a[24:27], v[120:123]\n"
"v_mfma_f32_16x16x32_bf16 v[120:123], a[132:135], a[28:31], v[120:123]\n"
"ds_read_b64_tr_b16 a[108:109], v26 offset:12288\n"
"v_mfma_f32_16x16x32_bf16 v[224:227], a[128:131], a[32:35], v[224:227]\n"
"v_mfma_f32_16x16x32_bf16 v[224:227], a[132:135], a[36:39], v[224:227]\n"
"ds_read_b64_tr_b16 a[110:111], v26 offset:12800\n"
"v_mfma_f32_16x16x32_bf16 v[240:243], a[128:131], a[40:43], v[240:243]\n"
"v_mfma_f32_16x16x32_bf16 v[240:243], a[132:135], a[44:47], v[240:243]\n"
"ds_read_b64_tr_b16 a[112:113], v27 offset:8192\n"
"v_mfma_f32_16x16x32_bf16 v[128:131], a[128:131], a[48:51], v[128:131]\n"
"v_mfma_f32_16x16x32_bf16 v[128:131], a[132:135], a[52:55], v[128:131]\n"
"v_mfma_f32_16x16x32_bf16 v[136:139], a[128:131], a[56:59], v[136:139]\n"
"buffer_load_dwordx4 v6, s[20:23], s36 offen offset:1024 lds\n"
"v_mfma_f32_16x16x32_bf16 v[136:139], a[132:135], a[60:63], v[136:139]\n"
"s_waitcnt lgkmcnt(6)\n"
"v_mfma_f32_16x16x32_bf16 v[196:199], a[136:139], a[0:3], v[196:199]\n"
"ds_read_b128 a[128:131], v19 offset:12288\n"
"s_waitcnt lgkmcnt(6)\n"
"v_mfma_f32_16x16x32_bf16 v[196:199], a[140:143], a[4:7], v[196:199]\n"
"ds_read_b128 a[132:135], v20 offset:12288\n"
"v_mfma_f32_16x16x32_bf16 v[212:215], a[136:139], a[8:11], v[212:215]\n"
"v_mfma_f32_16x16x32_bf16 v[212:215], a[140:143], a[12:15], v[212:215]\n"
"ds_read_b64_tr_b16 a[114:115], v27 offset:8704\n"
"v_mfma_f32_16x16x32_bf16 v[116:119], a[136:139], a[16:19], v[116:119]\n"
"v_mfma_f32_16x16x32_bf16 v[116:119], a[140:143], a[20:23], v[116:119]\n"
"ds_read_b64_tr_b16 a[116:117], v27 offset:12288\n"
"v_mfma_f32_16x16x32_bf16 v[124:127], a[136:139], a[24:27], v[124:127]\n"
"v_mfma_f32_16x16x32_bf16 v[124:127], a[140:143], a[28:31], v[124:127]\n"
"ds_read_b64_tr_b16 a[118:119], v27 offset:12800\n"
"v_mfma_f32_16x16x32_bf16 v[228:231], a[136:139], a[32:35], v[228:231]\n"
"v_mfma_f32_16x16x32_bf16 v[228:231], a[140:143], a[36:39], v[228:231]\n"
"ds_read_b64_tr_b16 a[120:121], v28 offset:8192\n"
"v_mfma_f32_16x16x32_bf16 v[244:247], a[136:139], a[40:43], v[244:247]\n"
"v_mfma_f32_16x16x32_bf16 v[244:247], a[140:143], a[44:47], v[244:247]\n"
"ds_read_b64_tr_b16 a[122:123], v28 offset:8704\n"
"v_mfma_f32_16x16x32_bf16 v[132:135], a[136:139], a[48:51], v[132:135]\n"
"buffer_load_dwordx4 v7, s[20:23], s36 offen offset:2048 lds\n"
"v_mfma_f32_16x16x32_bf16 v[132:135], a[140:143], a[52:55], v[132:135]\n"
"v_mfma_f32_16x16x32_bf16 v[140:143], a[136:139], a[56:59], v[140:143]\n"
"v_mfma_f32_16x16x32_bf16 v[140:143], a[140:143], a[60:63], v[140:143]\n"
"s_waitcnt lgkmcnt(6)\n"
"v_mfma_f32_16x16x32_bf16 v[200:203], a[128:131], a[0:3], v[200:203]\n"
"ds_read_b128 a[136:139], v19 offset:14336\n"
"s_waitcnt lgkmcnt(6)\n"
"v_mfma_f32_16x16x32_bf16 v[200:203], a[132:135], a[4:7], v[200:203]\n"
"ds_read_b128 a[140:143], v20 offset:14336\n"
"v_mfma_f32_16x16x32_bf16 v[216:219], a[128:131], a[8:11], v[216:219]\n"
"s_add_u32 s41, s40, 32768\n"
"s_cmp_eq_u32 s41, 163840\n"
"v_mfma_f32_16x16x32_bf16 v[216:219], a[132:135], a[12:15], v[216:219]\n"
"ds_read_b64_tr_b16 a[124:125], v28 offset:12288\n"
"v_mfma_f32_16x16x32_bf16 a[160:163], a[128:131], a[16:19], a[160:163]\n"
"s_cselect_b32 s41, 0, s41\n"
"v_add_u32 v19, s41, v17\n"
"v_mfma_f32_16x16x32_bf16 a[160:163], a[132:135], a[20:23], a[160:163]\n"
"ds_read_b64_tr_b16 a[126:127], v28 offset:12800\n"
"v_mfma_f32_16x16x32_bf16 a[168:171], a[128:131], a[24:27], a[168:171]\n"
"v_add_u32 v20, s41, v18\n"
"s_add_u32 s42, s41, 32768\n"
"v_mfma_f32_16x16x32_bf16 a[168:171], a[132:135], a[28:31], a[168:171]\n"
"s_cmp_eq_u32 s42, 163840\n"
"s_cselect_b32 s42, 0, s42\n"
"v_mfma_f32_16x16x32_bf16 v[232:235], a[128:131], a[32:35], v[232:235]\n"
"buffer_load_dwordx4 v8, s[20:23], s36 offen offset:3072 lds\n"
"v_mfma_f32_16x16x32_bf16 v[232:235], a[132:135], a[36:39], v[232:235]\n"
"s_add_u32 s36, s36, s60\n"
"v_mfma_f32_16x16x32_bf16 v[248:251], a[128:131], a[40:43], v[248:251]\n"
"v_add_u32 v25, s42, v21\n"
"v_add_u32 v26, s42, v22\n"
"v_mfma_f32_16x16x32_bf16 v[248:251], a[132:135], a[44:47], v[248:251]\n"
"v_add_u32 v27, s42, v23\n"
"v_add_u32 v28, s42, v24\n"
"v_mfma_f32_16x16x32_bf16 a[176:179], a[128:131], a[48:51], a[176:179]\n"
"s_cmp_lt_u32 s46, 13\n"
"s_cselect_b32 s26, 0x800000, s62\n"
"v_mfma_f32_16x16x32_bf16 a[176:179], a[132:135], a[52:55], a[176:179]\n"
"s_add_u32 m0, s39, s45\n"
"s_mov_b32 s38, s40\n"
"v_mfma_f32_16x16x32_bf16 a[184:187], a[128:131], a[56:59], a[184:187]\n"
"s_mov_b32 s39, s41\n"
"s_mov_b32 s40, s42\n"
"v_mfma_f32_16x16x32_bf16 a[184:187], a[132:135], a[60:63], a[184:187]\n"
"s_waitcnt vmcnt(8) lgkmcnt(0)\n"
"s_barrier\n"
"v_mfma_f32_16x16x32_bf16 v[204:207], a[136:139], a[0:3], v[204:207]\n"
"ds_read_b128 a[128:131], v19 offset:0\n"
"v_mfma_f32_16x16x32_bf16 v[204:207], a[140:143], a[4:7], v[204:207]\n"
"ds_read_b128 a[132:135], v20 offset:0\n"
"v_mfma_f32_16x16x32_bf16 v[220:223], a[136:139], a[8:11], v[220:223]\n"
"buffer_load_dwordx4 v9, s[24:27], s37 offen offset:0 lds\n"
"v_mfma_f32_16x16x32_bf16 v[220:223], a[140:143], a[12:15], v[220:223]\n"
"v_mfma_f32_16x16x32_bf16 a[164:167], a[136:139], a[16:19], a[164:167]\n"
"v_mfma_f32_16x16x32_bf16 a[164:167], a[140:143], a[20:23], a[164:167]\n"
"v_mfma_f32_16x16x32_bf16 a[172:175], a[136:139], a[24:27], a[172:175]\n"
"buffer_load_dwordx4 v10, s[24:27], s37 offen offset:1024 lds\n"
"v_mfma_f32_16x16x32_bf16 a[172:175], a[140:143], a[28:31], a[172:175]\n"
"v_mfma_f32_16x16x32_bf16 v[236:239], a[136:139], a[32:35], v[236:239]\n"
"v_mfma_f32_16x16x32_bf16 v[236:239], a[140:143], a[36:39], v[236:239]\n"
"v_mfma_f32_16x16x32_bf16 v[252:255], a[136:139], a[40:43], v[252:255]\n"
"buffer_load_dwordx4 v11, s[24:27], s37 offen offset:2048 lds\n"
"v_mfma_f32_16x16x32_bf16 v[252:255], a[140:143], a[44:47], v[252:255]\n"
"v_mfma_f32_16x16x32_bf16 a[180:183], a[136:139], a[48:51], a[180:183]\n"
"v_mfma_f32_16x16x32_bf16 a[180:183], a[140:143], a[52:55], a[180:183]\n"
"v_mfma_f32_16x16x32_bf16 a[188:191], a[136:139], a[56:59], a[188:191]\n"
"buffer_load_dwordx4 v12, s[24:27], s37 offen offset:3072 lds\n"
"v_mfma_f32_16x16x32_bf16 a[188:191], a[140:143], a[60:63], a[188:191]\n"
"s_add_u32 m0, m0, 0x1000\n"
"s_add_u32 s46, s46, 1\n"
"s_waitcnt lgkmcnt(1)\n"
"v_mfma_f32_16x16x32_bf16 v[32:35], a[128:131], a[64:67], v[32:35]\n"
"ds_read_b128 a[136:139], v19 offset:2048\n"
"s_waitcnt lgkmcnt(1)\n"
"v_mfma_f32_16x16x32_bf16 v[32:35], a[132:135], a[68:71], v[32:35]\n"
"ds_read_b128 a[140:143], v20 offset:2048\n"
"v_mfma_f32_16x16x32_bf16 v[144:147], a[128:131], a[72:75], v[144:147]\n"
"s_cmp_lt_u32 s46, 14\n"
"s_cselect_b32 s22, 0x80000, s34\n"
"v_mfma_f32_16x16x32_bf16 v[144:147], a[132:135], a[76:79], v[144:147]\n"
"ds_read_b64_tr_b16 a[0:1], v25 offset:0\n"
"v_mfma_f32_16x16x32_bf16 v[40:43], a[128:131], a[80:83], v[40:43]\n"
"buffer_load_dwordx4 v13, s[24:27], s37 offen offset:0 lds\n"
"v_mfma_f32_16x16x32_bf16 v[40:43], a[132:135], a[84:87], v[40:43]\n"
"ds_read_b64_tr_b16 a[2:3], v25 offset:512\n"
"v_mfma_f32_16x16x32_bf16 v[48:51], a[128:131], a[88:91], v[48:51]\n"
"v_mfma_f32_16x16x32_bf16 v[48:51], a[132:135], a[92:95], v[48:51]\n"
"ds_read_b64_tr_b16 a[4:5], v25 offset:4096\n"
"v_mfma_f32_16x16x32_bf16 v[160:163], a[128:131], a[96:99], v[160:163]\n"
"buffer_load_dwordx4 v14, s[24:27], s37 offen offset:1024 lds\n"
"v_mfma_f32_16x16x32_bf16 v[160:163], a[132:135], a[100:103], v[160:163]\n"
"ds_read_b64_tr_b16 a[6:7], v25 offset:4608\n"
"v_mfma_f32_16x16x32_bf16 v[176:179], a[128:131], a[104:107], v[176:179]\n"
"v_mfma_f32_16x16x32_bf16 v[176:179], a[132:135], a[108:111], v[176:179]\n"
"ds_read_b64_tr_b16 a[8:9], v26 offset:0\n"
"v_mfma_f32_16x16x32_bf16 v[56:59], a[128:131], a[112:115], v[56:59]\n"
"buffer_load_dwordx4 v15, s[24:27], s37 offen offset:2048 lds\n"
"v_mfma_f32_16x16x32_bf16 v[56:59], a[132:135], a[116:119], v[56:59]\n"
"v_mfma_f32_16x16x32_bf16 v[64:67], a[128:131], a[120:123], v[64:67]\n"
"buffer_load_dwordx4 v16, s[24:27], s37 offen offset:3072 lds\n"
"v_mfma_f32_16x16x32_bf16 v[64:67], a[132:135], a[124:127], v[64:67]\n"
"s_add_u32 m0, s38, s45\n"
"s_add_u32 s37, s37, s61\n"
"s_waitcnt lgkmcnt(6)\n"
"v_mfma_f32_16x16x32_bf16 v[36:39], a[136:139], a[64:67], v[36:39]\n"
"ds_read_b128 a[128:131], v19 offset:4096\n"
"s_waitcnt lgkmcnt(6)\n"
"v_mfma_f32_16x16x32_bf16 v[36:39], a[140:143], a[68:71], v[36:39]\n"
"ds_read_b128 a[132:135], v20 offset:4096\n"
"v_mfma_f32_16x16x32_bf16 v[148:151], a[136:139], a[72:75], v[148:151]\n"
"v_mfma_f32_16x16x32_bf16 v[148:151], a[140:143], a[76:79], v[148:151]\n"
"ds_read_b64_tr_b16 a[10:11], v26 offset:512\n"
"v_mfma_f32_16x16x32_bf16 v[44:47], a[136:139], a[80:83], v[44:47]\n"
"v_mfma_f32_16x16x32_bf16 v[44:47], a[140:143], a[84:87], v[44:47]\n"
"ds_read_b64_tr_b16 a[12:13], v26 offset:4096\n"
"v_mfma_f32_16x16x32_bf16 v[52:55], a[136:139], a[88:91], v[52:55]\n"
"buffer_load_dwordx4 v1, s[20:23], s36 offen offset:0 lds\n"
"v_mfma_f32_16x16x32_bf16 v[52:55], a[140:143], a[92:95], v[52:55]\n"
"ds_read_b64_tr_b16 a[14:15], v26 offset:4608\n"
"v_mfma_f32_16x16x32_bf16 v[164:167], a[136:139], a[96:99], v[164:167]\n"
"v_mfma_f32_16x16x32_bf16 v[164:167], a[140:143], a[100:103], v[164:167]\n"
"ds_read_b64_tr_b16 a[16:17], v27 offset:0\n"
"v_mfma_f32_16x16x32_bf16 v[180:183], a[136:139], a[104:107], v[180:183]\n"
"v_mfma_f32_16x16x32_bf16 v[180:183], a[140:143], a[108:111], v[180:183]\n"
"ds_read_b64_tr_b16 a[18:19], v27 offset:512\n"
"v_mfma_f32_16x16x32_bf16 v[60:63], a[136:139], a[112:115], v[60:63]\n"
"v_mfma_f32_16x16x32_bf16 v[60:63], a[140:143], a[116:119], v[60:63]\n"
"v_mfma_f32_16x16x32_bf16 v[68:71], a[136:139], a[120:123], v[68:71]\n"
"buffer_load_dwordx4 v2, s[20:23], s36 offen offset:1024 lds\n"
"v_mfma_f32_16x16x32_bf16 v[68:71], a[140:143], a[124:127], v[68:71]\n"
"s_waitcnt lgkmcnt(6)\n"
"v_mfma_f32_16x16x32_bf16 v[72:75], a[128:131], a[64:67], v[72:75]\n"
"ds_read_b128 a[136:139], v19 offset:6144\n"
"s_waitcnt lgkmcnt(6)\n"
"v_mfma_f32_16x16x32_bf16 v[72:75], a[132:135], a[68:71], v[72:75]\n"
"ds_read_b128 a[140:143], v20 offset:6144\n"
"v_mfma_f32_16x16x32_bf16 v[152:155], a[128:131], a[72:75], v[152:155]\n"
"v_mfma_f32_16x16x32_bf16 v[152:155], a[132:135], a[76:79], v[152:155]\n"
"ds_read_b64_tr_b16 a[20:21], v27 offset:4096\n"
"v_mfma_f32_16x16x32_bf16 v[80:83], a[128:131], a[80:83], v[80:83]\n"
"v_mfma_f32_16x16x32_bf16 v[80:83], a[132:135], a[84:87], v[80:83]\n"
"ds_read_b64_tr_b16 a[22:23], v27 offset:4608\n"
"v_mfma_f32_16x16x32_bf16 v[88:91], a[128:131], a[88:91], v[88:91]\n"
"v_mfma_f32_16x16x32_bf16 v[88:91], a[132:135], a[92:95], v[88:91]\n"
"ds_read_b64_tr_b16 a[24:25], v28 offset:0\n"
"v_mfma_f32_16x16x32_bf16 v[168:171], a[128:131], a[96:99], v[168:171]\n"
"v_mfma_f32_16x16x32_bf16 v[168:171], a[132:135], a[100:103], v[168:171]\n"
"ds_read_b64_tr_b16 a[26:27], v28 offset:512\n"
"v_mfma_f32_16x16x32_bf16 v[184:187], a[128:131], a[104:107], v[184:187]\n"
"v_mfma_f32_16x16x32_bf16 v[184:187], a[132:135], a[108:111], v[184:187]\n"
"ds_read_b64_tr_b16 a[28:29], v28 offset:4096\n"
"v_mfma_f32_16x16x32_bf16 v[96:99], a[128:131], a[112:115], v[96:99]\n"
"buffer_load_dwordx4 v3, s[20:23], s36 offen offset:2048 lds\n"
"v_mfma_f32_16x16x32_bf16 v[96:99], a[132:135], a[116:119], v[96:99]\n"
"v_mfma_f32_16x16x32_bf16 v[104:107], a[128:131], a[120:123], v[104:107]\n"
"v_mfma_f32_16x16x32_bf16 v[104:107], a[132:135], a[124:127], v[104:107]\n"
"s_waitcnt lgkmcnt(6)\n"
"v_mfma_f32_16x16x32_bf16 v[76:79], a[136:139], a[64:67], v[76:79]\n"
"ds_read_b128 a[128:131], v19 offset:8192\n"
"s_waitcnt lgkmcnt(6)\n"
"v_mfma_f32_16x16x32_bf16 v[76:79], a[140:143], a[68:71], v[76:79]\n"
"ds_read_b128 a[132:135], v20 offset:8192\n"
"v_mfma_f32_16x16x32_bf16 v[156:159], a[136:139], a[72:75], v[156:159]\n"
"v_mfma_f32_16x16x32_bf16 v[156:159], a[140:143], a[76:79], v[156:159]\n"
"ds_read_b64_tr_b16 a[30:31], v28 offset:4608\n"
"v_mfma_f32_16x16x32_bf16 v[84:87], a[136:139], a[80:83], v[84:87]\n"
"v_mfma_f32_16x16x32_bf16 v[84:87], a[140:143], a[84:87], v[84:87]\n"
"ds_read_b64_tr_b16 a[32:33], v25 offset:8192\n"
"v_mfma_f32_16x16x32_bf16 v[92:95], a[136:139], a[88:91], v[92:95]\n"
"v_mfma_f32_16x16x32_bf16 v[92:95], a[140:143], a[92:95], v[92:95]\n"
"ds_read_b64_tr_b16 a[34:35], v25 offset:8704\n"
"v_mfma_f32_16x16x32_bf16 v[172:175], a[136:139], a[96:99], v[172:175]\n"
"buffer_load_dwordx4 v4, s[20:23], s36 offen offset:3072 lds\n"
"v_mfma_f32_16x16x32_bf16 v[172:175], a[140:143], a[100:103], v[172:175]\n"
"ds_read_b64_tr_b16 a[36:37], v25 offset:12288\n"
"v_mfma_f32_16x16x32_bf16 v[188:191], a[136:139], a[104:107], v[188:191]\n"
"s_add_u32 m0, m0, 0x1000\n"
"v_mfma_f32_16x16x32_bf16 v[188:191], a[140:143], a[108:111], v[188:191]\n"
"ds_read_b64_tr_b16 a[38:39], v25 offset:12800\n"
"v_mfma_f32_16x16x32_bf16 v[100:103], a[136:139], a[112:115], v[100:103]\n"
"v_mfma_f32_16x16x32_bf16 v[100:103], a[140:143], a[116:119], v[100:103]\n"
"v_mfma_f32_16x16x32_bf16 v[108:111], a[136:139], a[120:123], v[108:111]\n"
"v_mfma_f32_16x16x32_bf16 v[108:111], a[140:143], a[124:127], v[108:111]\n"
"s_waitcnt lgkmcnt(6)\n"
"v_mfma_f32_16x16x32_bf16 v[192:195], a[128:131], a[64:67], v[192:195]\n"
"ds_read_b128 a[136:139], v19 offset:10240\n"
"s_waitcnt lgkmcnt(6)\n"
"v_mfma_f32_16x16x32_bf16 v[192:195], a[132:135], a[68:71], v[192:195]\n"
"ds_read_b128 a[140:143], v20 offset:10240\n"
"v_mfma_f32_16x16x32_bf16 v[208:211], a[128:131], a[72:75], v[208:211]\n"
"v_mfma_f32_16x16x32_bf16 v[208:211], a[132:135], a[76:79], v[208:211]\n"
"ds_read_b64_tr_b16 a[40:41], v26 offset:8192\n"
"v_mfma_f32_16x16x32_bf16 v[112:115], a[128:131], a[80:83], v[112:115]\n"
"buffer_load_dwordx4 v5, s[20:23], s36 offen offset:0 lds\n"
"v_mfma_f32_16x16x32_bf16 v[112:115], a[132:135], a[84:87], v[112:115]\n"
"ds_read_b64_tr_b16 a[42:43], v26 offset:8704\n"
"v_mfma_f32_16x16x32_bf16 v[120:123], a[128:131], a[88:91], v[120:123]\n"
"v_mfma_f32_16x16x32_bf16 v[120:123], a[132:135], a[92:95], v[120:123]\n"
"ds_read_b64_tr_b16 a[44:45], v26 offset:12288\n"
"v_mfma_f32_16x16x32_bf16 v[224:227], a[128:131], a[96:99], v[224:227]\n"
"v_mfma_f32_16x16x32_bf16 v[224:227], a[132:135], a[100:103], v[224:227]\n"
"ds_read_b64_tr_b16 a[46:47], v26 offset:12800\n"
"v_mfma_f32_16x16x32_bf16 v[240:243], a[128:131], a[104:107], v[240:243]\n"
"v_mfma_f32_16x16x32_bf16 v[240:243], a[132:135], a[108:111], v[240:243]\n"
"ds_read_b64_tr_b16 a[48:49], v27 offset:8192\n"
"v_mfma_f32_16x16x32_bf16 v[128:131], a[128:131], a[112:115], v[128:131]\n"
"v_mfma_f32_16x16x32_bf16 v[128:131], a[132:135], a[116:119], v[128:131]\n"
"v_mfma_f32_16x16x32_bf16 v[136:139], a[128:131], a[120:123], v[136:139]\n"
"buffer_load_dwordx4 v6, s[20:23], s36 offen offset:1024 lds\n"
"v_mfma_f32_16x16x32_bf16 v[136:139], a[132:135], a[124:127], v[136:139]\n"
"s_waitcnt lgkmcnt(6)\n"
"v_mfma_f32_16x16x32_bf16 v[196:199], a[136:139], a[64:67], v[196:199]\n"
"ds_read_b128 a[128:131], v19 offset:12288\n"
"s_waitcnt lgkmcnt(6)\n"
"v_mfma_f32_16x16x32_bf16 v[196:199], a[140:143], a[68:71], v[196:199]\n"
"ds_read_b128 a[132:135], v20 offset:12288\n"
"v_mfma_f32_16x16x32_bf16 v[212:215], a[136:139], a[72:75], v[212:215]\n"
"v_mfma_f32_16x16x32_bf16 v[212:215], a[140:143], a[76:79], v[212:215]\n"
"ds_read_b64_tr_b16 a[50:51], v27 offset:8704\n"
"v_mfma_f32_16x16x32_bf16 v[116:119], a[136:139], a[80:83], v[116:119]\n"
"v_mfma_f32_16x16x32_bf16 v[116:119], a[140:143], a[84:87], v[116:119]\n"
"ds_read_b64_tr_b16 a[52:53], v27 offset:12288\n"
"v_mfma_f32_16x16x32_bf16 v[124:127], a[136:139], a[88:91], v[124:127]\n"
"v_mfma_f32_16x16x32_bf16 v[124:127], a[140:143], a[92:95], v[124:127]\n"
"ds_read_b64_tr_b16 a[54:55], v27 offset:12800\n"
"v_mfma_f32_16x16x32_bf16 v[228:231], a[136:139], a[96:99], v[228:231]\n"
"v_mfma_f32_16x16x32_bf16 v[228:231], a[140:143], a[100:103], v[228:231]\n"
"ds_read_b64_tr_b16 a[56:57], v28 offset:8192\n"
"v_mfma_f32_16x16x32_bf16 v[244:247], a[136:139], a[104:107], v[244:247]\n"
"v_mfma_f32_16x16x32_bf16 v[244:247], a[140:143], a[108:111], v[244:247]\n"
"ds_read_b64_tr_b16 a[58:59], v28 offset:8704\n"
"v_mfma_f32_16x16x32_bf16 v[132:135], a[136:139], a[112:115], v[132:135]\n"
"buffer_load_dwordx4 v7, s[20:23], s36 offen offset:2048 lds\n"
"v_mfma_f32_16x16x32_bf16 v[132:135], a[140:143], a[116:119], v[132:135]\n"
"v_mfma_f32_16x16x32_bf16 v[140:143], a[136:139], a[120:123], v[140:143]\n"
"v_mfma_f32_16x16x32_bf16 v[140:143], a[140:143], a[124:127], v[140:143]\n"
"s_waitcnt lgkmcnt(6)\n"
"v_mfma_f32_16x16x32_bf16 v[200:203], a[128:131], a[64:67], v[200:203]\n"
"ds_read_b128 a[136:139], v19 offset:14336\n"
"s_waitcnt lgkmcnt(6)\n"
"v_mfma_f32_16x16x32_bf16 v[200:203], a[132:135], a[68:71], v[200:203]\n"
"ds_read_b128 a[140:143], v20 offset:14336\n"
"v_mfma_f32_16x16x32_bf16 v[216:219], a[128:131], a[72:75], v[216:219]\n"
"s_add_u32 s41, s40, 32768\n"
"s_cmp_eq_u32 s41, 163840\n"
"v_mfma_f32_16x16x32_bf16 v[216:219], a[132:135], a[76:79], v[216:219]\n"
"ds_read_b64_tr_b16 a[60:61], v28 offset:12288\n"
"v_mfma_f32_16x16x32_bf16 a[160:163], a[128:131], a[80:83], a[160:163]\n"
"s_cselect_b32 s41, 0, s41\n"
"v_add_u32 v19, s41, v17\n"
"v_mfma_f32_16x16x32_bf16 a[160:163], a[132:135], a[84:87], a[160:163]\n"
"ds_read_b64_tr_b16 a[62:63], v28 offset:12800\n"
"v_mfma_f32_16x16x32_bf16 a[168:171], a[128:131], a[88:91], a[168:171]\n"
"v_add_u32 v20, s41, v18\n"
"s_add_u32 s42, s41, 32768\n"
"v_mfma_f32_16x16x32_bf16 a[168:171], a[132:135], a[92:95], a[168:171]\n"
"s_cmp_eq_u32 s42, 163840\n"
"s_cselect_b32 s42, 0, s42\n"
"v_mfma_f32_16x16x32_bf16 v[232:235], a[128:131], a[96:99], v[232:235]\n"
"buffer_load_dwordx4 v8, s[20:23], s36 offen offset:3072 lds\n"
"v_mfma_f32_16x16x32_bf16 v[232:235], a[132:135], a[100:103], v[232:235]\n"
"s_add_u32 s36, s36, s60\n"
"v_mfma_f32_16x16x32_bf16 v[248:251], a[128:131], a[104:107], v[248:251]\n"
"v_add_u32 v25, s42, v21\n"
"v_add_u32 v26, s42, v22\n"
"v_mfma_f32_16x16x32_bf16 v[248:251], a[132:135], a[108:111], v[248:251]\n"
"v_add_u32 v27, s42, v23\n"
"v_add_u32 v28, s42, v24\n"
"v_mfma_f32_16x16x32_bf16 a[176:179], a[128:131], a[112:115], a[176:179]\n"
"s_cmp_lt_u32 s46, 13\n"
"s_cselect_b32 s26, 0x800000, s62\n"
"v_mfma_f32_16x16x32_bf16 a[176:179], a[132:135], a[116:119], a[176:179]\n"
"s_add_u32 m0, s39, s45\n"
"s_mov_b32 s38, s40\n"
"v_mfma_f32_16x16x32_bf16 a[184:187], a[128:131], a[120:123], a[184:187]\n"
"s_mov_b32 s39, s41\n"
"s_mov_b32 s40, s42\n"
"v_mfma_f32_16x16x32_bf16 a[184:187], a[132:135], a[124:127], a[184:187]\n"
"s_waitcnt vmcnt(8) lgkmcnt(0)\n"
"s_barrier\n"
"v_mfma_f32_16x16x32_bf16 v[204:207], a[136:139], a[64:67], v[204:207]\n"
"ds_read_b128 a[128:131], v19 offset:0\n"
"v_mfma_f32_16x16x32_bf16 v[204:207], a[140:143], a[68:71], v[204:207]\n"
"ds_read_b128 a[132:135], v20 offset:0\n"
"v_mfma_f32_16x16x32_bf16 v[220:223], a[136:139], a[72:75], v[220:223]\n"
"buffer_load_dwordx4 v9, s[24:27], s37 offen offset:0 lds\n"
"v_mfma_f32_16x16x32_bf16 v[220:223], a[140:143], a[76:79], v[220:223]\n"
"v_mfma_f32_16x16x32_bf16 a[164:167], a[136:139], a[80:83], a[164:167]\n"
"v_mfma_f32_16x16x32_bf16 a[164:167], a[140:143], a[84:87], a[164:167]\n"
"v_mfma_f32_16x16x32_bf16 a[172:175], a[136:139], a[88:91], a[172:175]\n"
"buffer_load_dwordx4 v10, s[24:27], s37 offen offset:1024 lds\n"
"v_mfma_f32_16x16x32_bf16 a[172:175], a[140:143], a[92:95], a[172:175]\n"
"v_mfma_f32_16x16x32_bf16 v[236:239], a[136:139], a[96:99], v[236:239]\n"
"v_mfma_f32_16x16x32_bf16 v[236:239], a[140:143], a[100:103], v[236:239]\n"
"v_mfma_f32_16x16x32_bf16 v[252:255], a[136:139], a[104:107], v[252:255]\n"
"buffer_load_dwordx4 v11, s[24:27], s37 offen offset:2048 lds\n"
"v_mfma_f32_16x16x32_bf16 v[252:255], a[140:143], a[108:111], v[252:255]\n"
"v_mfma_f32_16x16x32_bf16 a[180:183], a[136:139], a[112:115], a[180:183]\n"
"v_mfma_f32_16x16x32_bf16 a[180:183], a[140:143], a[116:119], a[180:183]\n"
"v_mfma_f32_16x16x32_bf16 a[188:191], a[136:139], a[120:123], a[188:191]\n"
"buffer_load_dwordx4 v12, s[24:27], s37 offen offset:3072 lds\n"
"v_mfma_f32_16x16x32_bf16 a[188:191], a[140:143], a[124:127], a[188:191]\n"
"s_add_u32 m0, m0, 0x1000\n"
"s_add_u32 s46, s46, 1\n"
"s_cmp_lt_u32 s46, 14\n"
"s_cbranch_scc1 LV%=\n"
"s_cmp_eq_u32 s46, 16\n"
"s_cbranch_scc1 LMV%=\n"
"v_accvgpr_read_b32 v1, a220\n"
"v_accvgpr_read_b32 v2, a221\n"
"v_accvgpr_read_b32 v3, a222\n"
"v_accvgpr_read_b32 v4, a223\n"
"v_accvgpr_read_b32 v5, a224\n"
"v_accvgpr_read_b32 v6, a225\n"
"v_accvgpr_read_b32 v7, a226\n"
"v_accvgpr_read_b32 v8, a227\n"
"v_accvgpr_read_b32 v9, a220\n"
"v_accvgpr_read_b32 v10, a221\n"
"v_accvgpr_read_b32 v11, a222\n"
"v_accvgpr_read_b32 v12, a223\n"
"v_accvgpr_read_b32 v13, a224\n"
"v_accvgpr_read_b32 v14, a225\n"
"v_accvgpr_read_b32 v15, a226\n"
"v_accvgpr_read_b32 v16, a227\n"
"s_mov_b64 s[20:21], s[32:33]\n"
"s_mov_b64 s[24:25], s[32:33]\n"
"s_mov_b32 s62, s34\n"
"s_mov_b32 s36, 0\n"
"s_mov_b32 s37, 256\n"
"s_mov_b32 s60, 256\n"
"s_mov_b32 s61, 256\n"
"s_branch LV%=\n"
"LMV%=:\n"
"v_accvgpr_read_b32 v9, a144\n"
"v_accvgpr_read_b32 v10, a145\n"
"v_accvgpr_read_b32 v11, a146\n"
"v_accvgpr_read_b32 v12, a147\n"
"v_accvgpr_read_b32 v13, a148\n"
"v_accvgpr_read_b32 v14, a149\n"
"v_accvgpr_read_b32 v15, a150\n"
"v_accvgpr_read_b32 v20, a152\n"
"v_accvgpr_read_b32 v19, a151\n"
"v_accvgpr_read_b32 v17, a153\n"
"v_accvgpr_read_b32 v18, a154\n"
"v_xor_b32 v21, 64, v20\n"
"v_add_u32 v30, 98304, v20\n"
"v_add_u32 v31, 98304, v21\n"
"s_add_u32 m0, s85, 65536\n"
"s_nop 0\n"
"buffer_load_dwordx4 v9, s[80:83], s84 offen offset:0 lds\n"
"v_cvt_pk_bf16_f32 v32, v32, v33\n"
"v_cvt_pk_bf16_f32 v33, v34, v35\n"
"v_cvt_pk_bf16_f32 v34, v36, v37\n"
"v_cvt_pk_bf16_f32 v35, v38, v39\n"
"s_waitcnt vmcnt(63)\n"
"ds_read_b128 v[36:39], v30 offset:0\n"
"buffer_load_dwordx4 v10, s[80:83], s84 offen offset:1024 lds\n"
"v_cvt_pk_bf16_f32 v144, v144, v145\n"
"v_cvt_pk_bf16_f32 v145, v146, v147\n"
"v_cvt_pk_bf16_f32 v146, v148, v149\n"
"v_cvt_pk_bf16_f32 v147, v150, v151\n"
"ds_read_b128 v[148:151], v30 offset:2048\n"
"buffer_load_dwordx4 v11, s[80:83], s84 offen offset:2048 lds\n"
"v_cvt_pk_bf16_f32 v40, v40, v41\n"
"v_cvt_pk_bf16_f32 v41, v42, v43\n"
"v_cvt_pk_bf16_f32 v42, v44, v45\n"
"v_cvt_pk_bf16_f32 v43, v46, v47\n"
"ds_read_b128 v[44:47], v31 offset:0\n"
"buffer_load_dwordx4 v12, s[80:83], s84 offen offset:3072 lds\n"
"v_cvt_pk_bf16_f32 v152, v152, v153\n"
"v_cvt_pk_bf16_f32 v153, v154, v155\n"
"v_cvt_pk_bf16_f32 v154, v156, v157\n"
"v_cvt_pk_bf16_f32 v155, v158, v159\n"
"ds_read_b128 v[156:159], v31 offset:2048\n"
"s_add_u32 m0, s85, 69632\n"
"s_nop 0\n"
"buffer_load_dwordx4 v13, s[80:83], s84 offen offset:0 lds\n"
"v_cvt_pk_bf16_f32 v48, v48, v49\n"
"v_cvt_pk_bf16_f32 v49, v50, v51\n"
"v_cvt_pk_bf16_f32 v50, v52, v53\n"
"v_cvt_pk_bf16_f32 v51, v54, v55\n"
"ds_read_b128 v[52:55], v30 offset:4096\n"
"buffer_load_dwordx4 v14, s[80:83], s84 offen offset:1024 lds\n"
"v_cvt_pk_bf16_f32 v160, v160, v161\n"
"v_cvt_pk_bf16_f32 v161, v162, v163\n"
"v_cvt_pk_bf16_f32 v162, v164, v165\n"
"v_cvt_pk_bf16_f32 v163, v166, v167\n"
"ds_read_b128 v[164:167], v30 offset:6144\n"
"buffer_load_dwordx4 v15, s[80:83], s84 offen offset:2048 lds\n"
"s_add_u32 s84, s84, s56\n"
"v_cvt_pk_bf16_f32 v56, v56, v57\n"
"v_cvt_pk_bf16_f32 v57, v58, v59\n"
"v_cvt_pk_bf16_f32 v58, v60, v61\n"
"v_cvt_pk_bf16_f32 v59, v62, v63\n"
"ds_read_b128 v[60:63], v31 offset:4096\n"
"v_cvt_pk_bf16_f32 v168, v168, v169\n"
"v_cvt_pk_bf16_f32 v169, v170, v171\n"
"v_cvt_pk_bf16_f32 v170, v172, v173\n"
"v_cvt_pk_bf16_f32 v171, v174, v175\n"
"ds_read_b128 v[172:175], v31 offset:6144\n"
"v_cvt_pk_bf16_f32 v96, v96, v97\n"
"v_cvt_pk_bf16_f32 v97, v98, v99\n"
"v_cvt_pk_bf16_f32 v98, v100, v101\n"
"v_cvt_pk_bf16_f32 v99, v102, v103\n"
"ds_read_b128 v[100:103], v30 offset:32768\n"
"v_cvt_pk_bf16_f32 v208, v208, v209\n"
"v_cvt_pk_bf16_f32 v209, v210, v211\n"
"v_cvt_pk_bf16_f32 v210, v212, v213\n"
"v_cvt_pk_bf16_f32 v211, v214, v215\n"
"ds_read_b128 v[212:215], v30 offset:34816\n"
"v_cvt_pk_bf16_f32 v104, v104, v105\n"
"v_cvt_pk_bf16_f32 v105, v106, v107\n"
"v_cvt_pk_bf16_f32 v106, v108, v109\n"
"v_cvt_pk_bf16_f32 v107, v110, v111\n"
"ds_read_b128 v[108:111], v31 offset:32768\n"
"v_cvt_pk_bf16_f32 v216, v216, v217\n"
"v_cvt_pk_bf16_f32 v217, v218, v219\n"
"v_cvt_pk_bf16_f32 v218, v220, v221\n"
"v_cvt_pk_bf16_f32 v219, v222, v223\n"
"ds_read_b128 v[220:223], v31 offset:34816\n"
"v_cvt_pk_bf16_f32 v112, v112, v113\n"
"v_cvt_pk_bf16_f32 v113, v114, v115\n"
"v_cvt_pk_bf16_f32 v114, v116, v117\n"
"v_cvt_pk_bf16_f32 v115, v118, v119\n"
"ds_read_b128 v[116:119], v30 offset:36864\n"
"v_cvt_pk_bf16_f32 v224, v224, v225\n"
"v_cvt_pk_bf16_f32 v225, v226, v227\n"
"v_cvt_pk_bf16_f32 v226, v228, v229\n"
"v_cvt_pk_bf16_f32 v227, v230, v231\n"
"ds_read_b128 v[228:231], v30 offset:38912\n"
"v_cvt_pk_bf16_f32 v120, v120, v121\n"
"v_cvt_pk_bf16_f32 v121, v122, v123\n"
"v_cvt_pk_bf16_f32 v122, v124, v125\n"
"v_cvt_pk_bf16_f32 v123, v126, v127\n"
"ds_read_b128 v[124:127], v31 offset:36864\n"
"v_cvt_pk_bf16_f32 v232, v232, v233\n"
"v_cvt_pk_bf16_f32 v233, v234, v235\n"
"v_cvt_pk_bf16_f32 v234, v236, v237\n"
"v_cvt_pk_bf16_f32 v235, v238, v239\n"
"ds_read_b128 v[236:239], v31 offset:38912\n"
"v_cvt_pk_bf16_f32 v64, v64, v65\n"
"v_cvt_pk_bf16_f32 v65, v66, v67\n"
"v_cvt_pk_bf16_f32 v66, v68, v69\n"
"v_cvt_pk_bf16_f32 v67, v70, v71\n"
"s_waitcnt vmcnt(11)\n"
"ds_read_b128 v[68:71], v20 offset:0\n"
"v_cvt_pk_bf16_f32 v176, v176, v177\n"
"v_cvt_pk_bf16_f32 v177, v178, v179\n"
"v_cvt_pk_bf16_f32 v178, v180, v181\n"
"v_cvt_pk_bf16_f32 v179, v182, v183\n"
"ds_read_b128 v[180:183], v20 offset:2048\n"
"v_cvt_pk_bf16_f32 v72, v72, v73\n"
"v_cvt_pk_bf16_f32 v73, v74, v75\n"
"v_cvt_pk_bf16_f32 v74, v76, v77\n"
"v_cvt_pk_bf16_f32 v75, v78, v79\n"
"ds_read_b128 v[76:79], v21 offset:0\n"
"v_cvt_pk_bf16_f32 v184, v184, v185\n"
"v_cvt_pk_bf16_f32 v185, v186, v187\n"
"v_cvt_pk_bf16_f32 v186, v188, v189\n"
"v_cvt_pk_bf16_f32 v187, v190, v191\n"
"ds_read_b128 v[188:191], v21 offset:2048\n"
"v_cvt_pk_bf16_f32 v80, v80, v81\n"
"v_cvt_pk_bf16_f32 v81, v82, v83\n"
"v_cvt_pk_bf16_f32 v82, v84, v85\n"
"v_cvt_pk_bf16_f32 v83, v86, v87\n"
"ds_read_b128 v[84:87], v20 offset:4096\n"
"v_cvt_pk_bf16_f32 v192, v192, v193\n"
"v_cvt_pk_bf16_f32 v193, v194, v195\n"
"v_cvt_pk_bf16_f32 v194, v196, v197\n"
"v_cvt_pk_bf16_f32 v195, v198, v199\n"
"ds_read_b128 v[196:199], v20 offset:6144\n"
"v_cvt_pk_bf16_f32 v88, v88, v89\n"
"v_cvt_pk_bf16_f32 v89, v90, v91\n"
"v_cvt_pk_bf16_f32 v90, v92, v93\n"
"v_cvt_pk_bf16_f32 v91, v94, v95\n"
"ds_read_b128 v[92:95], v21 offset:4096\n"
"v_cvt_pk_bf16_f32 v200, v200, v201\n"
"v_cvt_pk_bf16_f32 v201, v202, v203\n"
"v_cvt_pk_bf16_f32 v202, v204, v205\n"
"v_cvt_pk_bf16_f32 v203, v206, v207\n"
"ds_read_b128 v[204:207], v21 offset:6144\n"
"v_cvt_pk_bf16_f32 v128, v128, v129\n"
"v_cvt_pk_bf16_f32 v129, v130, v131\n"
"v_cvt_pk_bf16_f32 v130, v132, v133\n"
"v_cvt_pk_bf16_f32 v131, v134, v135\n"
"v_accvgpr_read_b32 v1, a160\n"
"v_accvgpr_read_b32 v2, a161\n"
"v_accvgpr_read_b32 v3, a162\n"
"v_accvgpr_read_b32 v4, a163\n"
"v_accvgpr_read_b32 v5, a164\n"
"v_accvgpr_read_b32 v6, a165\n"
"v_accvgpr_read_b32 v7, a166\n"
"v_accvgpr_read_b32 v8, a167\n"
"v_cvt_pk_bf16_f32 v2, v1, v2\n"
"v_cvt_pk_bf16_f32 v3, v3, v4\n"
"v_cvt_pk_bf16_f32 v4, v5, v6\n"
"v_cvt_pk_bf16_f32 v5, v7, v8\n"
"s_mov_b32 s43, 36864\n"
"buffer_store_dwordx4 v[2:5], v19, s[76:79], s43 offen offset:1024 sc0 sc1\n"
"s_nop 0\n"
"v_accvgpr_read_b32 v1, a168\n"
"v_accvgpr_read_b32 v2, a169\n"
"v_accvgpr_read_b32 v3, a170\n"
"v_accvgpr_read_b32 v4, a171\n"
"v_accvgpr_read_b32 v5, a172\n"
"v_accvgpr_read_b32 v6, a173\n"
"v_accvgpr_read_b32 v7, a174\n"
"v_accvgpr_read_b32 v8, a175\n"
"v_cvt_pk_bf16_f32 v2, v1, v2\n"
"v_cvt_pk_bf16_f32 v3, v3, v4\n"
"v_cvt_pk_bf16_f32 v4, v5, v6\n"
"v_cvt_pk_bf16_f32 v5, v7, v8\n"
"s_mov_b32 s43, 36864\n"
"buffer_store_dwordx4 v[2:5], v19, s[76:79], s43 offen offset:3072 sc0 sc1\n"
"s_nop 0\n"
"v_accvgpr_read_b32 v1, a176\n"
"v_accvgpr_read_b32 v2, a177\n"
"v_accvgpr_read_b32 v3, a178\n"
"v_accvgpr_read_b32 v4, a179\n"
"v_accvgpr_read_b32 v5, a180\n"
"v_accvgpr_read_b32 v6, a181\n"
"v_accvgpr_read_b32 v7, a182\n"
"v_accvgpr_read_b32 v8, a183\n"
"v_cvt_pk_bf16_f32 v2, v1, v2\n"
"v_cvt_pk_bf16_f32 v3, v3, v4\n"
"v_cvt_pk_bf16_f32 v4, v5, v6\n"
"v_cvt_pk_bf16_f32 v5, v7, v8\n"
"s_mov_b32 s43, 45056\n"
"buffer_store_dwordx4 v[2:5], v19, s[76:79], s43 offen offset:1024 sc0 sc1\n"
"s_nop 0\n"
"v_accvgpr_read_b32 v1, a184\n"
"v_accvgpr_read_b32 v2, a185\n"
"v_accvgpr_read_b32 v3, a186\n"
"v_accvgpr_read_b32 v4, a187\n"
"v_accvgpr_read_b32 v5, a188\n"
"v_accvgpr_read_b32 v6, a189\n"
"v_accvgpr_read_b32 v7, a190\n"
"v_accvgpr_read_b32 v8, a191\n"
"v_cvt_pk_bf16_f32 v2, v1, v2\n"
"v_cvt_pk_bf16_f32 v3, v3, v4\n"
"v_cvt_pk_bf16_f32 v4, v5, v6\n"
"v_cvt_pk_bf16_f32 v5, v7, v8\n"
"s_mov_b32 s43, 45056\n"
"buffer_store_dwordx4 v[2:5], v19, s[76:79], s43 offen offset:3072 sc0 sc1\n"
"s_nop 0\n"
"s_waitcnt vmcnt(11)\n"
"ds_read_b128 v[132:135], v20 offset:32768\n"
"v_cvt_pk_bf16_f32 v240, v240, v241\n"
"v_cvt_pk_bf16_f32 v241, v242, v243\n"
"v_cvt_pk_bf16_f32 v242, v244, v245\n"
"v_cvt_pk_bf16_f32 v243, v246, v247\n"
"ds_read_b128 v[244:247], v20 offset:34816\n"
"v_cvt_pk_bf16_f32 v136, v136, v137\n"
"v_cvt_pk_bf16_f32 v137, v138, v139\n"
"v_cvt_pk_bf16_f32 v138, v140, v141\n"
"v_cvt_pk_bf16_f32 v139, v142, v143\n"
"ds_read_b128 v[140:143], v21 offset:32768\n"
"v_cvt_pk_bf16_f32 v248, v248, v249\n"
"v_cvt_pk_bf16_f32 v249, v250, v251\n"
"v_cvt_pk_bf16_f32 v250, v252, v253\n"
"v_cvt_pk_bf16_f32 v251, v254, v255\n"
"ds_read_b128 v[252:255], v21 offset:34816\n"
"v_accvgpr_read_b32 v21, a192\n"
"v_accvgpr_read_b32 v22, a193\n"
"v_accvgpr_read_b32 v23, a194\n"
"v_accvgpr_read_b32 v24, a195\n"
"v_accvgpr_read_b32 v25, a192\n"
"v_accvgpr_read_b32 v26, a193\n"
"v_accvgpr_read_b32 v27, a194\n"
"v_accvgpr_read_b32 v28, a195\n"
"v_add_u32 v25, 65536, v25\n"
"v_add_u32 v26, 65536, v26\n"
"v_add_u32 v27, 65536, v27\n"
"v_add_u32 v28, 65536, v28\n"
"buffer_store_dwordx4 v[32:35], v19, s[76:79], 0 offen offset:0 sc0 sc1\n"
"s_mov_b32 s43, 4096\n"
"buffer_store_dwordx4 v[40:43], v19, s[76:79], s43 offen offset:0 sc0 sc1\n"
"s_waitcnt lgkmcnt(15)\n"
"v_mfma_f32_16x16x32_bf16 a[200:203], a[196:199], v[36:39], 0\n"
"v_mfma_f32_16x16x32_bf16 a[208:211], v[36:39], v[36:39], 0\n"
"v_mfma_f32_16x16x32_bf16 a[204:207], a[196:199], v[148:151], 0\n"
"v_mfma_f32_16x16x32_bf16 a[212:215], v[148:151], v[148:151], 0\n"
"v_mfma_f32_16x16x32_bf16 a[200:203], a[196:199], v[44:47], a[200:203]\n"
"v_mfma_f32_16x16x32_bf16 a[208:211], v[44:47], v[44:47], a[208:211]\n"
"v_mfma_f32_16x16x32_bf16 a[204:207], a[196:199], v[156:159], a[204:207]\n"
"v_mfma_f32_16x16x32_bf16 a[212:215], v[156:159], v[156:159], a[212:215]\n"
"v_mfma_f32_16x16x32_bf16 a[200:203], a[196:199], v[52:55], a[200:203]\n"
"v_mfma_f32_16x16x32_bf16 a[208:211], v[52:55], v[52:55], a[208:211]\n"
"v_mfma_f32_16x16x32_bf16 a[204:207], a[196:199], v[164:167], a[204:207]\n"
"v_mfma_f32_16x16x32_bf16 a[212:215], v[164:167], v[164:167], a[212:215]\n"
"v_mfma_f32_16x16x32_bf16 a[200:203], a[196:199], v[60:63], a[200:203]\n"
"v_mfma_f32_16x16x32_bf16 a[208:211], v[60:63], v[60:63], a[208:211]\n"
"v_mfma_f32_16x16x32_bf16 a[204:207], a[196:199], v[172:175], a[204:207]\n"
"v_mfma_f32_16x16x32_bf16 a[212:215], v[172:175], v[172:175], a[212:215]\n"
"v_mfma_f32_16x16x32_bf16 a[200:203], a[196:199], v[100:103], a[200:203]\n"
"v_mfma_f32_16x16x32_bf16 a[208:211], v[100:103], v[100:103], a[208:211]\n"
"v_mfma_f32_16x16x32_bf16 a[204:207], a[196:199], v[212:215], a[204:207]\n"
"v_mfma_f32_16x16x32_bf16 a[212:215], v[212:215], v[212:215], a[212:215]\n"
"v_mfma_f32_16x16x32_bf16 a[200:203], a[196:199], v[108:111], a[200:203]\n"
"v_mfma_f32_16x16x32_bf16 a[208:211], v[108:111], v[108:111], a[208:211]\n"
"v_mfma_f32_16x16x32_bf16 a[204:207], a[196:199], v[220:223], a[204:207]\n"
"v_mfma_f32_16x16x32_bf16 a[212:215], v[220:223], v[220:223], a[212:215]\n"
"v_mfma_f32_16x16x32_bf16 a[200:203], a[196:199], v[116:119], a[200:203]\n"
"v_mfma_f32_16x16x32_bf16 a[208:211], v[116:119], v[116:119], a[208:211]\n"
"s_waitcnt lgkmcnt(14)\n"
"v_mfma_f32_16x16x32_bf16 a[204:207], a[196:199], v[228:231], a[204:207]\n"
"v_mfma_f32_16x16x32_bf16 a[212:215], v[228:231], v[228:231], a[212:215]\n"
"s_waitcnt lgkmcnt(13)\n"
"v_mfma_f32_16x16x32_bf16 a[200:203], a[196:199], v[124:127], a[200:203]\n"
"v_mfma_f32_16x16x32_bf16 a[208:211], v[124:127], v[124:127], a[208:211]\n"
"s_waitcnt lgkmcnt(12)\n"
"v_mfma_f32_16x16x32_bf16 a[204:207], a[196:199], v[236:239], a[204:207]\n"
"v_mfma_f32_16x16x32_bf16 a[212:215], v[236:239], v[236:239], a[212:215]\n"
"s_waitcnt lgkmcnt(11)\n"
"v_mfma_f32_16x16x32_bf16 a[200:203], a[196:199], v[68:71], a[200:203]\n"
"v_mfma_f32_16x16x32_bf16 a[208:211], v[68:71], v[68:71], a[208:211]\n"
"s_waitcnt lgkmcnt(10)\n"
"v_mfma_f32_16x16x32_bf16 a[204:207], a[196:199], v[180:183], a[204:207]\n"
"v_mfma_f32_16x16x32_bf16 a[212:215], v[180:183], v[180:183], a[212:215]\n"
"s_waitcnt lgkmcnt(9)\n"
"v_mfma_f32_16x16x32_bf16 a[200:203], a[196:199], v[76:79], a[200:203]\n"
"v_mfma_f32_16x16x32_bf16 a[208:211], v[76:79], v[76:79], a[208:211]\n"
"s_waitcnt lgkmcnt(8)\n"
"v_mfma_f32_16x16x32_bf16 a[204:207], a[196:199], v[188:191], a[204:207]\n"
"v_mfma_f32_16x16x32_bf16 a[212:215], v[188:191], v[188:191], a[212:215]\n"
"s_waitcnt lgkmcnt(7)\n"
"v_mfma_f32_16x16x32_bf16 a[200:203], a[196:199], v[84:87], a[200:203]\n"
"v_mfma_f32_16x16x32_bf16 a[208:211], v[84:87], v[84:87], a[208:211]\n"
"s_waitcnt lgkmcnt(6)\n"
"v_mfma_f32_16x16x32_bf16 a[204:207], a[196:199], v[196:199], a[204:207]\n"
"v_mfma_f32_16x16x32_bf16 a[212:215], v[196:199], v[196:199], a[212:215]\n"
"s_waitcnt lgkmcnt(5)\n"
"v_mfma_f32_16x16x32_bf16 a[200:203], a[196:199], v[92:95], a[200:203]\n"
"v_mfma_f32_16x16x32_bf16 a[208:211], v[92:95], v[92:95], a[208:211]\n"
"s_waitcnt lgkmcnt(4)\n"
"v_mfma_f32_16x16x32_bf16 a[204:207], a[196:199], v[204:207], a[204:207]\n"
"v_mfma_f32_16x16x32_bf16 a[212:215], v[204:207], v[204:207], a[212:215]\n"
"s_waitcnt lgkmcnt(3)\n"
"v_mfma_f32_16x16x32_bf16 a[200:203], a[196:199], v[132:135], a[200:203]\n"
"v_mfma_f32_16x16x32_bf16 a[208:211], v[132:135], v[132:135], a[208:211]\n"
"s_waitcnt lgkmcnt(2)\n"
"v_mfma_f32_16x16x32_bf16 a[204:207], a[196:199], v[244:247], a[204:207]\n"
"v_mfma_f32_16x16x32_bf16 a[212:215], v[244:247], v[244:247], a[212:215]\n"
"s_waitcnt lgkmcnt(1)\n"
"v_mfma_f32_16x16x32_bf16 a[200:203], a[196:199], v[140:143], a[200:203]\n"
"v_mfma_f32_16x16x32_bf16 a[208:211], v[140:143], v[140:143], a[208:211]\n"
"s_waitcnt lgkmcnt(0)\n"
"v_mfma_f32_16x16x32_bf16 a[204:207], a[196:199], v[252:255], a[204:207]\n"
"v_mfma_f32_16x16x32_bf16 a[212:215], v[252:255], v[252:255], a[212:215]\n"
"s_waitcnt lgkmcnt(0)\n"
"s_barrier\n"
"v_and_b32 v30, 15, v29\n"
"v_lshrrev_b32 v31, 2, v30\n"
"v_lshl_add_u32 v31, v31, 4, v30\n"
"v_lshlrev_b32 v31, 2, v31\n"
"v_and_b32 v30, 3, v29\n"
"s_nop 3\n"
"v_accvgpr_read_b32 v1, a208\n"
"v_accvgpr_read_b32 v2, a209\n"
"v_accvgpr_read_b32 v3, a210\n"
"v_accvgpr_read_b32 v4, a211\n"
"v_accvgpr_read_b32 v5, a212\n"
"v_accvgpr_read_b32 v6, a213\n"
"v_accvgpr_read_b32 v7, a214\n"
"v_accvgpr_read_b32 v8, a215\n"
"v_accvgpr_read_b32 v40, a200\n"
"v_accvgpr_read_b32 v42, a204\n"
"v_cmp_eq_u32 vcc, 1, v30\n"
"s_nop 1\n"
"v_cndmask_b32 v1, v1, v2, vcc\n"
"v_cndmask_b32 v5, v5, v6, vcc\n"
"v_cmp_eq_u32 vcc, 2, v30\n"
"s_nop 1\n"
"v_cndmask_b32 v1, v1, v3, vcc\n"
"v_cndmask_b32 v5, v5, v7, vcc\n"
"v_cmp_eq_u32 vcc, 3, v30\n"
"s_nop 1\n"
"v_cndmask_b32 v1, v1, v4, vcc\n"
"v_cndmask_b32 v5, v5, v8, vcc\n"
"ds_bpermute_b32 v2, v31, v1\n"
"ds_bpermute_b32 v6, v31, v5\n"
"s_cmp_eq_u32 s54, 1\n"
"s_mov_b32 s43, 0x3db8aa3b\n"
"s_cselect_b32 s43, 0xbfb8aa3b, s43\n"
"v_mul_f32 v40, 0x3b124925, v40\n"
"s_waitcnt lgkmcnt(1)\n"
"v_mul_f32 v2, 0x3b124925, v2\n"
"v_fma_f32 v2, -v40, v40, v2\n"
"v_max_f32 v2, 0, v2\n"
"v_add_f32 v2, 0x3727c5ac, v2\n"
"v_rsq_f32 v2, v2\n"
"s_nop 0\n"
"v_mul_f32 v32, s43, v2\n"
"v_mul_f32 v40, v40, -v32\n"
"v_mul_f32 v42, 0x3b124925, v42\n"
"s_waitcnt lgkmcnt(0)\n"
"v_mul_f32 v6, 0x3b124925, v6\n"
"v_fma_f32 v6, -v42, v42, v6\n"
"v_max_f32 v6, 0, v6\n"
"v_add_f32 v6, 0x3727c5ac, v6\n"
"v_rsq_f32 v6, v6\n"
"s_nop 0\n"
"v_mul_f32 v34, s43, v6\n"
"v_mul_f32 v42, v42, -v34\n"
"v_lshlrev_b32 v2, 16, v36\n"
"v_and_b32 v3, 0xffff0000, v36\n"
"s_add_u32 m0, s85, 98304\n"
"s_nop 0\n"
"buffer_load_dwordx4 v9, s[80:83], s84 offen offset:0 lds\n"
"v_lshlrev_b32 v4, 16, v37\n"
"v_and_b32 v5, 0xffff0000, v37\n"
"v_pk_fma_f32 v[2:3], v[2:3], v[32:33], v[40:41] op_sel_hi:[1,0,0]\n"
"v_lshlrev_b32 v6, 16, v38\n"
"v_and_b32 v7, 0xffff0000, v38\n"
"v_pk_fma_f32 v[4:5], v[4:5], v[32:33], v[40:41] op_sel_hi:[1,0,0]\n"
"v_cvt_pk_bf16_f32 v36, v2, v3\n"
"v_lshlrev_b32 v2, 16, v39\n"
"v_and_b32 v3, 0xffff0000, v39\n"
"v_pk_fma_f32 v[6:7], v[6:7], v[32:33], v[40:41] op_sel_hi:[1,0,0]\n"
"v_cvt_pk_bf16_f32 v37, v4, v5\n"
"v_lshlrev_b32 v4, 16, v148\n"
"v_and_b32 v5, 0xffff0000, v148\n"
"v_pk_fma_f32 v[2:3], v[2:3], v[32:33], v[40:41] op_sel_hi:[1,0,0]\n"
"v_cvt_pk_bf16_f32 v38, v6, v7\n"
"buffer_load_dwordx4 v10, s[80:83], s84 offen offset:1024 lds\n"
"v_lshlrev_b32 v6, 16, v149\n"
"v_and_b32 v7, 0xffff0000, v149\n"
"v_pk_fma_f32 v[4:5], v[4:5], v[34:35], v[42:43] op_sel_hi:[1,0,0]\n"
"v_cvt_pk_bf16_f32 v39, v2, v3\n"
"v_lshlrev_b32 v2, 16, v150\n"
"v_and_b32 v3, 0xffff0000, v150\n"
"v_pk_fma_f32 v[6:7], v[6:7], v[34:35], v[42:43] op_sel_hi:[1,0,0]\n"
"v_cvt_pk_bf16_f32 v148, v4, v5\n"
"v_lshlrev_b32 v4, 16, v151\n"
"v_and_b32 v5, 0xffff0000, v151\n"
"v_pk_fma_f32 v[2:3], v[2:3], v[34:35], v[42:43] op_sel_hi:[1,0,0]\n"
"v_cvt_pk_bf16_f32 v149, v6, v7\n"
"v_lshlrev_b32 v6, 16, v44\n"
"v_and_b32 v7, 0xffff0000, v44\n"
"v_pk_fma_f32 v[4:5], v[4:5], v[34:35], v[42:43] op_sel_hi:[1,0,0]\n"
"v_cvt_pk_bf16_f32 v150, v2, v3\n"
"buffer_load_dwordx4 v11, s[80:83], s84 offen offset:2048 lds\n"
"v_lshlrev_b32 v2, 16, v45\n"
"v_and_b32 v3, 0xffff0000, v45\n"
"v_pk_fma_f32 v[6:7], v[6:7], v[32:33], v[40:41] op_sel_hi:[1,0,0]\n"
"v_cvt_pk_bf16_f32 v151, v4, v5\n"
"v_lshlrev_b32 v4, 16, v46\n"
"v_and_b32 v5, 0xffff0000, v46\n"
"v_pk_fma_f32 v[2:3], v[2:3], v[32:33], v[40:41] op_sel_hi:[1,0,0]\n"
"v_cvt_pk_bf16_f32 v44, v6, v7\n"
"v_lshlrev_b32 v6, 16, v47\n"
"v_and_b32 v7, 0xffff0000, v47\n"
"v_pk_fma_f32 v[4:5], v[4:5], v[32:33], v[40:41] op_sel_hi:[1,0,0]\n"
"v_cvt_pk_bf16_f32 v45, v2, v3\n"
"v_lshlrev_b32 v2, 16, v156\n"
"v_and_b32 v3, 0xffff0000, v156\n"
"v_pk_fma_f32 v[6:7], v[6:7], v[32:33], v[40:41] op_sel_hi:[1,0,0]\n"
"v_cvt_pk_bf16_f32 v46, v4, v5\n"
"buffer_load_dwordx4 v12, s[80:83], s84 offen offset:3072 lds\n"
"v_lshlrev_b32 v4, 16, v157\n"
"v_and_b32 v5, 0xffff0000, v157\n"
"v_pk_fma_f32 v[2:3], v[2:3], v[34:35], v[42:43] op_sel_hi:[1,0,0]\n"
"v_cvt_pk_bf16_f32 v47, v6, v7\n"
"v_lshlrev_b32 v6, 16, v158\n"
"v_and_b32 v7, 0xffff0000, v158\n"
"v_pk_fma_f32 v[4:5], v[4:5], v[34:35], v[42:43] op_sel_hi:[1,0,0]\n"
"v_cvt_pk_bf16_f32 v156, v2, v3\n"
"v_lshlrev_b32 v2, 16, v159\n"
"v_and_b32 v3, 0xffff0000, v159\n"
"v_pk_fma_f32 v[6:7], v[6:7], v[34:35], v[42:43] op_sel_hi:[1,0,0]\n"
"v_cvt_pk_bf16_f32 v157, v4, v5\n"
"v_lshlrev_b32 v4, 16, v52\n"
"v_and_b32 v5, 0xffff0000, v52\n"
"v_pk_fma_f32 v[2:3], v[2:3], v[34:35], v[42:43] op_sel_hi:[1,0,0]\n"
"v_cvt_pk_bf16_f32 v158, v6, v7\n"
"s_add_u32 m0, s85, 102400\n"
"s_nop 0\n"
"buffer_load_dwordx4 v13, s[80:83], s84 offen offset:0 lds\n"
"v_lshlrev_b32 v6, 16, v53\n"
"v_and_b32 v7, 0xffff0000, v53\n"
"v_pk_fma_f32 v[4:5], v[4:5], v[32:33], v[40:41] op_sel_hi:[1,0,0]\n"
"v_cvt_pk_bf16_f32 v159, v2, v3\n"
"v_lshlrev_b32 v2, 16, v54\n"
"v_and_b32 v3, 0xffff0000, v54\n"
"v_pk_fma_f32 v[6:7], v[6:7], v[32:33], v[40:41] op_sel_hi:[1,0,0]\n"
"v_cvt_pk_bf16_f32 v52, v4, v5\n"
"v_lshlrev_b32 v4, 16, v55\n"
"v_and_b32 v5, 0xffff0000, v55\n"
"v_pk_fma_f32 v[2:3], v[2:3], v[32:33], v[40:41] op_sel_hi:[1,0,0]\n"
"v_cvt_pk_bf16_f32 v53, v6, v7\n"
"v_lshlrev_b32 v6, 16, v164\n"
"v_and_b32 v7, 0xffff0000, v164\n"
"v_pk_fma_f32 v[4:5], v[4:5], v[32:33], v[40:41] op_sel_hi:[1,0,0]\n"
"v_cvt_pk_bf16_f32 v54, v2, v3\n"
"buffer_load_dwordx4 v14, s[80:83], s84 offen offset:1024 lds\n"
"v_lshlrev_b32 v2, 16, v165\n"
"v_and_b32 v3, 0xffff0000, v165\n"
"v_pk_fma_f32 v[6:7], v[6:7], v[34:35], v[42:43] op_sel_hi:[1,0,0]\n"
"v_cvt_pk_bf16_f32 v55, v4, v5\n"
"v_lshlrev_b32 v4, 16, v166\n"
"v_and_b32 v5, 0xffff0000, v166\n"
"v_pk_fma_f32 v[2:3], v[2:3], v[34:35], v[42:43] op_sel_hi:[1,0,0]\n"
"v_cvt_pk_bf16_f32 v164, v6, v7\n"
"v_lshlrev_b32 v6, 16, v167\n"
"v_and_b32 v7, 0xffff0000, v167\n"
"v_pk_fma_f32 v[4:5], v[4:5], v[34:35], v[42:43] op_sel_hi:[1,0,0]\n"
"v_cvt_pk_bf16_f32 v165, v2, v3\n"
"v_lshlrev_b32 v2, 16, v60\n"
"v_and_b32 v3, 0xffff0000, v60\n"
"v_pk_fma_f32 v[6:7], v[6:7], v[34:35], v[42:43] op_sel_hi:[1,0,0]\n"
"v_cvt_pk_bf16_f32 v166, v4, v5\n"
"s_mov_b32 s43, 4096\n"
"buffer_store_dwordx4 v[48:51], v19, s[76:79], s43 offen offset:2048 sc0 sc1\n"
"v_lshlrev_b32 v4, 16, v61\n"
"v_and_b32 v5, 0xffff0000, v61\n"
"v_pk_fma_f32 v[2:3], v[2:3], v[32:33], v[40:41] op_sel_hi:[1,0,0]\n"
"v_cvt_pk_bf16_f32 v167, v6, v7\n"
"v_lshlrev_b32 v6, 16, v62\n"
"v_and_b32 v7, 0xffff0000, v62\n"
"v_pk_fma_f32 v[4:5], v[4:5], v[32:33], v[40:41] op_sel_hi:[1,0,0]\n"
"v_cvt_pk_bf16_f32 v60, v2, v3\n"
"v_lshlrev_b32 v2, 16, v63\n"
"v_and_b32 v3, 0xffff0000, v63\n"
"v_pk_fma_f32 v[6:7], v[6:7], v[32:33], v[40:41] op_sel_hi:[1,0,0]\n"
"v_cvt_pk_bf16_f32 v61, v4, v5\n"
"v_lshlrev_b32 v4, 16, v172\n"
"v_and_b32 v5, 0xffff0000, v172\n"
"v_pk_fma_f32 v[2:3], v[2:3], v[32:33], v[40:41] op_sel_hi:[1,0,0]\n"
"v_cvt_pk_bf16_f32 v62, v6, v7\n"
"buffer_load_dwordx4 v15, s[80:83], s84 offen offset:2048 lds\n"
"s_add_u32 s84, s84, s87\n"
"v_lshlrev_b32 v6, 16, v173\n"
"v_and_b32 v7, 0xffff0000, v173\n"
"v_pk_fma_f32 v[4:5], v[4:5], v[34:35], v[42:43] op_sel_hi:[1,0,0]\n"
"v_cvt_pk_bf16_f32 v63, v2, v3\n"
"v_lshlrev_b32 v2, 16, v174\n"
"v_and_b32 v3, 0xffff0000, v174\n"
"v_pk_fma_f32 v[6:7], v[6:7], v[34:35], v[42:43] op_sel_hi:[1,0,0]\n"
"v_cvt_pk_bf16_f32 v172, v4, v5\n"
"v_lshlrev_b32 v4, 16, v175\n"
"v_and_b32 v5, 0xffff0000, v175\n"
"v_pk_fma_f32 v[2:3], v[2:3], v[34:35], v[42:43] op_sel_hi:[1,0,0]\n"
"v_cvt_pk_bf16_f32 v173, v6, v7\n"
"v_lshlrev_b32 v6, 16, v68\n"
"v_and_b32 v7, 0xffff0000, v68\n"
"v_pk_fma_f32 v[4:5], v[4:5], v[34:35], v[42:43] op_sel_hi:[1,0,0]\n"
"v_cvt_pk_bf16_f32 v174, v2, v3\n"
"s_mov_b32 s43, 12288\n"
"buffer_store_dwordx4 v[56:59], v19, s[76:79], s43 offen offset:0 sc0 sc1\n"
"v_lshlrev_b32 v2, 16, v69\n"
"v_and_b32 v3, 0xffff0000, v69\n"
"v_pk_fma_f32 v[6:7], v[6:7], v[32:33], v[40:41] op_sel_hi:[1,0,0]\n"
"v_cvt_pk_bf16_f32 v175, v4, v5\n"
"v_lshlrev_b32 v4, 16, v70\n"
"v_and_b32 v5, 0xffff0000, v70\n"
"v_pk_fma_f32 v[2:3], v[2:3], v[32:33], v[40:41] op_sel_hi:[1,0,0]\n"
"v_cvt_pk_bf16_f32 v68, v6, v7\n"
"v_lshlrev_b32 v6, 16, v71\n"
"v_and_b32 v7, 0xffff0000, v71\n"
"v_pk_fma_f32 v[4:5], v[4:5], v[32:33], v[40:41] op_sel_hi:[1,0,0]\n"
"v_cvt_pk_bf16_f32 v69, v2, v3\n"
"v_lshlrev_b32 v2, 16, v180\n"
"v_and_b32 v3, 0xffff0000, v180\n"
"v_pk_fma_f32 v[6:7], v[6:7], v[32:33], v[40:41] op_sel_hi:[1,0,0]\n"
"v_cvt_pk_bf16_f32 v70, v4, v5\n"
"s_add_u32 m0, s85, 131072\n"
"s_nop 0\n"
"buffer_load_dwordx4 v9, s[80:83], s84 offen offset:0 lds\n"
"v_lshlrev_b32 v4, 16, v181\n"
"v_and_b32 v5, 0xffff0000, v181\n"
"v_pk_fma_f32 v[2:3], v[2:3], v[34:35], v[42:43] op_sel_hi:[1,0,0]\n"
"v_cvt_pk_bf16_f32 v71, v6, v7\n"
"v_lshlrev_b32 v6, 16, v182\n"
"v_and_b32 v7, 0xffff0000, v182\n"
"v_pk_fma_f32 v[4:5], v[4:5], v[34:35], v[42:43] op_sel_hi:[1,0,0]\n"
"v_cvt_pk_bf16_f32 v180, v2, v3\n"
"v_lshlrev_b32 v2, 16, v183\n"
"v_and_b32 v3, 0xffff0000, v183\n"
"v_pk_fma_f32 v[6:7], v[6:7], v[34:35], v[42:43] op_sel_hi:[1,0,0]\n"
"v_cvt_pk_bf16_f32 v181, v4, v5\n"
"v_lshlrev_b32 v4, 16, v76\n"
"v_and_b32 v5, 0xffff0000, v76\n"
"v_pk_fma_f32 v[2:3], v[2:3], v[34:35], v[42:43] op_sel_hi:[1,0,0]\n"
"v_cvt_pk_bf16_f32 v182, v6, v7\n"
"s_mov_b32 s43, 12288\n"
"buffer_store_dwordx4 v[64:67], v19, s[76:79], s43 offen offset:2048 sc0 sc1\n"
"v_lshlrev_b32 v6, 16, v77\n"
"v_and_b32 v7, 0xffff0000, v77\n"
"v_pk_fma_f32 v[4:5], v[4:5], v[32:33], v[40:41] op_sel_hi:[1,0,0]\n"
"v_cvt_pk_bf16_f32 v183, v2, v3\n"
"v_lshlrev_b32 v2, 16, v78\n"
"v_and_b32 v3, 0xffff0000, v78\n"
"v_pk_fma_f32 v[6:7], v[6:7], v[32:33], v[40:41] op_sel_hi:[1,0,0]\n"
"v_cvt_pk_bf16_f32 v76, v4, v5\n"
"v_lshlrev_b32 v4, 16, v79\n"
"v_and_b32 v5, 0xffff0000, v79\n"
"v_pk_fma_f32 v[2:3], v[2:3], v[32:33], v[40:41] op_sel_hi:[1,0,0]\n"
"v_cvt_pk_bf16_f32 v77, v6, v7\n"
"v_lshlrev_b32 v6, 16, v188\n"
"v_and_b32 v7, 0xffff0000, v188\n"
"v_pk_fma_f32 v[4:5], v[4:5], v[32:33], v[40:41] op_sel_hi:[1,0,0]\n"
"v_cvt_pk_bf16_f32 v78, v2, v3\n"
"buffer_load_dwordx4 v10, s[80:83], s84 offen offset:1024 lds\n"
"v_lshlrev_b32 v2, 16, v189\n"
"v_and_b32 v3, 0xffff0000, v189\n"
"v_pk_fma_f32 v[6:7], v[6:7], v[34:35], v[42:43] op_sel_hi:[1,0,0]\n"
"v_cvt_pk_bf16_f32 v79, v4, v5\n"
"v_lshlrev_b32 v4, 16, v190\n"
"v_and_b32 v5, 0xffff0000, v190\n"
"v_pk_fma_f32 v[2:3], v[2:3], v[34:35], v[42:43] op_sel_hi:[1,0,0]\n"
"v_cvt_pk_bf16_f32 v188, v6, v7\n"
"v_lshlrev_b32 v6, 16, v191\n"
"v_and_b32 v7, 0xffff0000, v191\n"
"v_pk_fma_f32 v[4:5], v[4:5], v[34:35], v[42:43] op_sel_hi:[1,0,0]\n"
"v_cvt_pk_bf16_f32 v189, v2, v3\n"
"v_lshlrev_b32 v2, 16, v84\n"
"v_and_b32 v3, 0xffff0000, v84\n"
"v_pk_fma_f32 v[6:7], v[6:7], v[34:35], v[42:43] op_sel_hi:[1,0,0]\n"
"v_cvt_pk_bf16_f32 v190, v4, v5\n"
"buffer_store_dwordx4 v[72:75], v19, s[76:79], 0 offen offset:1024 sc0 sc1\n"
"v_lshlrev_b32 v4, 16, v85\n"
"v_and_b32 v5, 0xffff0000, v85\n"
"v_pk_fma_f32 v[2:3], v[2:3], v[32:33], v[40:41] op_sel_hi:[1,0,0]\n"
"v_cvt_pk_bf16_f32 v191, v6, v7\n"
"v_lshlrev_b32 v6, 16, v86\n"
"v_and_b32 v7, 0xffff0000, v86\n"
"v_pk_fma_f32 v[4:5], v[4:5], v[32:33], v[40:41] op_sel_hi:[1,0,0]\n"
"v_cvt_pk_bf16_f32 v84, v2, v3\n"
"v_lshlrev_b32 v2, 16, v87\n"
"v_and_b32 v3, 0xffff0000, v87\n"
"v_pk_fma_f32 v[6:7], v[6:7], v[32:33], v[40:41] op_sel_hi:[1,0,0]\n"
"v_cvt_pk_bf16_f32 v85, v4, v5\n"
"v_lshlrev_b32 v4, 16, v196\n"
"v_and_b32 v5, 0xffff0000, v196\n"
"v_pk_fma_f32 v[2:3], v[2:3], v[32:33], v[40:41] op_sel_hi:[1,0,0]\n"
"v_cvt_pk_bf16_f32 v86, v6, v7\n"
"buffer_load_dwordx4 v11, s[80:83], s84 offen offset:2048 lds\n"
"v_lshlrev_b32 v6, 16, v197\n"
"v_and_b32 v7, 0xffff0000, v197\n"
"v_pk_fma_f32 v[4:5], v[4:5], v[34:35], v[42:43] op_sel_hi:[1,0,0]\n"
"v_cvt_pk_bf16_f32 v87, v2, v3\n"
"v_lshlrev_b32 v2, 16, v198\n"
"v_and_b32 v3, 0xffff0000, v198\n"
"v_pk_fma_f32 v[6:7], v[6:7], v[34:35], v[42:43] op_sel_hi:[1,0,0]\n"
"v_cvt_pk_bf16_f32 v196, v4, v5\n"
"v_lshlrev_b32 v4, 16, v199\n"
"v_and_b32 v5, 0xffff0000, v199\n"
"v_pk_fma_f32 v[2:3], v[2:3], v[34:35], v[42:43] op_sel_hi:[1,0,0]\n"
"v_cvt_pk_bf16_f32 v197, v6, v7\n"
"v_lshlrev_b32 v6, 16, v92\n"
"v_and_b32 v7, 0xffff0000, v92\n"
"v_pk_fma_f32 v[4:5], v[4:5], v[34:35], v[42:43] op_sel_hi:[1,0,0]\n"
"v_cvt_pk_bf16_f32 v198, v2, v3\n"
"s_mov_b32 s43, 4096\n"
"buffer_store_dwordx4 v[80:83], v19, s[76:79], s43 offen offset:1024 sc0 sc1\n"
"v_lshlrev_b32 v2, 16, v93\n"
"v_and_b32 v3, 0xffff0000, v93\n"
"v_pk_fma_f32 v[6:7], v[6:7], v[32:33], v[40:41] op_sel_hi:[1,0,0]\n"
"v_cvt_pk_bf16_f32 v199, v4, v5\n"
"v_lshlrev_b32 v4, 16, v94\n"
"v_and_b32 v5, 0xffff0000, v94\n"
"v_pk_fma_f32 v[2:3], v[2:3], v[32:33], v[40:41] op_sel_hi:[1,0,0]\n"
"v_cvt_pk_bf16_f32 v92, v6, v7\n"
"v_lshlrev_b32 v6, 16, v95\n"
"v_and_b32 v7, 0xffff0000, v95\n"
"v_pk_fma_f32 v[4:5], v[4:5], v[32:33], v[40:41] op_sel_hi:[1,0,0]\n"
"v_cvt_pk_bf16_f32 v93, v2, v3\n"
"v_lshlrev_b32 v2, 16, v204\n"
"v_and_b32 v3, 0xffff0000, v204\n"
"v_pk_fma_f32 v[6:7], v[6:7], v[32:33], v[40:41] op_sel_hi:[1,0,0]\n"
"v_cvt_pk_bf16_f32 v94, v4, v5\n"
"buffer_load_dwordx4 v12, s[80:83], s84 offen offset:3072 lds\n"
"v_lshlrev_b32 v4, 16, v205\n"
"v_and_b32 v5, 0xffff0000, v205\n"
"v_pk_fma_f32 v[2:3], v[2:3], v[34:35], v[42:43] op_sel_hi:[1,0,0]\n"
"v_cvt_pk_bf16_f32 v95, v6, v7\n"
"v_lshlrev_b32 v6, 16, v206\n"
"v_and_b32 v7, 0xffff0000, v206\n"
"v_pk_fma_f32 v[4:5], v[4:5], v[34:35], v[42:43] op_sel_hi:[1,0,0]\n"
"v_cvt_pk_bf16_f32 v204, v2, v3\n"
"v_lshlrev_b32 v2, 16, v207\n"
"v_and_b32 v3, 0xffff0000, v207\n"
"v_pk_fma_f32 v[6:7], v[6:7], v[34:35], v[42:43] op_sel_hi:[1,0,0]\n"
"v_cvt_pk_bf16_f32 v205, v4, v5\n"
"v_lshlrev_b32 v4, 16, v100\n"
"v_and_b32 v5, 0xffff0000, v100\n"
"v_pk_fma_f32 v[2:3], v[2:3], v[34:35], v[42:43] op_sel_hi:[1,0,0]\n"
"v_cvt_pk_bf16_f32 v206, v6, v7\n"
"s_mov_b32 s43, 4096\n"
"buffer_store_dwordx4 v[88:91], v19, s[76:79], s43 offen offset:3072 sc0 sc1\n"
"v_lshlrev_b32 v6, 16, v101\n"
"v_and_b32 v7, 0xffff0000, v101\n"
"v_pk_fma_f32 v[4:5], v[4:5], v[32:33], v[40:41] op_sel_hi:[1,0,0]\n"
"v_cvt_pk_bf16_f32 v207, v2, v3\n"
"v_lshlrev_b32 v2, 16, v102\n"
"v_and_b32 v3, 0xffff0000, v102\n"
"v_pk_fma_f32 v[6:7], v[6:7], v[32:33], v[40:41] op_sel_hi:[1,0,0]\n"
"v_cvt_pk_bf16_f32 v100, v4, v5\n"
"v_lshlrev_b32 v4, 16, v103\n"
"v_and_b32 v5, 0xffff0000, v103\n"
"v_pk_fma_f32 v[2:3], v[2:3], v[32:33], v[40:41] op_sel_hi:[1,0,0]\n"
"v_cvt_pk_bf16_f32 v101, v6, v7\n"
"v_lshlrev_b32 v6, 16, v212\n"
"v_and_b32 v7, 0xffff0000, v212\n"
"v_pk_fma_f32 v[4:5], v[4:5], v[32:33], v[40:41] op_sel_hi:[1,0,0]\n"
"v_cvt_pk_bf16_f32 v102, v2, v3\n"
"s_add_u32 m0, s85, 135168\n"
"s_nop 0\n"
"buffer_load_dwordx4 v13, s[80:83], s84 offen offset:0 lds\n"
"v_lshlrev_b32 v2, 16, v213\n"
"v_and_b32 v3, 0xffff0000, v213\n"
"v_pk_fma_f32 v[6:7], v[6:7], v[34:35], v[42:43] op_sel_hi:[1,0,0]\n"
"v_cvt_pk_bf16_f32 v103, v4, v5\n"
"v_lshlrev_b32 v4, 16, v214\n"
"v_and_b32 v5, 0xffff0000, v214\n"
"v_pk_fma_f32 v[2:3], v[2:3], v[34:35], v[42:43] op_sel_hi:[1,0,0]\n"
"v_cvt_pk_bf16_f32 v212, v6, v7\n"
"v_lshlrev_b32 v6, 16, v215\n"
"v_and_b32 v7, 0xffff0000, v215\n"
"v_pk_fma_f32 v[4:5], v[4:5], v[34:35], v[42:43] op_sel_hi:[1,0,0]\n"
"v_cvt_pk_bf16_f32 v213, v2, v3\n"
"v_lshlrev_b32 v2, 16, v108\n"
"v_and_b32 v3, 0xffff0000, v108\n"
"v_pk_fma_f32 v[6:7], v[6:7], v[34:35], v[42:43] op_sel_hi:[1,0,0]\n"
"v_cvt_pk_bf16_f32 v214, v4, v5\n"
"s_mov_b32 s43, 12288\n"
"buffer_store_dwordx4 v[96:99], v19, s[76:79], s43 offen offset:1024 sc0 sc1\n"
"v_lshlrev_b32 v4, 16, v109\n"
"v_and_b32 v5, 0xffff0000, v109\n"
"v_pk_fma_f32 v[2:3], v[2:3], v[32:33], v[40:41] op_sel_hi:[1,0,0]\n"
"v_cvt_pk_bf16_f32 v215, v6, v7\n"
"v_lshlrev_b32 v6, 16, v110\n"
"v_and_b32 v7, 0xffff0000, v110\n"
"v_pk_fma_f32 v[4:5], v[4:5], v[32:33], v[40:41] op_sel_hi:[1,0,0]\n"
"v_cvt_pk_bf16_f32 v108, v2, v3\n"
"v_lshlrev_b32 v2, 16, v111\n"
"v_and_b32 v3, 0xffff0000, v111\n"
"v_pk_fma_f32 v[6:7], v[6:7], v[32:33], v[40:41] op_sel_hi:[1,0,0]\n"
"v_cvt_pk_bf16_f32 v109, v4, v5\n"
"v_lshlrev_b32 v4, 16, v220\n"
"v_and_b32 v5, 0xffff0000, v220\n"
"v_pk_fma_f32 v[2:3], v[2:3], v[32:33], v[40:41] op_sel_hi:[1,0,0]\n"
"v_cvt_pk_bf16_f32 v110, v6, v7\n"
"buffer_load_dwordx4 v14, s[80:83], s84 offen offset:1024 lds\n"
"v_lshlrev_b32 v6, 16, v221\n"
"v_and_b32 v7, 0xffff0000, v221\n"
"v_pk_fma_f32 v[4:5], v[4:5], v[34:35], v[42:43] op_sel_hi:[1,0,0]\n"
"v_cvt_pk_bf16_f32 v111, v2, v3\n"
"v_lshlrev_b32 v2, 16, v222\n"
"v_and_b32 v3, 0xffff0000, v222\n"
"v_pk_fma_f32 v[6:7], v[6:7], v[34:35], v[42:43] op_sel_hi:[1,0,0]\n"
"v_cvt_pk_bf16_f32 v220, v4, v5\n"
"v_lshlrev_b32 v4, 16, v223\n"
"v_and_b32 v5, 0xffff0000, v223\n"
"v_pk_fma_f32 v[2:3], v[2:3], v[34:35], v[42:43] op_sel_hi:[1,0,0]\n"
"v_cvt_pk_bf16_f32 v221, v6, v7\n"
"v_lshlrev_b32 v6, 16, v116\n"
"v_and_b32 v7, 0xffff0000, v116\n"
"v_pk_fma_f32 v[4:5], v[4:5], v[34:35], v[42:43] op_sel_hi:[1,0,0]\n"
"v_cvt_pk_bf16_f32 v222, v2, v3\n"
"s_mov_b32 s43, 12288\n"
"buffer_store_dwordx4 v[104:107], v19, s[76:79], s43 offen offset:3072 sc0 sc1\n"
"v_lshlrev_b32 v2, 16, v117\n"
"v_and_b32 v3, 0xffff0000, v117\n"
"v_pk_fma_f32 v[6:7], v[6:7], v[32:33], v[40:41] op_sel_hi:[1,0,0]\n"
"v_cvt_pk_bf16_f32 v223, v4, v5\n"
"v_lshlrev_b32 v4, 16, v118\n"
"v_and_b32 v5, 0xffff0000, v118\n"
"v_pk_fma_f32 v[2:3], v[2:3], v[32:33], v[40:41] op_sel_hi:[1,0,0]\n"
"v_cvt_pk_bf16_f32 v116, v6, v7\n"
"v_lshlrev_b32 v6, 16, v119\n"
"v_and_b32 v7, 0xffff0000, v119\n"
"v_pk_fma_f32 v[4:5], v[4:5], v[32:33], v[40:41] op_sel_hi:[1,0,0]\n"
"v_cvt_pk_bf16_f32 v117, v2, v3\n"
"v_lshlrev_b32 v2, 16, v228\n"
"v_and_b32 v3, 0xffff0000, v228\n"
"v_pk_fma_f32 v[6:7], v[6:7], v[32:33], v[40:41] op_sel_hi:[1,0,0]\n"
"v_cvt_pk_bf16_f32 v118, v4, v5\n"
"buffer_load_dwordx4 v15, s[80:83], s84 offen offset:2048 lds\n"
"s_add_u32 s84, s84, s56\n"
"v_lshlrev_b32 v4, 16, v229\n"
"v_and_b32 v5, 0xffff0000, v229\n"
"v_pk_fma_f32 v[2:3], v[2:3], v[34:35], v[42:43] op_sel_hi:[1,0,0]\n"
"v_cvt_pk_bf16_f32 v119, v6, v7\n"
"v_lshlrev_b32 v6, 16, v230\n"
"v_and_b32 v7, 0xffff0000, v230\n"
"v_pk_fma_f32 v[4:5], v[4:5], v[34:35], v[42:43] op_sel_hi:[1,0,0]\n"
"v_cvt_pk_bf16_f32 v228, v2, v3\n"
"v_lshlrev_b32 v2, 16, v231\n"
"v_and_b32 v3, 0xffff0000, v231\n"
"v_pk_fma_f32 v[6:7], v[6:7], v[34:35], v[42:43] op_sel_hi:[1,0,0]\n"
"v_cvt_pk_bf16_f32 v229, v4, v5\n"
"v_lshlrev_b32 v4, 16, v124\n"
"v_and_b32 v5, 0xffff0000, v124\n"
"v_pk_fma_f32 v[2:3], v[2:3], v[34:35], v[42:43] op_sel_hi:[1,0,0]\n"
"v_cvt_pk_bf16_f32 v230, v6, v7\n"
"s_mov_b32 s43, 36864\n"
"buffer_store_dwordx4 v[112:115], v19, s[76:79], s43 offen offset:0 sc0 sc1\n"
"v_lshlrev_b32 v6, 16, v125\n"
"v_and_b32 v7, 0xffff0000, v125\n"
"v_pk_fma_f32 v[4:5], v[4:5], v[32:33], v[40:41] op_sel_hi:[1,0,0]\n"
"v_cvt_pk_bf16_f32 v231, v2, v3\n"
"v_lshlrev_b32 v2, 16, v126\n"
"v_and_b32 v3, 0xffff0000, v126\n"
"v_pk_fma_f32 v[6:7], v[6:7], v[32:33], v[40:41] op_sel_hi:[1,0,0]\n"
"v_cvt_pk_bf16_f32 v124, v4, v5\n"
"v_lshlrev_b32 v4, 16, v127\n"
"v_and_b32 v5, 0xffff0000, v127\n"
"v_pk_fma_f32 v[2:3], v[2:3], v[32:33], v[40:41] op_sel_hi:[1,0,0]\n"
"v_cvt_pk_bf16_f32 v125, v6, v7\n"
"v_lshlrev_b32 v6, 16, v236\n"
"v_and_b32 v7, 0xffff0000, v236\n"
"v_pk_fma_f32 v[4:5], v[4:5], v[32:33], v[40:41] op_sel_hi:[1,0,0]\n"
"v_cvt_pk_bf16_f32 v126, v2, v3\n"
"s_add_u32 m0, s85, 0\n"
"s_nop 0\n"
"buffer_load_dwordx4 v9, s[80:83], s84 offen offset:0 lds\n"
"v_lshlrev_b32 v2, 16, v237\n"
"v_and_b32 v3, 0xffff0000, v237\n"
"v_pk_fma_f32 v[6:7], v[6:7], v[34:35], v[42:43] op_sel_hi:[1,0,0]\n"
"v_cvt_pk_bf16_f32 v127, v4, v5\n"
"v_lshlrev_b32 v4, 16, v238\n"
"v_and_b32 v5, 0xffff0000, v238\n"
"v_pk_fma_f32 v[2:3], v[2:3], v[34:35], v[42:43] op_sel_hi:[1,0,0]\n"
"v_cvt_pk_bf16_f32 v236, v6, v7\n"
"v_lshlrev_b32 v6, 16, v239\n"
"v_and_b32 v7, 0xffff0000, v239\n"
"v_pk_fma_f32 v[4:5], v[4:5], v[34:35], v[42:43] op_sel_hi:[1,0,0]\n"
"v_cvt_pk_bf16_f32 v237, v2, v3\n"
"v_lshlrev_b32 v2, 16, v132\n"
"v_and_b32 v3, 0xffff0000, v132\n"
"v_pk_fma_f32 v[6:7], v[6:7], v[34:35], v[42:43] op_sel_hi:[1,0,0]\n"
"v_cvt_pk_bf16_f32 v238, v4, v5\n"
"s_mov_b32 s43, 36864\n"
"buffer_store_dwordx4 v[120:123], v19, s[76:79], s43 offen offset:2048 sc0 sc1\n"
"v_lshlrev_b32 v4, 16, v133\n"
"v_and_b32 v5, 0xffff0000, v133\n"
"v_pk_fma_f32 v[2:3], v[2:3], v[32:33], v[40:41] op_sel_hi:[1,0,0]\n"
"v_cvt_pk_bf16_f32 v239, v6, v7\n"
"v_lshlrev_b32 v6, 16, v134\n"
"v_and_b32 v7, 0xffff0000, v134\n"
"v_pk_fma_f32 v[4:5], v[4:5], v[32:33], v[40:41] op_sel_hi:[1,0,0]\n"
"v_cvt_pk_bf16_f32 v132, v2, v3\n"
"v_lshlrev_b32 v2, 16, v135\n"
"v_and_b32 v3, 0xffff0000, v135\n"
"v_pk_fma_f32 v[6:7], v[6:7], v[32:33], v[40:41] op_sel_hi:[1,0,0]\n"
"v_cvt_pk_bf16_f32 v133, v4, v5\n"
"v_lshlrev_b32 v4, 16, v244\n"
"v_and_b32 v5, 0xffff0000, v244\n"
"v_pk_fma_f32 v[2:3], v[2:3], v[32:33], v[40:41] op_sel_hi:[1,0,0]\n"
"v_cvt_pk_bf16_f32 v134, v6, v7\n"
"buffer_load_dwordx4 v10, s[80:83], s84 offen offset:1024 lds\n"
"v_lshlrev_b32 v6, 16, v245\n"
"v_and_b32 v7, 0xffff0000, v245\n"
"v_pk_fma_f32 v[4:5], v[4:5], v[34:35], v[42:43] op_sel_hi:[1,0,0]\n"
"v_cvt_pk_bf16_f32 v135, v2, v3\n"
"v_lshlrev_b32 v2, 16, v246\n"
"v_and_b32 v3, 0xffff0000, v246\n"
"v_pk_fma_f32 v[6:7], v[6:7], v[34:35], v[42:43] op_sel_hi:[1,0,0]\n"
"v_cvt_pk_bf16_f32 v244, v4, v5\n"
"v_lshlrev_b32 v4, 16, v247\n"
"v_and_b32 v5, 0xffff0000, v247\n"
"v_pk_fma_f32 v[2:3], v[2:3], v[34:35], v[42:43] op_sel_hi:[1,0,0]\n"
"v_cvt_pk_bf16_f32 v245, v6, v7\n"
"v_lshlrev_b32 v6, 16, v140\n"
"v_and_b32 v7, 0xffff0000, v140\n"
"v_pk_fma_f32 v[4:5], v[4:5], v[34:35], v[42:43] op_sel_hi:[1,0,0]\n"
"v_cvt_pk_bf16_f32 v246, v2, v3\n"
"s_mov_b32 s43, 45056\n"
"buffer_store_dwordx4 v[128:131], v19, s[76:79], s43 offen offset:0 sc0 sc1\n"
"v_lshlrev_b32 v2, 16, v141\n"
"v_and_b32 v3, 0xffff0000, v141\n"
"v_pk_fma_f32 v[6:7], v[6:7], v[32:33], v[40:41] op_sel_hi:[1,0,0]\n"
"v_cvt_pk_bf16_f32 v247, v4, v5\n"
"v_lshlrev_b32 v4, 16, v142\n"
"v_and_b32 v5, 0xffff0000, v142\n"
"v_pk_fma_f32 v[2:3], v[2:3], v[32:33], v[40:41] op_sel_hi:[1,0,0]\n"
"v_cvt_pk_bf16_f32 v140, v6, v7\n"
"v_lshlrev_b32 v6, 16, v143\n"
"v_and_b32 v7, 0xffff0000, v143\n"
"v_pk_fma_f32 v[4:5], v[4:5], v[32:33], v[40:41] op_sel_hi:[1,0,0]\n"
"v_cvt_pk_bf16_f32 v141, v2, v3\n"
"v_lshlrev_b32 v2, 16, v252\n"
"v_and_b32 v3, 0xffff0000, v252\n"
"v_pk_fma_f32 v[6:7], v[6:7], v[32:33], v[40:41] op_sel_hi:[1,0,0]\n"
"v_cvt_pk_bf16_f32 v142, v4, v5\n"
"buffer_load_dwordx4 v11, s[80:83], s84 offen offset:2048 lds\n"
"v_lshlrev_b32 v4, 16, v253\n"
"v_and_b32 v5, 0xffff0000, v253\n"
"v_pk_fma_f32 v[2:3], v[2:3], v[34:35], v[42:43] op_sel_hi:[1,0,0]\n"
"v_cvt_pk_bf16_f32 v143, v6, v7\n"
"v_lshlrev_b32 v6, 16, v254\n"
"v_and_b32 v7, 0xffff0000, v254\n"
"v_pk_fma_f32 v[4:5], v[4:5], v[34:35], v[42:43] op_sel_hi:[1,0,0]\n"
"v_cvt_pk_bf16_f32 v252, v2, v3\n"
"v_lshlrev_b32 v2, 16, v255\n"
"v_and_b32 v3, 0xffff0000, v255\n"
"v_pk_fma_f32 v[6:7], v[6:7], v[34:35], v[42:43] op_sel_hi:[1,0,0]\n"
"v_cvt_pk_bf16_f32 v253, v4, v5\n"
"v_pk_fma_f32 v[2:3], v[2:3], v[34:35], v[42:43] op_sel_hi:[1,0,0]\n"
"v_cvt_pk_bf16_f32 v254, v6, v7\n"
"s_mov_b32 s43, 45056\n"
"buffer_store_dwordx4 v[136:139], v19, s[76:79], s43 offen offset:2048 sc0 sc1\n"
"v_cvt_pk_bf16_f32 v255, v2, v3\n"
"s_mov_b32 s38, 65536\n"
"s_mov_b32 s39, 32768\n"
"s_lshl_b32 s41, s86, 1\n"
"s_sub_u32 s41, s41, 4\n"
"s_sub_u32 s46, s86, 1\n"
"s_lshl_b32 s53, s52, 11\n"
"s_sub_u32 s53, s53, 2048\n"
"s_waitcnt vmcnt(35)\n"
"s_barrier\n"
"ds_read_b64_tr_b16 a[0:1], v25 offset:0\n"
"ds_read_b64_tr_b16 a[2:3], v25 offset:512\n"
"ds_read_b64_tr_b16 a[4:5], v25 offset:4096\n"
"ds_read_b64_tr_b16 a[6:7], v25 offset:4608\n"
"ds_read_b64_tr_b16 a[8:9], v25 offset:8192\n"
"ds_read_b64_tr_b16 a[10:11], v25 offset:8704\n"
"ds_read_b64_tr_b16 a[12:13], v25 offset:12288\n"
"ds_read_b64_tr_b16 a[14:15], v25 offset:12800\n"
"ds_read_b64_tr_b16 a[16:17], v25 offset:16384\n"
"ds_read_b64_tr_b16 a[18:19], v25 offset:16896\n"
"ds_read_b64_tr_b16 a[20:21], v25 offset:20480\n"
"ds_read_b64_tr_b16 a[22:23], v25 offset:20992\n"
"ds_read_b64_tr_b16 a[24:25], v25 offset:24576\n"
"ds_read_b64_tr_b16 a[26:27], v25 offset:25088\n"
"ds_read_b64_tr_b16 a[28:29], v26 offset:0\n"
"ds_read_b64_tr_b16 a[30:31], v26 offset:512\n"
"ds_read_b64_tr_b16 a[32:33], v26 offset:4096\n"
"ds_read_b64_tr_b16 a[34:35], v26 offset:4608\n"
"ds_read_b64_tr_b16 a[36:37], v26 offset:8192\n"
"ds_read_b64_tr_b16 a[38:39], v26 offset:8704\n"
"s_mov_b32 s48, 0xbfb8aa3b\n"
"LP%=:\n"
"s_cmp_eq_u32 s54, 1\n"
"s_cbranch_scc1 LH%=\n"
"LQ%=:\n"
"s_waitcnt lgkmcnt(15)\n"
"v_mfma_f32_16x16x32_bf16 v[32:35], a[0:3], v[36:39], 0\n"
"ds_read_b64_tr_b16 a[40:41], v26 offset:12288\n"
"v_cvt_pk_bf16_f32 v97, v66, v67\n"
"v_mfma_f32_16x16x32_bf16 v[40:43], a[0:3], v[148:151], 0\n"
"ds_read_b64_tr_b16 a[42:43], v26 offset:12800\n"
"v_cvt_pk_bf16_f32 v104, v72, v73\n"
"v_mfma_f32_16x16x32_bf16 v[32:35], a[4:7], v[44:47], v[32:35]\n"
"ds_read_b64_tr_b16 a[44:45], v26 offset:16384\n"
"v_cvt_pk_bf16_f32 v105, v74, v75\n"
"v_mfma_f32_16x16x32_bf16 v[40:43], a[4:7], v[156:159], v[40:43]\n"
"ds_read_b64_tr_b16 a[46:47], v26 offset:16896\n"
"v_cvt_pk_bf16_f32 v98, v80, v81\n"
"s_waitcnt lgkmcnt(15)\n"
"v_mfma_f32_16x16x32_bf16 v[32:35], a[8:11], v[52:55], v[32:35]\n"
"ds_read_b64_tr_b16 a[48:49], v26 offset:20480\n"
"v_cvt_pk_bf16_f32 v99, v82, v83\n"
"v_mfma_f32_16x16x32_bf16 v[40:43], a[8:11], v[164:167], v[40:43]\n"
"ds_read_b64_tr_b16 a[50:51], v26 offset:20992\n"
"buffer_store_dwordx4 v[96:99], v17, s[28:31], s53 offen offset:1024 sc0 sc1\n"
"v_mfma_f32_16x16x32_bf16 v[32:35], a[12:15], v[60:63], v[32:35]\n"
"ds_read_b64_tr_b16 a[52:53], v26 offset:24576\n"
"s_waitcnt lgkmcnt(11)\n"
"v_mfma_f32_16x16x32_bf16 v[40:43], a[12:15], v[172:175], v[40:43]\n"
"ds_read_b64_tr_b16 a[54:55], v26 offset:25088\n"
"buffer_load_dwordx4 v12, s[80:83], s84 offen offset:3072 lds\n"
"v_mfma_f32_16x16x32_bf16 v[32:35], a[16:19], v[68:71], v[32:35]\n"
"ds_read_b64_tr_b16 a[0:1], v27 offset:0\n"
"s_add_u32 s38, s38, 32768\n"
"v_mfma_f32_16x16x32_bf16 v[40:43], a[16:19], v[180:183], v[40:43]\n"
"ds_read_b64_tr_b16 a[2:3], v27 offset:512\n"
"s_add_u32 m0, m0, 0x1000\n"
"v_mfma_f32_16x16x32_bf16 v[32:35], a[20:23], v[76:79], v[32:35]\n"
"ds_read_b64_tr_b16 a[4:5], v27 offset:4096\n"
"s_cmp_eq_u32 s38, 163840\n"
"v_mfma_f32_16x16x32_bf16 v[40:43], a[20:23], v[188:191], v[40:43]\n"
"ds_read_b64_tr_b16 a[6:7], v27 offset:4608\n"
"s_cselect_b32 s38, 0, s38\n"
"v_mfma_f32_16x16x32_bf16 v[32:35], a[24:27], v[84:87], v[32:35]\n"
"ds_read_b64_tr_b16 a[8:9], v27 offset:8192\n"
"v_add_u32 v25, s38, v21\n"
"v_mfma_f32_16x16x32_bf16 v[40:43], a[24:27], v[196:199], v[40:43]\n"
"ds_read_b64_tr_b16 a[10:11], v27 offset:8704\n"
"v_add_u32 v26, s38, v22\n"
"v_mfma_f32_16x16x32_bf16 v[48:51], a[28:31], v[36:39], 0\n"
"ds_read_b64_tr_b16 a[12:13], v27 offset:12288\n"
"s_waitcnt lgkmcnt(11)\n"
"v_mfma_f32_16x16x32_bf16 v[56:59], a[28:31], v[148:151], 0\n"
"ds_read_b64_tr_b16 a[14:15], v27 offset:12800\n"
"buffer_load_dwordx4 v13, s[80:83], s84 offen offset:0 lds\n"
"v_mfma_f32_16x16x32_bf16 v[48:51], a[32:35], v[44:47], v[48:51]\n"
"ds_read_b64_tr_b16 a[16:17], v27 offset:16384\n"
"v_cvt_pk_bf16_f32 v106, v88, v89\n"
"v_mfma_f32_16x16x32_bf16 v[56:59], a[32:35], v[156:159], v[56:59]\n"
"ds_read_b64_tr_b16 a[18:19], v27 offset:16896\n"
"v_cvt_pk_bf16_f32 v107, v90, v91\n"
"v_mfma_f32_16x16x32_bf16 v[48:51], a[36:39], v[52:55], v[48:51]\n"
"ds_read_b64_tr_b16 a[20:21], v27 offset:20480\n"
"buffer_store_dwordx4 v[104:107], v18, s[28:31], s53 offen offset:1024 sc0 sc1\n"
"v_mfma_f32_16x16x32_bf16 v[56:59], a[36:39], v[164:167], v[56:59]\n"
"ds_read_b64_tr_b16 a[22:23], v27 offset:20992\n"
"v_mfma_f32_16x16x32_bf16 v[48:51], a[40:43], v[60:63], v[48:51]\n"
"ds_read_b64_tr_b16 a[24:25], v27 offset:24576\n"
"v_mfma_f32_16x16x32_bf16 v[56:59], a[40:43], v[172:175], v[56:59]\n"
"ds_read_b64_tr_b16 a[26:27], v27 offset:25088\n"
"v_mfma_f32_16x16x32_bf16 v[48:51], a[44:47], v[68:71], v[48:51]\n"
"ds_read_b64_tr_b16 a[28:29], v28 offset:0\n"
"s_waitcnt lgkmcnt(11)\n"
"v_mfma_f32_16x16x32_bf16 v[56:59], a[44:47], v[180:183], v[56:59]\n"
"ds_read_b64_tr_b16 a[30:31], v28 offset:512\n"
"buffer_load_dwordx4 v14, s[80:83], s84 offen offset:1024 lds\n"
"v_mfma_f32_16x16x32_bf16 v[48:51], a[48:51], v[76:79], v[48:51]\n"
"ds_read_b64_tr_b16 a[32:33], v28 offset:4096\n"
"v_add_u32 v27, s38, v23\n"
"v_mfma_f32_16x16x32_bf16 v[56:59], a[48:51], v[188:191], v[56:59]\n"
"ds_read_b64_tr_b16 a[34:35], v28 offset:4608\n"
"v_mfma_f32_16x16x32_bf16 v[48:51], a[52:55], v[84:87], v[48:51]\n"
"ds_read_b64_tr_b16 a[36:37], v28 offset:8192\n"
"v_mfma_f32_16x16x32_bf16 v[56:59], a[52:55], v[196:199], v[56:59]\n"
"ds_read_b64_tr_b16 a[38:39], v28 offset:8704\n"
"v_mfma_f32_16x16x32_bf16 v[64:67], a[0:3], v[36:39], 0\n"
"ds_read_b64_tr_b16 a[40:41], v28 offset:12288\n"
"v_mfma_f32_16x16x32_bf16 v[72:75], a[0:3], v[148:151], 0\n"
"ds_read_b64_tr_b16 a[42:43], v28 offset:12800\n"
"v_mfma_f32_16x16x32_bf16 v[64:67], a[4:7], v[44:47], v[64:67]\n"
"ds_read_b64_tr_b16 a[44:45], v28 offset:16384\n"
"s_waitcnt lgkmcnt(11)\n"
"v_mfma_f32_16x16x32_bf16 v[72:75], a[4:7], v[156:159], v[72:75]\n"
"ds_read_b64_tr_b16 a[46:47], v28 offset:16896\n"
"buffer_load_dwordx4 v15, s[80:83], s84 offen offset:2048 lds\n"
"v_mfma_f32_16x16x32_bf16 v[64:67], a[8:11], v[52:55], v[64:67]\n"
"ds_read_b64_tr_b16 a[48:49], v28 offset:20480\n"
"s_add_u32 s84, s84, s87\n"
"v_mfma_f32_16x16x32_bf16 v[72:75], a[8:11], v[164:167], v[72:75]\n"
"ds_read_b64_tr_b16 a[50:51], v28 offset:20992\n"
"s_add_u32 m0, s39, s85\n"
"v_mfma_f32_16x16x32_bf16 v[64:67], a[12:15], v[60:63], v[64:67]\n"
"ds_read_b64_tr_b16 a[52:53], v28 offset:24576\n"
"s_sub_u32 s41, s41, 1\n"
"v_mfma_f32_16x16x32_bf16 v[72:75], a[12:15], v[172:175], v[72:75]\n"
"ds_read_b64_tr_b16 a[54:55], v28 offset:25088\n"
"s_waitcnt vmcnt(20)\n"
"s_barrier\n"
"v_mfma_f32_16x16x32_bf16 v[64:67], a[16:19], v[68:71], v[64:67]\n"
"ds_read_b64_tr_b16 a[0:1], v25 offset:0\n"
"s_cmp_ge_i32 s41, 0\n"
"v_mfma_f32_16x16x32_bf16 v[72:75], a[16:19], v[180:183], v[72:75]\n"
"ds_read_b64_tr_b16 a[2:3], v25 offset:512\n"
"s_cselect_b32 s82, s55, 0\n"
"v_mfma_f32_16x16x32_bf16 v[64:67], a[20:23], v[76:79], v[64:67]\n"
"ds_read_b64_tr_b16 a[4:5], v25 offset:4096\n"
"s_waitcnt lgkmcnt(11)\n"
"v_mfma_f32_16x16x32_bf16 v[72:75], a[20:23], v[188:191], v[72:75]\n"
"ds_read_b64_tr_b16 a[6:7], v25 offset:4608\n"
"buffer_load_dwordx4 v9, s[80:83], s84 offen offset:0 lds\n"
"v_mfma_f32_16x16x32_bf16 v[64:67], a[24:27], v[84:87], v[64:67]\n"
"ds_read_b64_tr_b16 a[8:9], v25 offset:8192\n"
"v_add_u32 v28, s38, v24\n"
"v_mfma_f32_16x16x32_bf16 v[72:75], a[24:27], v[196:199], v[72:75]\n"
"ds_read_b64_tr_b16 a[10:11], v25 offset:8704\n"
"s_add_u32 s39, s39, 32768\n"
"v_mfma_f32_16x16x32_bf16 v[80:83], a[28:31], v[36:39], 0\n"
"ds_read_b64_tr_b16 a[12:13], v25 offset:12288\n"
"s_cmp_eq_u32 s39, 163840\n"
"v_mfma_f32_16x16x32_bf16 v[88:91], a[28:31], v[148:151], 0\n"
"ds_read_b64_tr_b16 a[14:15], v25 offset:12800\n"
"s_cselect_b32 s39, 0, s39\n"
"v_mfma_f32_16x16x32_bf16 v[80:83], a[32:35], v[44:47], v[80:83]\n"
"ds_read_b64_tr_b16 a[16:17], v25 offset:16384\n"
"s_lshr_b32 s47, s52, 2\n"
"v_mfma_f32_16x16x32_bf16 v[88:91], a[32:35], v[156:159], v[88:91]\n"
"ds_read_b64_tr_b16 a[18:19], v25 offset:16896\n"
"s_lshl_b32 s47, s47, 20\n"
"v_mfma_f32_16x16x32_bf16 v[80:83], a[36:39], v[52:55], v[80:83]\n"
"ds_read_b64_tr_b16 a[20:21], v25 offset:20480\n"
"s_waitcnt lgkmcnt(11)\n"
"v_mfma_f32_16x16x32_bf16 v[88:91], a[36:39], v[164:167], v[88:91]\n"
"ds_read_b64_tr_b16 a[22:23], v25 offset:20992\n"
"buffer_load_dwordx4 v10, s[80:83], s84 offen offset:1024 lds\n"
"v_mfma_f32_16x16x32_bf16 v[80:83], a[40:43], v[60:63], v[80:83]\n"
"ds_read_b64_tr_b16 a[24:25], v25 offset:24576\n"
"s_and_b32 s53, s52, 3\n"
"v_mfma_f32_16x16x32_bf16 v[88:91], a[40:43], v[172:175], v[88:91]\n"
"ds_read_b64_tr_b16 a[26:27], v25 offset:25088\n"
"s_lshl_b32 s53, s53, 11\n"
"v_mfma_f32_16x16x32_bf16 v[80:83], a[44:47], v[68:71], v[80:83]\n"
"ds_read_b64_tr_b16 a[28:29], v26 offset:0\n"
"s_add_u32 s53, s53, s47\n"
"v_mfma_f32_16x16x32_bf16 v[88:91], a[44:47], v[180:183], v[88:91]\n"
"ds_read_b64_tr_b16 a[30:31], v26 offset:512\n"
"s_swappc_b64 s[58:59], s[88:89]\n"
"v_mfma_f32_16x16x32_bf16 v[80:83], a[48:51], v[76:79], v[80:83]\n"
"ds_read_b64_tr_b16 a[32:33], v26 offset:4096\n"
"s_add_u32 s52, s52, 1\n"
"v_mfma_f32_16x16x32_bf16 v[88:91], a[48:51], v[188:191], v[88:91]\n"
"ds_read_b64_tr_b16 a[34:35], v26 offset:4608\n"
"s_mov_b32 s30, 0x1000000\n"
"v_mfma_f32_16x16x32_bf16 v[80:83], a[52:55], v[84:87], v[80:83]\n"
"ds_read_b64_tr_b16 a[36:37], v26 offset:8192\n"
"s_waitcnt lgkmcnt(11)\n"
"v_mfma_f32_16x16x32_bf16 v[88:91], a[52:55], v[196:199], v[88:91]\n"
"ds_read_b64_tr_b16 a[38:39], v26 offset:8704\n"
"buffer_load_dwordx4 v11, s[80:83], s84 offen offset:2048 lds\n"
"v_mfma_f32_16x16x32_bf16 v[32:35], a[0:3], v[92:95], v[32:35]\n"
"ds_read_b64_tr_b16 a[40:41], v26 offset:12288\n"
"v_mfma_f32_16x16x32_bf16 v[40:43], a[0:3], v[204:207], v[40:43]\n"
"ds_read_b64_tr_b16 a[42:43], v26 offset:12800\n"
"v_mfma_f32_16x16x32_bf16 v[32:35], a[4:7], v[100:103], v[32:35]\n"
"ds_read_b64_tr_b16 a[44:45], v26 offset:16384\n"
"v_mfma_f32_16x16x32_bf16 v[40:43], a[4:7], v[212:215], v[40:43]\n"
"ds_read_b64_tr_b16 a[46:47], v26 offset:16896\n"
"s_swappc_b64 s[58:59], s[88:89]\n"
"v_mfma_f32_16x16x32_bf16 v[32:35], a[8:11], v[108:111], v[32:35]\n"
"ds_read_b64_tr_b16 a[48:49], v26 offset:20480\n"
"v_mfma_f32_16x16x32_bf16 v[40:43], a[8:11], v[220:223], v[40:43]\n"
"ds_read_b64_tr_b16 a[50:51], v26 offset:20992\n"
"v_mfma_f32_16x16x32_bf16 v[32:35], a[12:15], v[116:119], v[32:35]\n"
"ds_read_b64_tr_b16 a[52:53], v26 offset:24576\n"
"s_waitcnt lgkmcnt(11)\n"
"v_mfma_f32_16x16x32_bf16 v[40:43], a[12:15], v[228:231], v[40:43]\n"
"ds_read_b64_tr_b16 a[54:55], v26 offset:25088\n"
"buffer_load_dwordx4 v12, s[80:83], s84 offen offset:3072 lds\n"
"v_mfma_f32_16x16x32_bf16 v[32:35], a[16:19], v[124:127], v[32:35]\n"
"ds_read_b64_tr_b16 a[0:1], v27 offset:0\n"
"s_add_u32 s38, s38, 32768\n"
"v_mfma_f32_16x16x32_bf16 v[40:43], a[16:19], v[236:239], v[40:43]\n"
"ds_read_b64_tr_b16 a[2:3], v27 offset:512\n"
"s_add_u32 m0, m0, 0x1000\n"
"v_mfma_f32_16x16x32_bf16 v[32:35], a[20:23], v[132:135], v[32:35]\n"
"ds_read_b64_tr_b16 a[4:5], v27 offset:4096\n"
"s_cmp_eq_u32 s38, 163840\n"
"v_mfma_f32_16x16x32_bf16 v[40:43], a[20:23], v[244:247], v[40:43]\n"
"ds_read_b64_tr_b16 a[6:7], v27 offset:4608\n"
"s_cselect_b32 s38, 0, s38\n"
"v_mfma_f32_16x16x32_bf16 v[32:35], a[24:27], v[140:143], v[32:35]\n"
"ds_read_b64_tr_b16 a[8:9], v27 offset:8192\n"
"v_add_u32 v25, s38, v21\n"
"v_mfma_f32_16x16x32_bf16 v[40:43], a[24:27], v[252:255], v[40:43]\n"
"ds_read_b64_tr_b16 a[10:11], v27 offset:8704\n"
"v_add_u32 v26, s38, v22\n"
"v_mfma_f32_16x16x32_bf16 v[48:51], a[28:31], v[92:95], v[48:51]\n"
"ds_read_b64_tr_b16 a[12:13], v27 offset:12288\n"
"s_waitcnt lgkmcnt(11)\n"
"v_mfma_f32_16x16x32_bf16 v[56:59], a[28:31], v[204:207], v[56:59]\n"
"ds_read_b64_tr_b16 a[14:15], v27 offset:12800\n"
"buffer_load_dwordx4 v13, s[80:83], s84 offen offset:0 lds\n"
"v_mfma_f32_16x16x32_bf16 v[48:51], a[32:35], v[100:103], v[48:51]\n"
"ds_read_b64_tr_b16 a[16:17], v27 offset:16384\n"
"v_cvt_pk_bf16_f32 v96, v32, v33\n"
"v_mfma_f32_16x16x32_bf16 v[56:59], a[32:35], v[212:215], v[56:59]\n"
"ds_read_b64_tr_b16 a[18:19], v27 offset:16896\n"
"v_cvt_pk_bf16_f32 v97, v34, v35\n"
"v_mfma_f32_16x16x32_bf16 v[48:51], a[36:39], v[108:111], v[48:51]\n"
"ds_read_b64_tr_b16 a[20:21], v27 offset:20480\n"
"v_cvt_pk_bf16_f32 v104, v40, v41\n"
"v_mfma_f32_16x16x32_bf16 v[56:59], a[36:39], v[220:223], v[56:59]\n"
"ds_read_b64_tr_b16 a[22:23], v27 offset:20992\n"
"v_cvt_pk_bf16_f32 v105, v42, v43\n"
"v_mfma_f32_16x16x32_bf16 v[48:51], a[40:43], v[116:119], v[48:51]\n"
"ds_read_b64_tr_b16 a[24:25], v27 offset:24576\n"
"v_mfma_f32_16x16x32_bf16 v[56:59], a[40:43], v[228:231], v[56:59]\n"
"ds_read_b64_tr_b16 a[26:27], v27 offset:25088\n"
"v_mfma_f32_16x16x32_bf16 v[48:51], a[44:47], v[124:127], v[48:51]\n"
"ds_read_b64_tr_b16 a[28:29], v28 offset:0\n"
"s_waitcnt lgkmcnt(11)\n"
"v_mfma_f32_16x16x32_bf16 v[56:59], a[44:47], v[236:239], v[56:59]\n"
"ds_read_b64_tr_b16 a[30:31], v28 offset:512\n"
"buffer_load_dwordx4 v14, s[80:83], s84 offen offset:1024 lds\n"
"v_mfma_f32_16x16x32_bf16 v[48:51], a[48:51], v[132:135], v[48:51]\n"
"ds_read_b64_tr_b16 a[32:33], v28 offset:4096\n"
"v_add_u32 v27, s38, v23\n"
"v_mfma_f32_16x16x32_bf16 v[56:59], a[48:51], v[244:247], v[56:59]\n"
"ds_read_b64_tr_b16 a[34:35], v28 offset:4608\n"
"v_mfma_f32_16x16x32_bf16 v[48:51], a[52:55], v[140:143], v[48:51]\n"
"ds_read_b64_tr_b16 a[36:37], v28 offset:8192\n"
"v_mfma_f32_16x16x32_bf16 v[56:59], a[52:55], v[252:255], v[56:59]\n"
"ds_read_b64_tr_b16 a[38:39], v28 offset:8704\n"
"v_mfma_f32_16x16x32_bf16 v[64:67], a[0:3], v[92:95], v[64:67]\n"
"ds_read_b64_tr_b16 a[40:41], v28 offset:12288\n"
"v_mfma_f32_16x16x32_bf16 v[72:75], a[0:3], v[204:207], v[72:75]\n"
"ds_read_b64_tr_b16 a[42:43], v28 offset:12800\n"
"v_mfma_f32_16x16x32_bf16 v[64:67], a[4:7], v[100:103], v[64:67]\n"
"ds_read_b64_tr_b16 a[44:45], v28 offset:16384\n"
"s_waitcnt lgkmcnt(11)\n"
"v_mfma_f32_16x16x32_bf16 v[72:75], a[4:7], v[212:215], v[72:75]\n"
"ds_read_b64_tr_b16 a[46:47], v28 offset:16896\n"
"buffer_load_dwordx4 v15, s[80:83], s84 offen offset:2048 lds\n"
"v_mfma_f32_16x16x32_bf16 v[64:67], a[8:11], v[108:111], v[64:67]\n"
"ds_read_b64_tr_b16 a[48:49], v28 offset:20480\n"
"s_add_u32 s84, s84, s56\n"
"v_mfma_f32_16x16x32_bf16 v[72:75], a[8:11], v[220:223], v[72:75]\n"
"ds_read_b64_tr_b16 a[50:51], v28 offset:20992\n"
"s_add_u32 m0, s39, s85\n"
"v_mfma_f32_16x16x32_bf16 v[64:67], a[12:15], v[116:119], v[64:67]\n"
"ds_read_b64_tr_b16 a[52:53], v28 offset:24576\n"
"s_sub_u32 s41, s41, 1\n"
"v_mfma_f32_16x16x32_bf16 v[72:75], a[12:15], v[228:231], v[72:75]\n"
"ds_read_b64_tr_b16 a[54:55], v28 offset:25088\n"
"s_waitcnt vmcnt(20)\n"
"s_barrier\n"
"v_mfma_f32_16x16x32_bf16 v[64:67], a[16:19], v[124:127], v[64:67]\n"
"ds_read_b64_tr_b16 a[0:1], v25 offset:0\n"
"s_cmp_ge_i32 s41, 0\n"
"v_mfma_f32_16x16x32_bf16 v[72:75], a[16:19], v[236:239], v[72:75]\n"
"ds_read_b64_tr_b16 a[2:3], v25 offset:512\n"
"s_cselect_b32 s82, s55, 0\n"
"v_mfma_f32_16x16x32_bf16 v[64:67], a[20:23], v[132:135], v[64:67]\n"
"ds_read_b64_tr_b16 a[4:5], v25 offset:4096\n"
"s_waitcnt lgkmcnt(11)\n"
"v_mfma_f32_16x16x32_bf16 v[72:75], a[20:23], v[244:247], v[72:75]\n"
"ds_read_b64_tr_b16 a[6:7], v25 offset:4608\n"
"buffer_load_dwordx4 v9, s[80:83], s84 offen offset:0 lds\n"
"v_mfma_f32_16x16x32_bf16 v[64:67], a[24:27], v[140:143], v[64:67]\n"
"ds_read_b64_tr_b16 a[8:9], v25 offset:8192\n"
"v_add_u32 v28, s38, v24\n"
"v_mfma_f32_16x16x32_bf16 v[72:75], a[24:27], v[252:255], v[72:75]\n"
"ds_read_b64_tr_b16 a[10:11], v25 offset:8704\n"
"s_add_u32 s39, s39, 32768\n"
"v_mfma_f32_16x16x32_bf16 v[80:83], a[28:31], v[92:95], v[80:83]\n"
"ds_read_b64_tr_b16 a[12:13], v25 offset:12288\n"
"s_cmp_eq_u32 s39, 163840\n"
"v_mfma_f32_16x16x32_bf16 v[88:91], a[28:31], v[204:207], v[88:91]\n"
"ds_read_b64_tr_b16 a[14:15], v25 offset:12800\n"
"s_cselect_b32 s39, 0, s39\n"
"v_mfma_f32_16x16x32_bf16 v[80:83], a[32:35], v[100:103], v[80:83]\n"
"ds_read_b64_tr_b16 a[16:17], v25 offset:16384\n"
"v_cvt_pk_bf16_f32 v98, v48, v49\n"
"v_mfma_f32_16x16x32_bf16 v[88:91], a[32:35], v[212:215], v[88:91]\n"
"ds_read_b64_tr_b16 a[18:19], v25 offset:16896\n"
"v_cvt_pk_bf16_f32 v99, v50, v51\n"
"v_mfma_f32_16x16x32_bf16 v[80:83], a[36:39], v[108:111], v[80:83]\n"
"ds_read_b64_tr_b16 a[20:21], v25 offset:20480\n"
"s_waitcnt lgkmcnt(11)\n"
"v_mfma_f32_16x16x32_bf16 v[88:91], a[36:39], v[220:223], v[88:91]\n"
"ds_read_b64_tr_b16 a[22:23], v25 offset:20992\n"
"buffer_load_dwordx4 v10, s[80:83], s84 offen offset:1024 lds\n"
"v_mfma_f32_16x16x32_bf16 v[80:83], a[40:43], v[116:119], v[80:83]\n"
"ds_read_b64_tr_b16 a[24:25], v25 offset:24576\n"
"buffer_store_dwordx4 v[96:99], v17, s[28:31], s53 offen offset:0 sc0 sc1\n"
"v_mfma_f32_16x16x32_bf16 v[88:91], a[40:43], v[228:231], v[88:91]\n"
"ds_read_b64_tr_b16 a[26:27], v25 offset:25088\n"
"v_cvt_pk_bf16_f32 v106, v56, v57\n"
"v_mfma_f32_16x16x32_bf16 v[80:83], a[44:47], v[124:127], v[80:83]\n"
"ds_read_b64_tr_b16 a[28:29], v26 offset:0\n"
"v_cvt_pk_bf16_f32 v107, v58, v59\n"
"v_mfma_f32_16x16x32_bf16 v[88:91], a[44:47], v[236:239], v[88:91]\n"
"ds_read_b64_tr_b16 a[30:31], v26 offset:512\n"
"buffer_store_dwordx4 v[104:107], v18, s[28:31], s53 offen offset:0 sc0 sc1\n"
"v_mfma_f32_16x16x32_bf16 v[80:83], a[48:51], v[132:135], v[80:83]\n"
"ds_read_b64_tr_b16 a[32:33], v26 offset:4096\n"
"v_cvt_pk_bf16_f32 v96, v64, v65\n"
"v_mfma_f32_16x16x32_bf16 v[88:91], a[48:51], v[244:247], v[88:91]\n"
"ds_read_b64_tr_b16 a[34:35], v26 offset:4608\n"
"s_sub_u32 s46, s46, 1\n"
"v_mfma_f32_16x16x32_bf16 v[80:83], a[52:55], v[140:143], v[80:83]\n"
"ds_read_b64_tr_b16 a[36:37], v26 offset:8192\n"
"s_waitcnt lgkmcnt(11)\n"
"v_mfma_f32_16x16x32_bf16 v[88:91], a[52:55], v[252:255], v[88:91]\n"
"ds_read_b64_tr_b16 a[38:39], v26 offset:8704\n"
"buffer_load_dwordx4 v11, s[80:83], s84 offen offset:2048 lds\n"
"s_cbranch_scc0 LQ%=\n"
"s_nop 7\n"
"v_cvt_pk_bf16_f32 v97, v66, v67\n"
"v_cvt_pk_bf16_f32 v104, v72, v73\n"
"v_cvt_pk_bf16_f32 v105, v74, v75\n"
"v_cvt_pk_bf16_f32 v98, v80, v81\n"
"v_cvt_pk_bf16_f32 v99, v82, v83\n"
"buffer_store_dwordx4 v[96:99], v17, s[28:31], s53 offen offset:1024 sc0 sc1\n"
"v_cvt_pk_bf16_f32 v106, v88, v89\n"
"v_cvt_pk_bf16_f32 v107, v90, v91\n"
"buffer_store_dwordx4 v[104:107], v18, s[28:31], s53 offen offset:1024 sc0 sc1\n"
"s_branch LE%=\n"
"LH%=:\n"
"s_waitcnt lgkmcnt(15)\n"
"v_mfma_f32_16x16x32_bf16 v[32:35], a[0:3], v[36:39], 0\n"
"ds_read_b64_tr_b16 a[40:41], v26 offset:12288\n"
"v_exp_f32 v112, v64\n"
"v_exp_f32 v113, v65\n"
"v_mfma_f32_16x16x32_bf16 v[40:43], a[0:3], v[148:151], 0\n"
"ds_read_b64_tr_b16 a[42:43], v26 offset:12800\n"
"v_exp_f32 v114, v66\n"
"v_mfma_f32_16x16x32_bf16 v[32:35], a[4:7], v[44:47], v[32:35]\n"
"ds_read_b64_tr_b16 a[44:45], v26 offset:16384\n"
"v_exp_f32 v115, v67\n"
"v_fma_f32 v112, v112, s48, s48\n"
"v_fma_f32 v113, v113, s48, s48\n"
"v_mfma_f32_16x16x32_bf16 v[40:43], a[4:7], v[156:159], v[40:43]\n"
"ds_read_b64_tr_b16 a[46:47], v26 offset:16896\n"
"v_fma_f32 v114, v114, s48, s48\n"
"v_fma_f32 v115, v115, s48, s48\n"
"s_waitcnt lgkmcnt(15)\n"
"v_mfma_f32_16x16x32_bf16 v[32:35], a[8:11], v[52:55], v[32:35]\n"
"ds_read_b64_tr_b16 a[48:49], v26 offset:20480\n"
"v_rcp_f32 v112, v112\n"
"v_rcp_f32 v113, v113\n"
"v_mfma_f32_16x16x32_bf16 v[40:43], a[8:11], v[164:167], v[40:43]\n"
"ds_read_b64_tr_b16 a[50:51], v26 offset:20992\n"
"v_rcp_f32 v114, v114\n"
"v_rcp_f32 v115, v115\n"
"v_mfma_f32_16x16x32_bf16 v[32:35], a[12:15], v[60:63], v[32:35]\n"
"ds_read_b64_tr_b16 a[52:53], v26 offset:24576\n"
"s_waitcnt lgkmcnt(11)\n"
"v_mul_f32 v112, v112, v64\n"
"v_mul_f32 v113, v113, v65\n"
"v_mfma_f32_16x16x32_bf16 v[40:43], a[12:15], v[172:175], v[40:43]\n"
"ds_read_b64_tr_b16 a[54:55], v26 offset:25088\n"
"buffer_load_dwordx4 v12, s[80:83], s84 offen offset:3072 lds\n"
"v_mul_f32 v114, v114, v66\n"
"v_mul_f32 v115, v115, v67\n"
"v_mfma_f32_16x16x32_bf16 v[32:35], a[16:19], v[68:71], v[32:35]\n"
"ds_read_b64_tr_b16 a[0:1], v27 offset:0\n"
"s_add_u32 s38, s38, 32768\n"
"v_cvt_pk_bf16_f32 v96, v112, v113\n"
"v_cvt_pk_bf16_f32 v97, v114, v115\n"
"v_mfma_f32_16x16x32_bf16 v[40:43], a[16:19], v[180:183], v[40:43]\n"
"ds_read_b64_tr_b16 a[2:3], v27 offset:512\n"
"s_add_u32 m0, m0, 0x1000\n"
"v_exp_f32 v120, v72\n"
"v_mfma_f32_16x16x32_bf16 v[32:35], a[20:23], v[76:79], v[32:35]\n"
"ds_read_b64_tr_b16 a[4:5], v27 offset:4096\n"
"s_cmp_eq_u32 s38, 163840\n"
"v_exp_f32 v121, v73\n"
"v_mfma_f32_16x16x32_bf16 v[40:43], a[20:23], v[188:191], v[40:43]\n"
"ds_read_b64_tr_b16 a[6:7], v27 offset:4608\n"
"s_cselect_b32 s38, 0, s38\n"
"v_exp_f32 v122, v74\n"
"v_mfma_f32_16x16x32_bf16 v[32:35], a[24:27], v[84:87], v[32:35]\n"
"ds_read_b64_tr_b16 a[8:9], v27 offset:8192\n"
"v_add_u32 v25, s38, v21\n"
"v_exp_f32 v123, v75\n"
"v_fma_f32 v120, v120, s48, s48\n"
"v_fma_f32 v121, v121, s48, s48\n"
"v_mfma_f32_16x16x32_bf16 v[40:43], a[24:27], v[196:199], v[40:43]\n"
"ds_read_b64_tr_b16 a[10:11], v27 offset:8704\n"
"v_add_u32 v26, s38, v22\n"
"v_fma_f32 v122, v122, s48, s48\n"
"v_fma_f32 v123, v123, s48, s48\n"
"v_mfma_f32_16x16x32_bf16 v[48:51], a[28:31], v[36:39], 0\n"
"ds_read_b64_tr_b16 a[12:13], v27 offset:12288\n"
"s_waitcnt lgkmcnt(11)\n"
"v_rcp_f32 v120, v120\n"
"v_mfma_f32_16x16x32_bf16 v[56:59], a[28:31], v[148:151], 0\n"
"ds_read_b64_tr_b16 a[14:15], v27 offset:12800\n"
"buffer_load_dwordx4 v13, s[80:83], s84 offen offset:0 lds\n"
"v_rcp_f32 v121, v121\n"
"v_mfma_f32_16x16x32_bf16 v[48:51], a[32:35], v[44:47], v[48:51]\n"
"ds_read_b64_tr_b16 a[16:17], v27 offset:16384\n"
"v_rcp_f32 v122, v122\n"
"v_rcp_f32 v123, v123\n"
"v_mfma_f32_16x16x32_bf16 v[56:59], a[32:35], v[156:159], v[56:59]\n"
"ds_read_b64_tr_b16 a[18:19], v27 offset:16896\n"
"v_mul_f32 v120, v120, v72\n"
"v_mul_f32 v121, v121, v73\n"
"v_mul_f32 v122, v122, v74\n"
"v_mfma_f32_16x16x32_bf16 v[48:51], a[36:39], v[52:55], v[48:51]\n"
"ds_read_b64_tr_b16 a[20:21], v27 offset:20480\n"
"v_mul_f32 v123, v123, v75\n"
"v_cvt_pk_bf16_f32 v104, v120, v121\n"
"v_cvt_pk_bf16_f32 v105, v122, v123\n"
"v_mfma_f32_16x16x32_bf16 v[56:59], a[36:39], v[164:167], v[56:59]\n"
"ds_read_b64_tr_b16 a[22:23], v27 offset:20992\n"
"v_exp_f32 v112, v80\n"
"v_mfma_f32_16x16x32_bf16 v[48:51], a[40:43], v[60:63], v[48:51]\n"
"ds_read_b64_tr_b16 a[24:25], v27 offset:24576\n"
"v_exp_f32 v113, v81\n"
"v_exp_f32 v114, v82\n"
"v_mfma_f32_16x16x32_bf16 v[56:59], a[40:43], v[172:175], v[56:59]\n"
"ds_read_b64_tr_b16 a[26:27], v27 offset:25088\n"
"v_exp_f32 v115, v83\n"
"v_fma_f32 v112, v112, s48, s48\n"
"v_fma_f32 v113, v113, s48, s48\n"
"v_mfma_f32_16x16x32_bf16 v[48:51], a[44:47], v[68:71], v[48:51]\n"
"ds_read_b64_tr_b16 a[28:29], v28 offset:0\n"
"s_waitcnt lgkmcnt(11)\n"
"v_fma_f32 v114, v114, s48, s48\n"
"v_fma_f32 v115, v115, s48, s48\n"
"v_mfma_f32_16x16x32_bf16 v[56:59], a[44:47], v[180:183], v[56:59]\n"
"ds_read_b64_tr_b16 a[30:31], v28 offset:512\n"
"buffer_load_dwordx4 v14, s[80:83], s84 offen offset:1024 lds\n"
"v_rcp_f32 v112, v112\n"
"v_mfma_f32_16x16x32_bf16 v[48:51], a[48:51], v[76:79], v[48:51]\n"
"ds_read_b64_tr_b16 a[32:33], v28 offset:4096\n"
"v_add_u32 v27, s38, v23\n"
"v_rcp_f32 v113, v113\n"
"v_mfma_f32_16x16x32_bf16 v[56:59], a[48:51], v[188:191], v[56:59]\n"
"ds_read_b64_tr_b16 a[34:35], v28 offset:4608\n"
"v_rcp_f32 v114, v114\n"
"v_rcp_f32 v115, v115\n"
"v_mfma_f32_16x16x32_bf16 v[48:51], a[52:55], v[84:87], v[48:51]\n"
"ds_read_b64_tr_b16 a[36:37], v28 offset:8192\n"
"v_mul_f32 v112, v112, v80\n"
"v_mul_f32 v113, v113, v81\n"
"v_mul_f32 v114, v114, v82\n"
"v_mfma_f32_16x16x32_bf16 v[56:59], a[52:55], v[196:199], v[56:59]\n"
"ds_read_b64_tr_b16 a[38:39], v28 offset:8704\n"
"v_mul_f32 v115, v115, v83\n"
"v_cvt_pk_bf16_f32 v98, v112, v113\n"
"v_cvt_pk_bf16_f32 v99, v114, v115\n"
"v_mfma_f32_16x16x32_bf16 v[64:67], a[0:3], v[36:39], 0\n"
"ds_read_b64_tr_b16 a[40:41], v28 offset:12288\n"
"buffer_store_dwordx4 v[96:99], v17, s[28:31], s53 offen offset:1024 sc0 sc1\n"
"v_exp_f32 v120, v88\n"
"v_mfma_f32_16x16x32_bf16 v[72:75], a[0:3], v[148:151], 0\n"
"ds_read_b64_tr_b16 a[42:43], v28 offset:12800\n"
"v_exp_f32 v121, v89\n"
"v_exp_f32 v122, v90\n"
"v_mfma_f32_16x16x32_bf16 v[64:67], a[4:7], v[44:47], v[64:67]\n"
"ds_read_b64_tr_b16 a[44:45], v28 offset:16384\n"
"s_waitcnt lgkmcnt(11)\n"
"v_exp_f32 v123, v91\n"
"v_mfma_f32_16x16x32_bf16 v[72:75], a[4:7], v[156:159], v[72:75]\n"
"ds_read_b64_tr_b16 a[46:47], v28 offset:16896\n"
"buffer_load_dwordx4 v15, s[80:83], s84 offen offset:2048 lds\n"
"v_fma_f32 v120, v120, s48, s48\n"
"v_fma_f32 v121, v121, s48, s48\n"
"v_mfma_f32_16x16x32_bf16 v[64:67], a[8:11], v[52:55], v[64:67]\n"
"ds_read_b64_tr_b16 a[48:49], v28 offset:20480\n"
"s_add_u32 s84, s84, s87\n"
"v_fma_f32 v122, v122, s48, s48\n"
"v_fma_f32 v123, v123, s48, s48\n"
"v_mfma_f32_16x16x32_bf16 v[72:75], a[8:11], v[164:167], v[72:75]\n"
"ds_read_b64_tr_b16 a[50:51], v28 offset:20992\n"
"s_add_u32 m0, s39, s85\n"
"v_rcp_f32 v120, v120\n"
"v_mfma_f32_16x16x32_bf16 v[64:67], a[12:15], v[60:63], v[64:67]\n"
"ds_read_b64_tr_b16 a[52:53], v28 offset:24576\n"
"s_sub_u32 s41, s41, 1\n"
"v_rcp_f32 v121, v121\n"
"v_mfma_f32_16x16x32_bf16 v[72:75], a[12:15], v[172:175], v[72:75]\n"
"ds_read_b64_tr_b16 a[54:55], v28 offset:25088\n"
"s_waitcnt vmcnt(22)\n"
"s_barrier\n"
"v_rcp_f32 v122, v122\n"
"v_mfma_f32_16x16x32_bf16 v[64:67], a[16:19], v[68:71], v[64:67]\n"
"ds_read_b64_tr_b16 a[0:1], v25 offset:0\n"
"s_cmp_ge_i32 s41, 0\n"
"v_rcp_f32 v123, v123\n"
"v_mfma_f32_16x16x32_bf16 v[72:75], a[16:19], v[180:183], v[72:75]\n"
"ds_read_b64_tr_b16 a[2:3], v25 offset:512\n"
"s_cselect_b32 s82, s55, 0\n"
"v_mul_f32 v120, v120, v88\n"
"v_mul_f32 v121, v121, v89\n"
"v_mul_f32 v122, v122, v90\n"
"v_mfma_f32_16x16x32_bf16 v[64:67], a[20:23], v[76:79], v[64:67]\n"
"ds_read_b64_tr_b16 a[4:5], v25 offset:4096\n"
"s_waitcnt lgkmcnt(11)\n"
"v_mul_f32 v123, v123, v91\n"
"v_cvt_pk_bf16_f32 v106, v120, v121\n"
"v_mfma_f32_16x16x32_bf16 v[72:75], a[20:23], v[188:191], v[72:75]\n"
"ds_read_b64_tr_b16 a[6:7], v25 offset:4608\n"
"buffer_load_dwordx4 v9, s[80:83], s84 offen offset:0 lds\n"
"v_cvt_pk_bf16_f32 v107, v122, v123\n"
"buffer_store_dwordx4 v[104:107], v18, s[28:31], s53 offen offset:1024 sc0 sc1\n"
"v_mfma_f32_16x16x32_bf16 v[64:67], a[24:27], v[84:87], v[64:67]\n"
"ds_read_b64_tr_b16 a[8:9], v25 offset:8192\n"
"v_add_u32 v28, s38, v24\n"
"v_mfma_f32_16x16x32_bf16 v[72:75], a[24:27], v[196:199], v[72:75]\n"
"ds_read_b64_tr_b16 a[10:11], v25 offset:8704\n"
"s_add_u32 s39, s39, 32768\n"
"v_mfma_f32_16x16x32_bf16 v[80:83], a[28:31], v[36:39], 0\n"
"ds_read_b64_tr_b16 a[12:13], v25 offset:12288\n"
"s_cmp_eq_u32 s39, 163840\n"
"v_mfma_f32_16x16x32_bf16 v[88:91], a[28:31], v[148:151], 0\n"
"ds_read_b64_tr_b16 a[14:15], v25 offset:12800\n"
"s_cselect_b32 s39, 0, s39\n"
"v_mfma_f32_16x16x32_bf16 v[80:83], a[32:35], v[44:47], v[80:83]\n"
"ds_read_b64_tr_b16 a[16:17], v25 offset:16384\n"
"v_mfma_f32_16x16x32_bf16 v[88:91], a[32:35], v[156:159], v[88:91]\n"
"ds_read_b64_tr_b16 a[18:19], v25 offset:16896\n"
"v_mfma_f32_16x16x32_bf16 v[80:83], a[36:39], v[52:55], v[80:83]\n"
"ds_read_b64_tr_b16 a[20:21], v25 offset:20480\n"
"s_waitcnt lgkmcnt(11)\n"
"v_mfma_f32_16x16x32_bf16 v[88:91], a[36:39], v[164:167], v[88:91]\n"
"ds_read_b64_tr_b16 a[22:23], v25 offset:20992\n"
"buffer_load_dwordx4 v10, s[80:83], s84 offen offset:1024 lds\n"
"v_mfma_f32_16x16x32_bf16 v[80:83], a[40:43], v[60:63], v[80:83]\n"
"ds_read_b64_tr_b16 a[24:25], v25 offset:24576\n"
"s_add_u32 s53, s53, 2048\n"
"v_mfma_f32_16x16x32_bf16 v[88:91], a[40:43], v[172:175], v[88:91]\n"
"ds_read_b64_tr_b16 a[26:27], v25 offset:25088\n"
"v_mfma_f32_16x16x32_bf16 v[80:83], a[44:47], v[68:71], v[80:83]\n"
"ds_read_b64_tr_b16 a[28:29], v26 offset:0\n"
"v_mfma_f32_16x16x32_bf16 v[88:91], a[44:47], v[180:183], v[88:91]\n"
"ds_read_b64_tr_b16 a[30:31], v26 offset:512\n"
"s_swappc_b64 s[58:59], s[88:89]\n"
"v_mfma_f32_16x16x32_bf16 v[80:83], a[48:51], v[76:79], v[80:83]\n"
"ds_read_b64_tr_b16 a[32:33], v26 offset:4096\n"
"v_mfma_f32_16x16x32_bf16 v[88:91], a[48:51], v[188:191], v[88:91]\n"
"ds_read_b64_tr_b16 a[34:35], v26 offset:4608\n"
"s_mov_b32 s30, 0x1000000\n"
"v_mfma_f32_16x16x32_bf16 v[80:83], a[52:55], v[84:87], v[80:83]\n"
"ds_read_b64_tr_b16 a[36:37], v26 offset:8192\n"
"s_waitcnt lgkmcnt(11)\n"
"v_mfma_f32_16x16x32_bf16 v[88:91], a[52:55], v[196:199], v[88:91]\n"
"ds_read_b64_tr_b16 a[38:39], v26 offset:8704\n"
"buffer_load_dwordx4 v11, s[80:83], s84 offen offset:2048 lds\n"
"v_mfma_f32_16x16x32_bf16 v[32:35], a[0:3], v[92:95], v[32:35]\n"
"ds_read_b64_tr_b16 a[40:41], v26 offset:12288\n"
"v_mfma_f32_16x16x32_bf16 v[40:43], a[0:3], v[204:207], v[40:43]\n"
"ds_read_b64_tr_b16 a[42:43], v26 offset:12800\n"
"v_mfma_f32_16x16x32_bf16 v[32:35], a[4:7], v[100:103], v[32:35]\n"
"ds_read_b64_tr_b16 a[44:45], v26 offset:16384\n"
"v_mfma_f32_16x16x32_bf16 v[40:43], a[4:7], v[212:215], v[40:43]\n"
"ds_read_b64_tr_b16 a[46:47], v26 offset:16896\n"
"s_swappc_b64 s[58:59], s[88:89]\n"
"v_mfma_f32_16x16x32_bf16 v[32:35], a[8:11], v[108:111], v[32:35]\n"
"ds_read_b64_tr_b16 a[48:49], v26 offset:20480\n"
"v_mfma_f32_16x16x32_bf16 v[40:43], a[8:11], v[220:223], v[40:43]\n"
"ds_read_b64_tr_b16 a[50:51], v26 offset:20992\n"
"v_mfma_f32_16x16x32_bf16 v[32:35], a[12:15], v[116:119], v[32:35]\n"
"ds_read_b64_tr_b16 a[52:53], v26 offset:24576\n"
"s_waitcnt lgkmcnt(11)\n"
"v_mfma_f32_16x16x32_bf16 v[40:43], a[12:15], v[228:231], v[40:43]\n"
"ds_read_b64_tr_b16 a[54:55], v26 offset:25088\n"
"buffer_load_dwordx4 v12, s[80:83], s84 offen offset:3072 lds\n"
"v_mfma_f32_16x16x32_bf16 v[32:35], a[16:19], v[124:127], v[32:35]\n"
"ds_read_b64_tr_b16 a[0:1], v27 offset:0\n"
"s_add_u32 s38, s38, 32768\n"
"v_mfma_f32_16x16x32_bf16 v[40:43], a[16:19], v[236:239], v[40:43]\n"
"ds_read_b64_tr_b16 a[2:3], v27 offset:512\n"
"s_add_u32 m0, m0, 0x1000\n"
"v_mfma_f32_16x16x32_bf16 v[32:35], a[20:23], v[132:135], v[32:35]\n"
"ds_read_b64_tr_b16 a[4:5], v27 offset:4096\n"
"s_cmp_eq_u32 s38, 163840\n"
"v_mfma_f32_16x16x32_bf16 v[40:43], a[20:23], v[244:247], v[40:43]\n"
"ds_read_b64_tr_b16 a[6:7], v27 offset:4608\n"
"s_cselect_b32 s38, 0, s38\n"
"v_mfma_f32_16x16x32_bf16 v[32:35], a[24:27], v[140:143], v[32:35]\n"
"ds_read_b64_tr_b16 a[8:9], v27 offset:8192\n"
"v_add_u32 v25, s38, v21\n"
"v_mfma_f32_16x16x32_bf16 v[40:43], a[24:27], v[252:255], v[40:43]\n"
"ds_read_b64_tr_b16 a[10:11], v27 offset:8704\n"
"v_add_u32 v26, s38, v22\n"
"v_mfma_f32_16x16x32_bf16 v[48:51], a[28:31], v[92:95], v[48:51]\n"
"ds_read_b64_tr_b16 a[12:13], v27 offset:12288\n"
"s_waitcnt lgkmcnt(11)\n"
"v_mfma_f32_16x16x32_bf16 v[56:59], a[28:31], v[204:207], v[56:59]\n"
"ds_read_b64_tr_b16 a[14:15], v27 offset:12800\n"
"buffer_load_dwordx4 v13, s[80:83], s84 offen offset:0 lds\n"
"v_mfma_f32_16x16x32_bf16 v[48:51], a[32:35], v[100:103], v[48:51]\n"
"ds_read_b64_tr_b16 a[16:17], v27 offset:16384\n"
"v_exp_f32 v112, v32\n"
"v_mfma_f32_16x16x32_bf16 v[56:59], a[32:35], v[212:215], v[56:59]\n"
"ds_read_b64_tr_b16 a[18:19], v27 offset:16896\n"
"v_exp_f32 v113, v33\n"
"v_exp_f32 v114, v34\n"
"v_mfma_f32_16x16x32_bf16 v[48:51], a[36:39], v[108:111], v[48:51]\n"
"ds_read_b64_tr_b16 a[20:21], v27 offset:20480\n"
"v_exp_f32 v115, v35\n"
"v_fma_f32 v112, v112, s48, s48\n"
"v_fma_f32 v113, v113, s48, s48\n"
"v_mfma_f32_16x16x32_bf16 v[56:59], a[36:39], v[220:223], v[56:59]\n"
"ds_read_b64_tr_b16 a[22:23], v27 offset:20992\n"
"v_fma_f32 v114, v114, s48, s48\n"
"v_fma_f32 v115, v115, s48, s48\n"
"v_mfma_f32_16x16x32_bf16 v[48:51], a[40:43], v[116:119], v[48:51]\n"
"ds_read_b64_tr_b16 a[24:25], v27 offset:24576\n"
"v_rcp_f32 v112, v112\n"
"v_rcp_f32 v113, v113\n"
"v_mfma_f32_16x16x32_bf16 v[56:59], a[40:43], v[228:231], v[56:59]\n"
"ds_read_b64_tr_b16 a[26:27], v27 offset:25088\n"
"v_rcp_f32 v114, v114\n"
"v_mfma_f32_16x16x32_bf16 v[48:51], a[44:47], v[124:127], v[48:51]\n"
"ds_read_b64_tr_b16 a[28:29], v28 offset:0\n"
"s_waitcnt lgkmcnt(11)\n"
"v_rcp_f32 v115, v115\n"
"v_mul_f32 v112, v112, v32\n"
"v_mul_f32 v113, v113, v33\n"
"v_mfma_f32_16x16x32_bf16 v[56:59], a[44:47], v[236:239], v[56:59]\n"
"ds_read_b64_tr_b16 a[30:31], v28 offset:512\n"
"buffer_load_dwordx4 v14, s[80:83], s84 offen offset:1024 lds\n"
"v_mul_f32 v114, v114, v34\n"
"v_mul_f32 v115, v115, v35\n"
"v_mfma_f32_16x16x32_bf16 v[48:51], a[48:51], v[132:135], v[48:51]\n"
"ds_read_b64_tr_b16 a[32:33], v28 offset:4096\n"
"v_add_u32 v27, s38, v23\n"
"v_cvt_pk_bf16_f32 v96, v112, v113\n"
"v_cvt_pk_bf16_f32 v97, v114, v115\n"
"v_mfma_f32_16x16x32_bf16 v[56:59], a[48:51], v[244:247], v[56:59]\n"
"ds_read_b64_tr_b16 a[34:35], v28 offset:4608\n"
"v_exp_f32 v120, v40\n"
"v_mfma_f32_16x16x32_bf16 v[48:51], a[52:55], v[140:143], v[48:51]\n"
"ds_read_b64_tr_b16 a[36:37], v28 offset:8192\n"
"v_exp_f32 v121, v41\n"
"v_exp_f32 v122, v42\n"
"v_mfma_f32_16x16x32_bf16 v[56:59], a[52:55], v[252:255], v[56:59]\n"
"ds_read_b64_tr_b16 a[38:39], v28 offset:8704\n"
"v_exp_f32 v123, v43\n"
"v_fma_f32 v120, v120, s48, s48\n"
"v_fma_f32 v121, v121, s48, s48\n"
"v_mfma_f32_16x16x32_bf16 v[64:67], a[0:3], v[92:95], v[64:67]\n"
"ds_read_b64_tr_b16 a[40:41], v28 offset:12288\n"
"v_fma_f32 v122, v122, s48, s48\n"
"v_fma_f32 v123, v123, s48, s48\n"
"v_mfma_f32_16x16x32_bf16 v[72:75], a[0:3], v[204:207], v[72:75]\n"
"ds_read_b64_tr_b16 a[42:43], v28 offset:12800\n"
"v_rcp_f32 v120, v120\n"
"v_rcp_f32 v121, v121\n"
"v_mfma_f32_16x16x32_bf16 v[64:67], a[4:7], v[100:103], v[64:67]\n"
"ds_read_b64_tr_b16 a[44:45], v28 offset:16384\n"
"s_waitcnt lgkmcnt(11)\n"
"v_rcp_f32 v122, v122\n"
"v_mfma_f32_16x16x32_bf16 v[72:75], a[4:7], v[212:215], v[72:75]\n"
"ds_read_b64_tr_b16 a[46:47], v28 offset:16896\n"
"buffer_load_dwordx4 v15, s[80:83], s84 offen offset:2048 lds\n"
"v_rcp_f32 v123, v123\n"
"v_mul_f32 v120, v120, v40\n"
"v_mfma_f32_16x16x32_bf16 v[64:67], a[8:11], v[108:111], v[64:67]\n"
"ds_read_b64_tr_b16 a[48:49], v28 offset:20480\n"
"s_add_u32 s84, s84, s56\n"
"v_mul_f32 v121, v121, v41\n"
"v_mul_f32 v122, v122, v42\n"
"v_mfma_f32_16x16x32_bf16 v[72:75], a[8:11], v[220:223], v[72:75]\n"
"ds_read_b64_tr_b16 a[50:51], v28 offset:20992\n"
"s_add_u32 m0, s39, s85\n"
"v_mul_f32 v123, v123, v43\n"
"v_cvt_pk_bf16_f32 v104, v120, v121\n"
"v_mfma_f32_16x16x32_bf16 v[64:67], a[12:15], v[116:119], v[64:67]\n"
"ds_read_b64_tr_b16 a[52:53], v28 offset:24576\n"
"s_sub_u32 s41, s41, 1\n"
"v_cvt_pk_bf16_f32 v105, v122, v123\n"
"v_exp_f32 v112, v48\n"
"v_mfma_f32_16x16x32_bf16 v[72:75], a[12:15], v[228:231], v[72:75]\n"
"ds_read_b64_tr_b16 a[54:55], v28 offset:25088\n"
"s_waitcnt vmcnt(22)\n"
"s_barrier\n"
"v_mfma_f32_16x16x32_bf16 v[64:67], a[16:19], v[124:127], v[64:67]\n"
"ds_read_b64_tr_b16 a[0:1], v25 offset:0\n"
"s_cmp_ge_i32 s41, 0\n"
"v_exp_f32 v113, v49\n"
"v_mfma_f32_16x16x32_bf16 v[72:75], a[16:19], v[236:239], v[72:75]\n"
"ds_read_b64_tr_b16 a[2:3], v25 offset:512\n"
"s_cselect_b32 s82, s55, 0\n"
"v_exp_f32 v114, v50\n"
"v_mfma_f32_16x16x32_bf16 v[64:67], a[20:23], v[132:135], v[64:67]\n"
"ds_read_b64_tr_b16 a[4:5], v25 offset:4096\n"
"s_waitcnt lgkmcnt(11)\n"
"v_exp_f32 v115, v51\n"
"v_fma_f32 v112, v112, s48, s48\n"
"v_mfma_f32_16x16x32_bf16 v[72:75], a[20:23], v[244:247], v[72:75]\n"
"ds_read_b64_tr_b16 a[6:7], v25 offset:4608\n"
"buffer_load_dwordx4 v9, s[80:83], s84 offen offset:0 lds\n"
"v_fma_f32 v113, v113, s48, s48\n"
"v_fma_f32 v114, v114, s48, s48\n"
"v_fma_f32 v115, v115, s48, s48\n"
"v_mfma_f32_16x16x32_bf16 v[64:67], a[24:27], v[140:143], v[64:67]\n"
"ds_read_b64_tr_b16 a[8:9], v25 offset:8192\n"
"v_add_u32 v28, s38, v24\n"
"v_rcp_f32 v112, v112\n"
"v_mfma_f32_16x16x32_bf16 v[72:75], a[24:27], v[252:255], v[72:75]\n"
"ds_read_b64_tr_b16 a[10:11], v25 offset:8704\n"
"s_add_u32 s39, s39, 32768\n"
"v_rcp_f32 v113, v113\n"
"v_mfma_f32_16x16x32_bf16 v[80:83], a[28:31], v[92:95], v[80:83]\n"
"ds_read_b64_tr_b16 a[12:13], v25 offset:12288\n"
"s_cmp_eq_u32 s39, 163840\n"
"v_rcp_f32 v114, v114\n"
"v_mfma_f32_16x16x32_bf16 v[88:91], a[28:31], v[204:207], v[88:91]\n"
"ds_read_b64_tr_b16 a[14:15], v25 offset:12800\n"
"s_cselect_b32 s39, 0, s39\n"
"v_rcp_f32 v115, v115\n"
"v_mul_f32 v112, v112, v48\n"
"v_mfma_f32_16x16x32_bf16 v[80:83], a[32:35], v[100:103], v[80:83]\n"
"ds_read_b64_tr_b16 a[16:17], v25 offset:16384\n"
"v_mul_f32 v113, v113, v49\n"
"v_mul_f32 v114, v114, v50\n"
"v_mul_f32 v115, v115, v51\n"
"v_mfma_f32_16x16x32_bf16 v[88:91], a[32:35], v[212:215], v[88:91]\n"
"ds_read_b64_tr_b16 a[18:19], v25 offset:16896\n"
"v_cvt_pk_bf16_f32 v98, v112, v113\n"
"v_cvt_pk_bf16_f32 v99, v114, v115\n"
"buffer_store_dwordx4 v[96:99], v17, s[28:31], s53 offen offset:0 sc0 sc1\n"
"v_mfma_f32_16x16x32_bf16 v[80:83], a[36:39], v[108:111], v[80:83]\n"
"ds_read_b64_tr_b16 a[20:21], v25 offset:20480\n"
"s_waitcnt lgkmcnt(11)\n"
"v_exp_f32 v120, v56\n"
"v_mfma_f32_16x16x32_bf16 v[88:91], a[36:39], v[220:223], v[88:91]\n"
"ds_read_b64_tr_b16 a[22:23], v25 offset:20992\n"
"buffer_load_dwordx4 v10, s[80:83], s84 offen offset:1024 lds\n"
"v_exp_f32 v121, v57\n"
"v_mfma_f32_16x16x32_bf16 v[80:83], a[40:43], v[116:119], v[80:83]\n"
"ds_read_b64_tr_b16 a[24:25], v25 offset:24576\n"
"v_exp_f32 v122, v58\n"
"v_exp_f32 v123, v59\n"
"v_mfma_f32_16x16x32_bf16 v[88:91], a[40:43], v[228:231], v[88:91]\n"
"ds_read_b64_tr_b16 a[26:27], v25 offset:25088\n"
"v_fma_f32 v120, v120, s48, s48\n"
"v_fma_f32 v121, v121, s48, s48\n"
"v_fma_f32 v122, v122, s48, s48\n"
"v_mfma_f32_16x16x32_bf16 v[80:83], a[44:47], v[124:127], v[80:83]\n"
"ds_read_b64_tr_b16 a[28:29], v26 offset:0\n"
"v_fma_f32 v123, v123, s48, s48\n"
"v_rcp_f32 v120, v120\n"
"v_mfma_f32_16x16x32_bf16 v[88:91], a[44:47], v[236:239], v[88:91]\n"
"ds_read_b64_tr_b16 a[30:31], v26 offset:512\n"
"v_rcp_f32 v121, v121\n"
"v_rcp_f32 v122, v122\n"
"v_mfma_f32_16x16x32_bf16 v[80:83], a[48:51], v[132:135], v[80:83]\n"
"ds_read_b64_tr_b16 a[32:33], v26 offset:4096\n"
"v_rcp_f32 v123, v123\n"
"v_mul_f32 v120, v120, v56\n"
"v_mfma_f32_16x16x32_bf16 v[88:91], a[48:51], v[244:247], v[88:91]\n"
"ds_read_b64_tr_b16 a[34:35], v26 offset:4608\n"
"s_sub_u32 s46, s46, 1\n"
"v_mul_f32 v121, v121, v57\n"
"v_mul_f32 v122, v122, v58\n"
"v_mfma_f32_16x16x32_bf16 v[80:83], a[52:55], v[140:143], v[80:83]\n"
"ds_read_b64_tr_b16 a[36:37], v26 offset:8192\n"
"s_waitcnt lgkmcnt(11)\n"
"v_mul_f32 v123, v123, v59\n"
"v_cvt_pk_bf16_f32 v106, v120, v121\n"
"v_mfma_f32_16x16x32_bf16 v[88:91], a[52:55], v[252:255], v[88:91]\n"
"ds_read_b64_tr_b16 a[38:39], v26 offset:8704\n"
"buffer_load_dwordx4 v11, s[80:83], s84 offen offset:2048 lds\n"
"v_cvt_pk_bf16_f32 v107, v122, v123\n"
"buffer_store_dwordx4 v[104:107], v18, s[28:31], s53 offen offset:0 sc0 sc1\n"
"s_cbranch_scc0 LH%=\n"
"s_nop 7\n"
"v_exp_f32 v112, v64\n"
"v_exp_f32 v113, v65\n"
"v_exp_f32 v114, v66\n"
"v_exp_f32 v115, v67\n"
"v_fma_f32 v112, v112, s48, s48\n"
"v_fma_f32 v113, v113, s48, s48\n"
"v_fma_f32 v114, v114, s48, s48\n"
"v_fma_f32 v115, v115, s48, s48\n"
"v_rcp_f32 v112, v112\n"
"v_rcp_f32 v113, v113\n"
"v_rcp_f32 v114, v114\n"
"v_rcp_f32 v115, v115\n"
"v_mul_f32 v112, v112, v64\n"
"v_mul_f32 v113, v113, v65\n"
"v_mul_f32 v114, v114, v66\n"
"v_mul_f32 v115, v115, v67\n"
"v_cvt_pk_bf16_f32 v96, v112, v113\n"
"v_cvt_pk_bf16_f32 v97, v114, v115\n"
"v_exp_f32 v120, v72\n"
"v_exp_f32 v121, v73\n"
"v_exp_f32 v122, v74\n"
"v_exp_f32 v123, v75\n"
"v_fma_f32 v120, v120, s48, s48\n"
"v_fma_f32 v121, v121, s48, s48\n"
"v_fma_f32 v122, v122, s48, s48\n"
"v_fma_f32 v123, v123, s48, s48\n"
"v_rcp_f32 v120, v120\n"
"v_rcp_f32 v121, v121\n"
"v_rcp_f32 v122, v122\n"
"v_rcp_f32 v123, v123\n"
"v_mul_f32 v120, v120, v72\n"
"v_mul_f32 v121, v121, v73\n"
"v_mul_f32 v122, v122, v74\n"
"v_mul_f32 v123, v123, v75\n"
"v_cvt_pk_bf16_f32 v104, v120, v121\n"
"v_cvt_pk_bf16_f32 v105, v122, v123\n"
"v_exp_f32 v112, v80\n"
"v_exp_f32 v113, v81\n"
"v_exp_f32 v114, v82\n"
"v_exp_f32 v115, v83\n"
"v_fma_f32 v112, v112, s48, s48\n"
"v_fma_f32 v113, v113, s48, s48\n"
"v_fma_f32 v114, v114, s48, s48\n"
"v_fma_f32 v115, v115, s48, s48\n"
"v_rcp_f32 v112, v112\n"
"v_rcp_f32 v113, v113\n"
"v_rcp_f32 v114, v114\n"
"v_rcp_f32 v115, v115\n"
"v_mul_f32 v112, v112, v80\n"
"v_mul_f32 v113, v113, v81\n"
"v_mul_f32 v114, v114, v82\n"
"v_mul_f32 v115, v115, v83\n"
"v_cvt_pk_bf16_f32 v98, v112, v113\n"
"v_cvt_pk_bf16_f32 v99, v114, v115\n"
"buffer_store_dwordx4 v[96:99], v17, s[28:31], s53 offen offset:1024 sc0 sc1\n"
"v_exp_f32 v120, v88\n"
"v_exp_f32 v121, v89\n"
"v_exp_f32 v122, v90\n"
"v_exp_f32 v123, v91\n"
"v_fma_f32 v120, v120, s48, s48\n"
"v_fma_f32 v121, v121, s48, s48\n"
"v_fma_f32 v122, v122, s48, s48\n"
"v_fma_f32 v123, v123, s48, s48\n"
"v_rcp_f32 v120, v120\n"
"v_rcp_f32 v121, v121\n"
"v_rcp_f32 v122, v122\n"
"v_rcp_f32 v123, v123\n"
"v_mul_f32 v120, v120, v88\n"
"v_mul_f32 v121, v121, v89\n"
"v_mul_f32 v122, v122, v90\n"
"v_mul_f32 v123, v123, v91\n"
"v_cvt_pk_bf16_f32 v106, v120, v121\n"
"v_cvt_pk_bf16_f32 v107, v122, v123\n"
"buffer_store_dwordx4 v[104:107], v18, s[28:31], s53 offen offset:1024 sc0 sc1\n"
"s_branch LE%=\n"
"LSTB%=:\n"
"buffer_store_dwordx4 v[144:147], v19, s[76:79], 0 offen offset:2048 sc0 sc1\n"
"buffer_store_dwordx4 v[152:155], v19, s[76:79], 0 offen offset:3072 sc0 sc1\n"
"s_swappc_b64 s[88:89], s[58:59]\n"
"buffer_store_dwordx4 v[160:163], v19, s[76:79], s57 offen offset:0 sc0 sc1\n"
"buffer_store_dwordx4 v[168:171], v19, s[76:79], s57 offen offset:1024 sc0 sc1\n"
"s_swappc_b64 s[88:89], s[58:59]\n"
"buffer_store_dwordx4 v[176:179], v19, s[76:79], s57 offen offset:2048 sc0 sc1\n"
"buffer_store_dwordx4 v[184:187], v19, s[76:79], s57 offen offset:3072 sc0 sc1\n"
"s_swappc_b64 s[88:89], s[58:59]\n"
"buffer_store_dwordx4 v[192:195], v19, s[76:79], s75 offen offset:0 sc0 sc1\n"
"buffer_store_dwordx4 v[200:203], v19, s[76:79], s75 offen offset:1024 sc0 sc1\n"
"s_swappc_b64 s[88:89], s[58:59]\n"
"buffer_store_dwordx4 v[208:211], v19, s[76:79], s75 offen offset:2048 sc0 sc1\n"
"buffer_store_dwordx4 v[216:219], v19, s[76:79], s75 offen offset:3072 sc0 sc1\n"
"s_swappc_b64 s[88:89], s[58:59]\n"
"buffer_store_dwordx4 v[224:227], v19, s[76:79], s97 offen offset:0 sc0 sc1\n"
"buffer_store_dwordx4 v[232:235], v19, s[76:79], s97 offen offset:1024 sc0 sc1\n"
"s_swappc_b64 s[88:89], s[58:59]\n"
"buffer_store_dwordx4 v[240:243], v19, s[76:79], s97 offen offset:2048 sc0 sc1\n"
"buffer_store_dwordx4 v[248:251], v19, s[76:79], s97 offen offset:3072 sc0 sc1\n"
"s_mov_b32 s78, 0\n"
"s_swappc_b64 s[88:89], s[58:59]\n"
"LSTBT%=:\n"
"buffer_store_dwordx4 v[144:147], v19, s[76:79], 0 offen offset:0 sc0 sc1\n"
"buffer_store_dwordx4 v[144:147], v19, s[76:79], 0 offen offset:0 sc0 sc1\n"
"s_swappc_b64 s[88:89], s[58:59]\n"
"s_branch LSTBT%=\n"
"LSTQ%=:\n"
"buffer_store_dwordx4 v[144:147], v19, s[76:79], 0 offen offset:2048 sc0 sc1\n"
"buffer_store_dwordx4 v[152:155], v19, s[76:79], 0 offen offset:3072 sc0 sc1\n"
"s_swappc_b64 s[88:89], s[58:59]\n"
"buffer_store_dwordx4 v[160:163], v19, s[76:79], s57 offen offset:0 sc0 sc1\n"
"buffer_store_dwordx4 v[168:171], v19, s[76:79], s57 offen offset:1024 sc0 sc1\n"
"s_swappc_b64 s[88:89], s[58:59]\n"
"buffer_store_dwordx4 v[176:179], v19, s[76:79], s57 offen offset:2048 sc0 sc1\n"
"s_swappc_b64 s[88:89], s[58:59]\n"
"buffer_store_dwordx4 v[184:187], v19, s[76:79], s57 offen offset:3072 sc0 sc1\n"
"s_swappc_b64 s[88:89], s[58:59]\n"
"buffer_store_dwordx4 v[192:195], v19, s[76:79], s75 offen offset:0 sc0 sc1\n"
"s_swappc_b64 s[88:89], s[58:59]\n"
"buffer_store_dwordx4 v[200:203], v19, s[76:79], s75 offen offset:1024 sc0 sc1\n"
"s_swappc_b64 s[88:89], s[58:59]\n"
"buffer_store_dwordx4 v[208:211], v19, s[76:79], s75 offen offset:2048 sc0 sc1\n"
"s_swappc_b64 s[88:89], s[58:59]\n"
"buffer_store_dwordx4 v[216:219], v19, s[76:79], s75 offen offset:3072 sc0 sc1\n"
"s_swappc_b64 s[88:89], s[58:59]\n"
"buffer_store_dwordx4 v[224:227], v19, s[76:79], s97 offen offset:0 sc0 sc1\n"
"s_swappc_b64 s[88:89], s[58:59]\n"
"buffer_store_dwordx4 v[232:235], v19, s[76:79], s97 offen offset:1024 sc0 sc1\n"
"s_swappc_b64 s[88:89], s[58:59]\n"
"buffer_store_dwordx4 v[240:243], v19, s[76:79], s97 offen offset:2048 sc0 sc1\n"
"s_swappc_b64 s[88:89], s[58:59]\n"
"buffer_store_dwordx4 v[248:251], v19, s[76:79], s97 offen offset:3072 sc0 sc1\n"
"s_mov_b32 s78, 0\n"
"s_swappc_b64 s[88:89], s[58:59]\n"
"LSTQT%=:\n"
"buffer_store_dwordx4 v[144:147], v19, s[76:79], 0 offen offset:0 sc0 sc1\n"
"s_swappc_b64 s[88:89], s[58:59]\n"
"s_branch LSTQT%=\n"
"LE%=:\n"
  :: "s"(x),"s"(c),"s"(wq),"s"(wk),"s"(wv),"s"(w1),"s"(ws),"s"(bid),"{v0}"(tid) : "v1","v2","v3","v4","v5","v6","v7","v8","v9","v10","v11","v12","v13","v14","v15","v16","v17","v18","v19","v20","v21","v22","v23","v24","v25","v26","v27","v28","v29","v30","v31","v32","v33","v34","v35","v36","v37","v38","v39","v40","v41","v42","v43","v44","v45","v46","v47","v48","v49","v50","v51","v52","v53","v54","v55","v56","v57","v58","v59","v60","v61","v62","v63","v64","v65","v66","v67","v68","v69","v70","v71","v72","v73","v74","v75","v76","v77","v78","v79","v80","v81","v82","v83","v84","v85","v86","v87","v88","v89","v90","v91","v92","v93","v94","v95","v96","v97","v98","v99","v100","v101","v102","v103","v104","v105","v106","v107","v108","v109","v110","v111","v112","v113","v114","v115","v116","v117","v118","v119","v120","v121","v122","v123","v124","v125","v126","v127","v128","v129","v130","v131","v132","v133","v134","v135","v136","v137","v138","v139","v140","v141","v142","v143","v144","v145","v146","v147","v148","v149","v150","v151","v152","v153","v154","v155","v156","v157","v158","v159","v160","v161","v162","v163","v164","v165","v166","v167","v168","v169","v170","v171","v172","v173","v174","v175","v176","v177","v178","v179","v180","v181","v182","v183","v184","v185","v186","v187","v188","v189","v190","v191","v192","v193","v194","v195","v196","v197","v198","v199","v200","v201","v202","v203","v204","v205","v206","v207","v208","v209","v210","v211","v212","v213","v214","v215","v216","v217","v218","v219","v220","v221","v222","v223","v224","v225","v226","v227","v228","v229","v230","v231","v232","v233","v234","v235","v236","v237","v238","v239","v240","v241","v242","v243","v244","v245","v246","v247","v248","v249","v250","v251","v252","v253","v254","v255","a0","a1","a2","a3","a4","a5","a6","a7","a8","a9","a10","a11","a12","a13","a14","a15","a16","a17","a18","a19","a20","a21","a22","a23","a24","a25","a26","a27","a28","a29","a30","a31","a32","a33","a34","a35","a36","a37","a38","a39","a40","a41","a42","a43","a44","a45","a46","a47","a48","a49","a50","a51","a52","a53","a54","a55","a56","a57","a58","a59","a60","a61","a62","a63","a64","a65","a66","a67","a68","a69","a70","a71","a72","a73","a74","a75","a76","a77","a78","a79","a80","a81","a82","a83","a84","a85","a86","a87","a88","a89","a90","a91","a92","a93","a94","a95","a96","a97","a98","a99","a100","a101","a102","a103","a104","a105","a106","a107","a108","a109","a110","a111","a112","a113","a114","a115","a116","a117","a118","a119","a120","a121","a122","a123","a124","a125","a126","a127","a128","a129","a130","a131","a132","a133","a134","a135","a136","a137","a138","a139","a140","a141","a142","a143","a144","a145","a146","a147","a148","a149","a150","a151","a152","a153","a154","a155","a156","a157","a158","a159","a160","a161","a162","a163","a164","a165","a166","a167","a168","a169","a170","a171","a172","a173","a174","a175","a176","a177","a178","a179","a180","a181","a182","a183","a184","a185","a186","a187","a188","a189","a190","a191","a192","a193","a194","a195","a196","a197","a198","a199","a200","a201","a202","a203","a204","a205","a206","a207","a208","a209","a210","a211","a212","a213","a214","a215","a216","a217","a218","a219","a220","a221","a222","a223","a224","a225","a226","a227","a228","a229","a230","a231","a232","a233","a234","a235","a236","a237","a238","a239","a240","a241","a242","a243","a244","a245","a246","a247","a248","a249","a250","a251","a252","a253","a254","a255","s20","s21","s22","s23","s24","s25","s26","s27","s28","s29","s30","s31","s32","s33","s34","s35","s36","s37","s38","s39","s40","s41","s42","s43","s44","s45","s46","s47","s48","s49","s50","s51","s52","s53","s54","s55","s56","s57","s58","s59","s60","s61","s62","s63","s64","s65","s66","s67","s68","s69","s70","s71","s72","s73","s74","s75","s76","s77","s78","s79","s80","s81","s82","s83","s84","s85","s86","s87","s88","s89","s90","s91","s92","s93","s94","s95","s96","s97","s98","s99","vcc","scc","memory");
}

__global__ __launch_bounds__(256,1) void k2_attn(const char* wo,const char* w2,char* ws,const char* x,char* out){
  unsigned tid=threadIdx.x, b=blockIdx.x, lane=tid&63;
  unsigned w=__builtin_amdgcn_readfirstlane(tid>>6);
  unsigned j=b>>3, h=2*(b&7)+(j&1), i=j>>2, ty=(j>>1)&1;
  unsigned b1=15-i, T1=ty?17:0, n1=ty?15-2*i:17, n2=ty?2*i+2:0, bf=ty?i:b1;
  unsigned p=lane, T0=T1, n=n1; if(p>=2*n1){p-=2*n1; T0=0; n=n2;}
  bool isK=(p==0)||((p&1)&&p!=2*n-1);
  unsigned tl=isK?((p+1)>>1):((p&1)?(p>>1):(p>>1)-1);
  unsigned tab=(isK?0u:16777216u)+(h<<20)+(((T0+tl)&31)<<15);
  unsigned g=lane>>4, r=lane&15;
  float mb=(float)((int)(4*g)-(int)r-(int)(16*w));
  unsigned ed=(lane>>3)*896+((((lane>>1)&3)^((lane>>4)&3))<<5)+(lane&1)*16;
  unsigned et=(4*g+(r>>2))*128+(((2*g+(r>>3))&3)<<5)+(r&3)*8;
  unsigned fs0=(7*h+1)>>1, nfs=(h&1)?3:4;
  const char* woh=wo+(size_t)h*256*896; const char* w2h=w2+(size_t)fs0*32*896; unsigned w2rec=(1792-fs0*32)*896;
  const char* w0=woh+w*48*896; const char* wa=w<2?woh+(192+48*w)*896:w2h+(48*w-64)*896; const char* wb=w==1?w2h-16*896:wa;
  unsigned recB=w==0?64*896:w2rec+64*896-48*896*w, recA=w<2?(64-48*w)*896:recB;
  unsigned qoff1=33554432u+((h*128+b1*8+w)<<13), qoff2=33554432u+((h*128+i*8+w)<<13);
  int rel1=64*(int)T1-128*(int)b1, rel2=-128*(int)i;
  unsigned obo=88080384u+((h*8+i)*4+w)*17408, flo=100663296u+((h*8+i)*4+w)*128;
  unsigned hoff=50331648u+(((bf*8+w)*56+fs0)<<10), poff=58720256u+(((h*128+bf*8+w)*14)<<10);
  unsigned dbgo=104857600u+65536+(b*4+w)*64, pf=(j>>1)*896;
  unsigned rfl=100663296u+bf*4096+64, cA=(h&1)*4+w, cB=8+(h&1)*3+(w<3?w:0);
  unsigned pb=58720256u+(((bf*8+(h>>1))*14)<<10), xb=(bf*128+(h>>1)*16)*896;
  if(lane==40) tab=rfl+h*4;
  if(lane==41) tab=rfl;
  if(lane==42) tab=n1==1?0x7fffffffu:(unsigned)(rel1+64*(int)(ty?n1-2:2*i+1));
  if(lane==44) tab=recA;
  if(lane==45) tab=recB;
  if(lane==46) tab=pb+cA*1024;
  if(lane==47) tab=xb+cA*64;
  if(lane==48) tab=pb+cB*1024;
  if(lane==49) tab=xb+cB*64;
  asm volatile(
"s_mov_b64 s[40:41], %0\n"
"s_and_b32 s41, s41, 0xffff\n"
"s_mov_b32 s71, 0x8000000\n"
"s_mov_b32 s42, s71\n"
"s_mov_b32 s43, 0x20000\n"
"s_mov_b64 s[52:53], s[40:41]\n"
"s_mov_b64 s[54:55], s[42:43]\n"
"s_lshl_b32 s62, %15, 13\n"
"s_mov_b32 s58, 0\n"
"s_mov_b32 s60, 0\n"
"s_mov_b32 s59, 0\n"
"v_add_u32 v6, 0x1000, v1\n"
"v_mov_b32 v7, v0\n"
"s_mov_b32 s67, %6\n"
"v_readlane_b32 s61, v2, s58\n"
"s_cmp_lt_u32 s58, 34\n"
"s_cselect_b32 s42, s71, 0\n"
"s_add_u32 s58, s58, 1\n"
"s_add_u32 m0, s60, s62\n"
"s_nop 1\n"
"buffer_load_dwordx4 v1, s[40:43], s61 offen offset:0 lds\n"
"buffer_load_dwordx4 v1, s[40:43], s61 offen offset:1024 lds\n"
"buffer_load_dwordx4 v1, s[40:43], s61 offen offset:2048 lds\n"
"buffer_load_dwordx4 v1, s[40:43], s61 offen offset:3072 lds\n"
"s_add_u32 m0, m0, 0x1000\n"
"s_nop 0\n"
"buffer_load_dwordx4 v6, s[40:43], s61 offen offset:0 lds\n"
"buffer_load_dwordx4 v6, s[40:43], s61 offen offset:1024 lds\n"
"buffer_load_dwordx4 v6, s[40:43], s61 offen offset:2048 lds\n"
"buffer_load_dwordx4 v6, s[40:43], s61 offen offset:3072 lds\n"
"s_add_u32 s60, s60, 32768\n"
"s_cmp_eq_u32 s60, 163840\n"
"s_cselect_b32 s60, 0, s60\n"
"s_add_u32 s65, s67, 4096\n"
"s_add_u32 s66, s67, 32768\n"
"s_add_u32 s70, s67, 36864\n"
"buffer_load_dwordx4 a[0:3], v0, s[52:55], s67 offen offset:0\n"
"buffer_load_dwordx4 a[4:7], v0, s[52:55], s67 offen offset:1024\n"
"buffer_load_dwordx4 a[8:11], v0, s[52:55], s67 offen offset:2048\n"
"buffer_load_dwordx4 a[12:15], v0, s[52:55], s67 offen offset:3072\n"
"buffer_load_dwordx4 a[16:19], v0, s[52:55], s65 offen offset:0\n"
"buffer_load_dwordx4 a[20:23], v0, s[52:55], s65 offen offset:1024\n"
"buffer_load_dwordx4 a[24:27], v0, s[52:55], s65 offen offset:2048\n"
"buffer_load_dwordx4 a[28:31], v0, s[52:55], s65 offen offset:3072\n"
"buffer_load_dwordx4 a[32:35], v0, s[52:55], s66 offen offset:0\n"
"buffer_load_dwordx4 a[36:39], v0, s[52:55], s66 offen offset:1024\n"
"buffer_load_dwordx4 a[40:43], v0, s[52:55], s66 offen offset:2048\n"
"buffer_load_dwordx4 a[44:47], v0, s[52:55], s66 offen offset:3072\n"
"buffer_load_dwordx4 a[48:51], v0, s[52:55], s70 offen offset:0\n"
"buffer_load_dwordx4 a[52:55], v0, s[52:55], s70 offen offset:1024\n"
"buffer_load_dwordx4 a[56:59], v0, s[52:55], s70 offen offset:2048\n"
"buffer_load_dwordx4 a[60:63], v0, s[52:55], s70 offen offset:3072\n"
"v_readlane_b32 s61, v2, s58\n"
"s_cmp_lt_u32 s58, 34\n"
"s_cselect_b32 s42, s71, 0\n"
"s_add_u32 s58, s58, 1\n"
"s_add_u32 m0, s60, s62\n"
"s_nop 1\n"
"buffer_load_dwordx4 v1, s[40:43], s61 offen offset:0 lds\n"
"buffer_load_dwordx4 v1, s[40:43], s61 offen offset:1024 lds\n"
"buffer_load_dwordx4 v1, s[40:43], s61 offen offset:2048 lds\n"
"buffer_load_dwordx4 v1, s[40:43], s61 offen offset:3072 lds\n"
"s_add_u32 m0, m0, 0x1000\n"
"s_nop 0\n"
"buffer_load_dwordx4 v6, s[40:43], s61 offen offset:0 lds\n"
"buffer_load_dwordx4 v6, s[40:43], s61 offen offset:1024 lds\n"
"buffer_load_dwordx4 v6, s[40:43], s61 offen offset:2048 lds\n"
"buffer_load_dwordx4 v6, s[40:43], s61 offen offset:3072 lds\n"
"s_add_u32 s60, s60, 32768\n"
"s_cmp_eq_u32 s60, 163840\n"
"s_cselect_b32 s60, 0, s60\n"
"v_readlane_b32 s61, v2, s58\n"
"s_cmp_lt_u32 s58, 34\n"
"s_cselect_b32 s42, s71, 0\n"
"s_add_u32 s58, s58, 1\n"
"s_add_u32 m0, s60, s62\n"
"s_nop 1\n"
"buffer_load_dwordx4 v1, s[40:43], s61 offen offset:0 lds\n"
"buffer_load_dwordx4 v1, s[40:43], s61 offen offset:1024 lds\n"
"buffer_load_dwordx4 v1, s[40:43], s61 offen offset:2048 lds\n"
"buffer_load_dwordx4 v1, s[40:43], s61 offen offset:3072 lds\n"
"s_add_u32 m0, m0, 0x1000\n"
"s_nop 0\n"
"buffer_load_dwordx4 v6, s[40:43], s61 offen offset:0 lds\n"
"buffer_load_dwordx4 v6, s[40:43], s61 offen offset:1024 lds\n"
"buffer_load_dwordx4 v6, s[40:43], s61 offen offset:2048 lds\n"
"buffer_load_dwordx4 v6, s[40:43], s61 offen offset:3072 lds\n"
"s_add_u32 s60, s60, 32768\n"
"s_cmp_eq_u32 s60, 163840\n"
"s_cselect_b32 s60, 0, s60\n"
"v_readlane_b32 s61, v2, s58\n"
"s_cmp_lt_u32 s58, 34\n"
"s_cselect_b32 s42, s71, 0\n"
"s_add_u32 s58, s58, 1\n"
"s_add_u32 m0, s60, s62\n"
"s_nop 1\n"
"buffer_load_dwordx4 v1, s[40:43], s61 offen offset:0 lds\n"
"buffer_load_dwordx4 v1, s[40:43], s61 offen offset:1024 lds\n"
"buffer_load_dwordx4 v1, s[40:43], s61 offen offset:2048 lds\n"
"s_add_u32 s60, s60, 32768\n"
"s_cmp_eq_u32 s60, 163840\n"
"s_cselect_b32 s60, 0, s60\n"
"v_readlane_b32 s50, v2, 44\n"
"v_readlane_b32 s98, v2, 45\n"
"s_mov_b64 s[44:45], %1\n"
"s_and_b32 s45, s45, 0xffff\n"
"s_mov_b32 s47, 0x20000\n"
"s_mov_b64 s[48:49], %2\n"
"s_and_b32 s49, s49, 0xffff\n"
"s_mov_b32 s51, 0x20000\n"
"s_mov_b64 s[96:97], %3\n"
"s_and_b32 s97, s97, 0xffff\n"
"s_mov_b32 s99, 0x20000\n"
"s_mov_b32 s46, 43008\n"
"v_lshlrev_b32 v13, 2, v0\n"
"v_lshlrev_b32 v14, 1, v0\n"
"s_mul_i32 s65, %17, 3\n"
"s_lshl_b32 s66, %17, 1\n"
"s_add_u32 s66, s66, 14336\n"
"buffer_load_dword a172, v13, s[44:47], s65 offen\n"
"buffer_load_dword a173, v14, s[96:99], s66 offen\n"
"buffer_load_dword a174, v0, s[48:51], %17 offen\n"
"s_mov_b32 s56, %4\n"
"s_mov_b32 s63, %8\n"
"s_mov_b32 s64, %5\n"
"s_mov_b32 s68, 0\n"
"v_mov_b32 v12, 0x3f803f80\n"
"v_accvgpr_write_b32 a136, v12\n"
"v_accvgpr_write_b32 a137, v12\n"
"v_accvgpr_write_b32 a138, v12\n"
"v_accvgpr_write_b32 a139, v12\n"
"s_add_u32 s82, %12, 229376\n"
"s_mov_b32 s67, %7\n"
"s_add_u32 s65, s67, 4096\n"
"s_add_u32 s66, s67, 32768\n"
"s_add_u32 s70, s67, 36864\n"
"s_mov_b64 s[92:93], s[52:53]\n"
"s_mov_b32 s95, 0x20000\n"
"s_cmp_eq_u32 s64, 0\n"
"s_cselect_b32 s67, %12, s67\n"
"s_cselect_b32 s65, s82, s65\n"
"s_cselect_b32 s66, s71, s66\n"
"s_cselect_b32 s70, s71, s70\n"
"v_readlane_b32 s89, v2, 42\n"
"v_mov_b64 v[112:113], 0\n"
"v_mov_b64 v[114:115], 0\n"
"v_mov_b64 v[116:117], 0\n"
"v_mov_b64 v[118:119], 0\n"
"v_mov_b64 v[120:121], 0\n"
"v_mov_b64 v[122:123], 0\n"
"v_mov_b64 v[124:125], 0\n"
"v_mov_b64 v[126:127], 0\n"
"v_mov_b64 v[128:129], 0\n"
"v_mov_b64 v[130:131], 0\n"
"v_mov_b64 v[132:133], 0\n"
"v_mov_b64 v[134:135], 0\n"
"v_mov_b64 v[136:137], 0\n"
"v_mov_b64 v[138:139], 0\n"
"v_mov_b64 v[140:141], 0\n"
"v_mov_b64 v[142:143], 0\n"
"v_mov_b64 v[144:145], 0\n"
"v_mov_b64 v[146:147], 0\n"
"v_mov_b64 v[148:149], 0\n"
"v_mov_b64 v[150:151], 0\n"
"v_mov_b64 v[152:153], 0\n"
"v_mov_b64 v[154:155], 0\n"
"v_mov_b64 v[156:157], 0\n"
"v_mov_b64 v[158:159], 0\n"
"v_mov_b64 v[160:161], 0\n"
"v_mov_b64 v[162:163], 0\n"
"v_mov_b64 v[164:165], 0\n"
"v_mov_b64 v[166:167], 0\n"
"v_mov_b64 v[168:169], 0\n"
"v_mov_b64 v[170:171], 0\n"
"v_mov_b64 v[172:173], 0\n"
"v_mov_b64 v[174:175], 0\n"
"v_mov_b64 v[176:177], 0\n"
"v_mov_b64 v[178:179], 0\n"
"v_mov_b64 v[180:181], 0\n"
"v_mov_b64 v[182:183], 0\n"
"v_mov_b64 v[184:185], 0\n"
"v_mov_b64 v[186:187], 0\n"
"v_mov_b64 v[188:189], 0\n"
"v_mov_b64 v[190:191], 0\n"
"v_mov_b64 v[192:193], 0\n"
"v_mov_b64 v[194:195], 0\n"
"v_mov_b64 v[196:197], 0\n"
"v_mov_b64 v[198:199], 0\n"
"v_mov_b64 v[200:201], 0\n"
"v_mov_b64 v[202:203], 0\n"
"v_mov_b64 v[204:205], 0\n"
"v_mov_b64 v[206:207], 0\n"
"v_mov_b64 v[8:9], 0\n"
"v_accvgpr_write_b32 a128, 0\n"
"v_accvgpr_write_b32 a129, 0\n"
"v_accvgpr_write_b32 a130, 0\n"
"v_accvgpr_write_b32 a131, 0\n"
"v_accvgpr_write_b32 a132, 0\n"
"v_accvgpr_write_b32 a133, 0\n"
"v_accvgpr_write_b32 a134, 0\n"
"v_accvgpr_write_b32 a135, 0\n"
"s_lshl_b32 s78, s56, 6\n"
"s_add_u32 s78, s78, s63\n"
"s_min_i32 s77, s78, 0\n"
"s_cmp_gt_i32 s63, 0\n"
"s_cselect_b32 s79, 1.0, 0\n"
"v_mov_b64 v[208:209], 0\n"
"v_mov_b64 v[210:211], 0\n"
"v_mov_b64 v[212:213], 0\n"
"v_mov_b64 v[214:215], 0\n"
"v_mov_b64 v[216:217], 0\n"
"v_mov_b64 v[218:219], 0\n"
"v_mov_b64 v[220:221], 0\n"
"v_mov_b64 v[222:223], 0\n"
"v_mov_b64 v[224:225], 0\n"
"v_mov_b64 v[226:227], 0\n"
"v_mov_b64 v[228:229], 0\n"
"v_mov_b64 v[230:231], 0\n"
"v_mov_b64 v[232:233], 0\n"
"v_mov_b64 v[234:235], 0\n"
"v_mov_b64 v[236:237], 0\n"
"v_mov_b64 v[238:239], 0\n"
"v_mov_b64 v[16:17], 0\n"
"v_mov_b64 v[18:19], 0\n"
"v_mov_b64 v[20:21], 0\n"
"v_mov_b64 v[22:23], 0\n"
"v_mov_b64 v[24:25], 0\n"
"v_mov_b64 v[26:27], 0\n"
"v_mov_b64 v[28:29], 0\n"
"v_mov_b64 v[30:31], 0\n"
"v_mov_b64 v[32:33], 0\n"
"v_mov_b64 v[34:35], 0\n"
"v_mov_b64 v[36:37], 0\n"
"v_mov_b64 v[38:39], 0\n"
"v_mov_b64 v[40:41], 0\n"
"v_mov_b64 v[42:43], 0\n"
"v_mov_b64 v[44:45], 0\n"
"v_mov_b64 v[46:47], 0\n"
"v_mov_b32 v240, 0x5d800000\n"
"v_mov_b32 v241, 0x21800000\n"
"v_mov_b32 v246, %11\n"
"s_add_u32 s84, %10, 0\n"
"s_add_u32 s85, %10, 4096\n"
"s_add_u32 s86, %10, 8192\n"
"s_add_u32 s87, %10, 12288\n"
"s_add_u32 s88, %10, 16384\n"
"v_lshrrev_b32 v12, 8, v0\n"
"v_bfe_u32 v13, v0, 6, 2\n"
"v_cmp_eq_u32 vcc, v12, v13\n"
"v_bfe_u32 v12, v0, 4, 1\n"
"v_lshlrev_b32 v12, 4, v12\n"
"v_mov_b32 v13, 0x3f80\n"
"v_lshlrev_b32 v12, v12, v13\n"
"v_cndmask_b32 v12, 0, v12, vcc\n"
"v_and_b32 v13, 32, v0\n"
"v_cmp_eq_u32 vcc, 0, v13\n"
"s_nop 1\n"
"v_cndmask_b32 v14, 0, v12, vcc\n"
"v_cndmask_b32 v15, v12, 0, vcc\n"
"v_accvgpr_write_b32 a240, v14\n"
"v_accvgpr_write_b32 a241, v15\n"
"v_accvgpr_write_b32 a242, 0\n"
"v_accvgpr_write_b32 a243, 0\n"
"v_accvgpr_write_b32 a244, 0\n"
"v_accvgpr_write_b32 a245, 0\n"
"v_accvgpr_write_b32 a246, v14\n"
"v_accvgpr_write_b32 a247, v15\n"
"s_min_i32 s77, s77, s89\n"
"s_waitcnt vmcnt(38)\n"
"s_barrier\n"
"ds_read_b128 a[64:67], v7 offset:0\n"
"ds_read_b128 a[68:71], v7 offset:1024\n"
"ds_read_b128 a[72:75], v7 offset:2048\n"
"ds_read_b128 a[76:79], v7 offset:3072\n"
"ds_read_b128 a[80:83], v7 offset:4096\n"
"ds_read_b128 a[84:87], v7 offset:5120\n"
"ds_read_b128 a[88:91], v7 offset:6144\n"
"ds_read_b128 a[92:95], v7 offset:7168\n"
"ds_read_b128 a[96:99], v7 offset:8192\n"
"ds_read_b128 a[100:103], v7 offset:9216\n"
"ds_read_b128 a[104:107], v7 offset:10240\n"
"ds_read_b128 a[108:111], v7 offset:11264\n"
"s_cmp_eq_u32 s56, 1\n"
"s_cbranch_scc0 LQ7%=\n"
"buffer_load_dwordx4 a[176:179], v0, s[52:55], s67 offen offset:0\n"
"buffer_load_dwordx4 a[180:183], v0, s[52:55], s67 offen offset:1024\n"
"buffer_load_dwordx4 a[184:187], v0, s[52:55], s67 offen offset:2048\n"
"buffer_load_dwordx4 a[188:191], v0, s[52:55], s67 offen offset:3072\n"
"buffer_load_dwordx4 a[192:195], v0, s[52:55], s65 offen offset:0\n"
"buffer_load_dwordx4 a[196:199], v0, s[52:55], s65 offen offset:1024\n"
"buffer_load_dwordx4 a[200:203], v0, s[52:55], s65 offen offset:2048\n"
"buffer_load_dwordx4 a[204:207], v0, s[52:55], s65 offen offset:3072\n"
"buffer_load_dwordx4 a[208:211], v0, s[52:55], s66 offen offset:0\n"
"buffer_load_dwordx4 a[212:215], v0, s[52:55], s66 offen offset:1024\n"
"buffer_load_dwordx4 a[216:219], v0, s[52:55], s66 offen offset:2048\n"
"buffer_load_dwordx4 a[220:223], v0, s[52:55], s66 offen offset:3072\n"
"buffer_load_dwordx4 a[224:227], v0, s[52:55], s70 offen offset:0\n"
"buffer_load_dwordx4 a[228:231], v0, s[52:55], s70 offen offset:1024\n"
"buffer_load_dwordx4 a[232:235], v0, s[52:55], s70 offen offset:2048\n"
"buffer_load_dwordx4 a[236:239], v0, s[52:55], s70 offen offset:3072\n"
"LQ7%=:\n"
"s_cmp_lt_i32 s63, 0\n"
"s_cbranch_scc1 LM0%=\n"
"v_cvt_f32_i32 v12, s63\n"
"v_add_f32 v12, v12, v3\n"
"v_mul_f32 v12, 0xf1c9f2ca, v12\n"
"v_add_f32 v16, 0x7149f2ca, v12\n"
"v_add_f32 v17, 0xf149f2ca, v12\n"
"v_add_f32 v18, 0xf2177617, v12\n"
"v_add_f32 v19, 0xf27c6f7c, v12\n"
"v_add_f32 v20, 0x74cb86af, v12\n"
"v_add_f32 v21, 0x74c85ee4, v12\n"
"v_add_f32 v22, 0x74c53719, v12\n"
"v_add_f32 v23, 0x74c20f4e, v12\n"
"v_add_f32 v24, 0xf3c3a333, v12\n"
"v_add_f32 v25, 0xf3d04260, v12\n"
"v_add_f32 v26, 0xf3dce18d, v12\n"
"v_add_f32 v27, 0xf3e980b9, v12\n"
"v_add_f32 v28, 0x749909fd, v12\n"
"v_add_f32 v29, 0x7495e232, v12\n"
"v_add_f32 v30, 0x7492ba67, v12\n"
"v_add_f32 v31, 0x748f929b, v12\n"
"v_add_f32 v32, 0xf446caff, v12\n"
"v_add_f32 v33, 0xf44d1a95, v12\n"
"v_add_f32 v34, 0xf4536a2b, v12\n"
"v_add_f32 v35, 0xf459b9c2, v12\n"
"v_add_f32 v36, 0x744d1a95, v12\n"
"v_add_f32 v37, 0x7446caff, v12\n"
"v_add_f32 v38, 0x74407b68, v12\n"
"v_add_f32 v39, 0x743a2bd2, v12\n"
"v_add_f32 v40, 0xf495e232, v12\n"
"v_add_f32 v41, 0xf49909fd, v12\n"
"v_add_f32 v42, 0xf49c31c8, v12\n"
"v_add_f32 v43, 0xf49f5993, v12\n"
"v_add_f32 v44, 0x73d04260, v12\n"
"v_add_f32 v45, 0x73c3a333, v12\n"
"v_add_f32 v46, 0x73b70407, v12\n"
"v_add_f32 v47, 0x73aa64da, v12\n"
"v_min_f32 v16, v16, v8\n"
"v_min_f32 v17, v17, v8\n"
"v_min_f32 v18, v18, v8\n"
"v_min_f32 v19, v19, v8\n"
"v_min_f32 v20, v20, v9\n"
"v_min_f32 v21, v21, v9\n"
"v_min_f32 v22, v22, v9\n"
"v_min_f32 v23, v23, v9\n"
"v_min_f32 v24, v24, v8\n"
"v_min_f32 v25, v25, v8\n"
"v_min_f32 v26, v26, v8\n"
"v_min_f32 v27, v27, v8\n"
"v_min_f32 v28, v28, v9\n"
"v_min_f32 v29, v29, v9\n"
"v_min_f32 v30, v30, v9\n"
"v_min_f32 v31, v31, v9\n"
"v_min_f32 v32, v32, v8\n"
"v_min_f32 v33, v33, v8\n"
"v_min_f32 v34, v34, v8\n"
"v_min_f32 v35, v35, v8\n"
"v_min_f32 v36, v36, v9\n"
"v_min_f32 v37, v37, v9\n"
"v_min_f32 v38, v38, v9\n"
"v_min_f32 v39, v39, v9\n"
"v_min_f32 v40, v40, v8\n"
"v_min_f32 v41, v41, v8\n"
"v_min_f32 v42, v42, v8\n"
"v_min_f32 v43, v43, v8\n"
"v_min_f32 v44, v44, v9\n"
"v_min_f32 v45, v45, v9\n"
"v_min_f32 v46, v46, v9\n"
"v_min_f32 v47, v47, v9\n"
"s_nop 1\n"
"LM0%=:\n"
"s_waitcnt vmcnt(37) lgkmcnt(11)\n"
"v_mfma_f32_16x16x32_bf16 v[48:51], a[64:67], a[0:3], v[16:19]\n"
"ds_read_b128 a[112:115], v7 offset:12288\n"
"s_waitcnt vmcnt(29)\n"
"v_mfma_f32_16x16x32_bf16 v[52:55], a[64:67], a[32:35], v[20:23]\n"
"s_add_u32 s63, s63, 64\n"
"s_waitcnt lgkmcnt(11)\n"
"v_mfma_f32_16x16x32_bf16 v[48:51], a[68:71], a[4:7], v[48:51]\n"
"ds_read_b128 a[116:119], v7 offset:13312\n"
"s_add_u32 s59, s59, 32768\n"
"s_waitcnt vmcnt(28)\n"
"v_mfma_f32_16x16x32_bf16 v[52:55], a[68:71], a[36:39], v[52:55]\n"
"buffer_load_dwordx4 v1, s[40:43], s61 offen offset:3072 lds\n"
"s_waitcnt lgkmcnt(11)\n"
"v_mfma_f32_16x16x32_bf16 v[48:51], a[72:75], a[8:11], v[48:51]\n"
"ds_read_b128 a[120:123], v7 offset:14336\n"
"s_add_u32 m0, m0, 0x1000\n"
"s_waitcnt vmcnt(28)\n"
"v_mfma_f32_16x16x32_bf16 v[52:55], a[72:75], a[40:43], v[52:55]\n"
"s_waitcnt lgkmcnt(11)\n"
"v_mfma_f32_16x16x32_bf16 v[48:51], a[76:79], a[12:15], v[48:51]\n"
"ds_read_b128 a[124:127], v7 offset:15360\n"
"s_waitcnt vmcnt(27) lgkmcnt(8)\n"
"v_mfma_f32_16x16x32_bf16 v[52:55], a[76:79], a[44:47], v[52:55]\n"
"s_cmp_eq_u32 s59, 163840\n"
"s_cselect_b32 s59, 0, s59\n"
"v_mfma_f32_16x16x32_bf16 v[48:51], a[80:83], a[16:19], v[48:51]\n"
"ds_read_b128 a[64:67], v7 offset:16384\n"
"v_add_u32 v253, s59, v0\n"
"s_waitcnt vmcnt(26)\n"
"v_mfma_f32_16x16x32_bf16 v[52:55], a[80:83], a[48:51], v[52:55]\n"
"v_mfma_f32_16x16x32_bf16 v[48:51], a[84:87], a[20:23], v[48:51]\n"
"ds_read_b128 a[68:71], v7 offset:17408\n"
"s_waitcnt vmcnt(25)\n"
"v_mfma_f32_16x16x32_bf16 v[52:55], a[84:87], a[52:55], v[52:55]\n"
"buffer_load_dwordx4 v6, s[40:43], s61 offen offset:0 lds\n"
"v_mfma_f32_16x16x32_bf16 v[48:51], a[88:91], a[24:27], v[48:51]\n"
"ds_read_b128 a[72:75], v7 offset:18432\n"
"s_waitcnt vmcnt(25)\n"
"v_mfma_f32_16x16x32_bf16 v[52:55], a[88:91], a[56:59], v[52:55]\n"
"v_mfma_f32_16x16x32_bf16 v[48:51], a[92:95], a[28:31], v[48:51]\n"
"ds_read_b128 a[76:79], v7 offset:19456\n"
"s_waitcnt vmcnt(24) lgkmcnt(8)\n"
"v_mfma_f32_16x16x32_bf16 v[52:55], a[92:95], a[60:63], v[52:55]\n"
"v_mfma_f32_16x16x32_bf16 v[56:59], a[96:99], a[0:3], v[24:27]\n"
"ds_read_b128 a[80:83], v7 offset:20480\n"
"v_mfma_f32_16x16x32_bf16 v[60:63], a[96:99], a[32:35], v[28:31]\n"
"v_mfma_f32_16x16x32_bf16 v[56:59], a[100:103], a[4:7], v[56:59]\n"
"ds_read_b128 a[84:87], v7 offset:21504\n"
"v_mfma_f32_16x16x32_bf16 v[60:63], a[100:103], a[36:39], v[60:63]\n"
"buffer_load_dwordx4 v6, s[40:43], s61 offen offset:1024 lds\n"
"v_mfma_f32_16x16x32_bf16 v[56:59], a[104:107], a[8:11], v[56:59]\n"
"ds_read_b128 a[88:91], v7 offset:22528\n"
"v_mfma_f32_16x16x32_bf16 v[60:63], a[104:107], a[40:43], v[60:63]\n"
"v_exp_f32 v48, v48\n"
"v_mfma_f32_16x16x32_bf16 v[56:59], a[108:111], a[12:15], v[56:59]\n"
"ds_read_b128 a[92:95], v7 offset:23552\n"
"s_waitcnt lgkmcnt(8)\n"
"v_mfma_f32_16x16x32_bf16 v[60:63], a[108:111], a[44:47], v[60:63]\n"
"v_exp_f32 v49, v49\n"
"v_mfma_f32_16x16x32_bf16 v[56:59], a[112:115], a[16:19], v[56:59]\n"
"ds_read_b128 a[96:99], v7 offset:24576\n"
"v_mfma_f32_16x16x32_bf16 v[60:63], a[112:115], a[48:51], v[60:63]\n"
"v_exp_f32 v50, v50\n"
"v_mfma_f32_16x16x32_bf16 v[56:59], a[116:119], a[20:23], v[56:59]\n"
"ds_read_b128 a[100:103], v7 offset:25600\n"
"v_mfma_f32_16x16x32_bf16 v[60:63], a[116:119], a[52:55], v[60:63]\n"
"buffer_load_dwordx4 v6, s[40:43], s61 offen offset:2048 lds\n"
"v_mfma_f32_16x16x32_bf16 v[56:59], a[120:123], a[24:27], v[56:59]\n"
"ds_read_b128 a[104:107], v7 offset:26624\n"
"v_mfma_f32_16x16x32_bf16 v[60:63], a[120:123], a[56:59], v[60:63]\n"
"v_exp_f32 v51, v51\n"
"v_mfma_f32_16x16x32_bf16 v[56:59], a[124:127], a[28:31], v[56:59]\n"
"ds_read_b128 a[108:111], v7 offset:27648\n"
"s_waitcnt lgkmcnt(8)\n"
"v_mfma_f32_16x16x32_bf16 v[60:63], a[124:127], a[60:63], v[60:63]\n"
"v_exp_f32 v52, v52\n"
"v_mfma_f32_16x16x32_bf16 v[64:67], a[64:67], a[0:3], v[32:35]\n"
"ds_read_b128 a[112:115], v7 offset:28672\n"
"v_cvt_pk_bf16_f32 v80, v48, v49\n"
"v_mfma_f32_16x16x32_bf16 v[68:71], a[64:67], a[32:35], v[36:39]\n"
"v_exp_f32 v53, v53\n"
"v_mfma_f32_16x16x32_bf16 v[64:67], a[68:71], a[4:7], v[64:67]\n"
"ds_read_b128 a[116:119], v7 offset:29696\n"
"v_cvt_pk_bf16_f32 v81, v50, v51\n"
"v_mfma_f32_16x16x32_bf16 v[68:71], a[68:71], a[36:39], v[68:71]\n"
"buffer_load_dwordx4 v6, s[40:43], s61 offen offset:3072 lds\n"
"s_add_u32 m0, s60, s62\n"
"v_mfma_f32_16x16x32_bf16 v[64:67], a[72:75], a[8:11], v[64:67]\n"
"ds_read_b128 a[120:123], v7 offset:30720\n"
"v_readlane_b32 s61, v2, s58\n"
"v_mfma_f32_16x16x32_bf16 v[68:71], a[72:75], a[40:43], v[68:71]\n"
"v_exp_f32 v54, v54\n"
"v_mfma_f32_16x16x32_bf16 v[64:67], a[76:79], a[12:15], v[64:67]\n"
"ds_read_b128 a[124:127], v7 offset:31744\n"
"s_waitcnt lgkmcnt(8)\n"
"v_mfma_f32_16x16x32_bf16 v[68:71], a[76:79], a[44:47], v[68:71]\n"
"s_waitcnt vmcnt(19)\n"
"s_barrier\n"
"v_mfma_f32_16x16x32_bf16 v[64:67], a[80:83], a[16:19], v[64:67]\n"
"ds_read_b128 a[64:67], v253 offset:0\n"
"s_cmp_lt_u32 s58, 34\n"
"v_mfma_f32_16x16x32_bf16 v[68:71], a[80:83], a[48:51], v[68:71]\n"
"v_exp_f32 v55, v55\n"
"v_mfma_f32_16x16x32_bf16 v[64:67], a[84:87], a[20:23], v[64:67]\n"
"ds_read_b128 a[68:71], v253 offset:1024\n"
"s_cselect_b32 s42, s71, 0\n"
"v_mfma_f32_16x16x32_bf16 v[68:71], a[84:87], a[52:55], v[68:71]\n"
"buffer_load_dwordx4 v1, s[40:43], s61 offen offset:0 lds\n"
"v_cvt_pk_bf16_f32 v88, v52, v53\n"
"v_mfma_f32_16x16x32_bf16 v[64:67], a[88:91], a[24:27], v[64:67]\n"
"ds_read_b128 a[72:75], v253 offset:2048\n"
"s_add_u32 s60, s60, 32768\n"
"v_mfma_f32_16x16x32_bf16 v[68:71], a[88:91], a[56:59], v[68:71]\n"
"v_exp_f32 v56, v56\n"
"v_mfma_f32_16x16x32_bf16 v[64:67], a[92:95], a[28:31], v[64:67]\n"
"ds_read_b128 a[76:79], v253 offset:3072\n"
"s_waitcnt lgkmcnt(8)\n"
"v_mfma_f32_16x16x32_bf16 v[68:71], a[92:95], a[60:63], v[68:71]\n"
"s_cmp_eq_u32 s60, 163840\n"
"s_cselect_b32 s60, 0, s60\n"
"v_mfma_f32_16x16x32_bf16 v[72:75], a[96:99], a[0:3], v[40:43]\n"
"ds_read_b128 a[80:83], v253 offset:4096\n"
"s_add_u32 s58, s58, 1\n"
"v_mfma_f32_16x16x32_bf16 v[76:79], a[96:99], a[32:35], v[44:47]\n"
"v_exp_f32 v57, v57\n"
"v_mfma_f32_16x16x32_bf16 v[72:75], a[100:103], a[4:7], v[72:75]\n"
"ds_read_b128 a[84:87], v253 offset:5120\n"
"v_mov_b32 v7, v253\n"
"v_mfma_f32_16x16x32_bf16 v[76:79], a[100:103], a[36:39], v[76:79]\n"
"buffer_load_dwordx4 v1, s[40:43], s61 offen offset:1024 lds\n"
"v_cvt_pk_bf16_f32 v89, v54, v55\n"
"v_mfma_f32_16x16x32_bf16 v[72:75], a[104:107], a[8:11], v[72:75]\n"
"ds_read_b128 a[88:91], v253 offset:6144\n"
"v_mfma_f32_16x16x32_bf16 v[76:79], a[104:107], a[40:43], v[76:79]\n"
"v_exp_f32 v58, v58\n"
"v_mfma_f32_16x16x32_bf16 v[72:75], a[108:111], a[12:15], v[72:75]\n"
"ds_read_b128 a[92:95], v253 offset:7168\n"
"s_waitcnt lgkmcnt(8)\n"
"v_mfma_f32_16x16x32_bf16 v[76:79], a[108:111], a[44:47], v[76:79]\n"
"v_exp_f32 v59, v59\n"
"v_mfma_f32_16x16x32_bf16 v[72:75], a[112:115], a[16:19], v[72:75]\n"
"ds_read_b128 a[96:99], v253 offset:8192\n"
"v_cvt_pk_bf16_f32 v82, v56, v57\n"
"v_mfma_f32_16x16x32_bf16 v[76:79], a[112:115], a[48:51], v[76:79]\n"
"v_exp_f32 v60, v60\n"
"v_mfma_f32_16x16x32_bf16 v[72:75], a[116:119], a[20:23], v[72:75]\n"
"ds_read_b128 a[100:103], v253 offset:9216\n"
"v_cvt_pk_bf16_f32 v83, v58, v59\n"
"v_mfma_f32_16x16x32_bf16 v[76:79], a[116:119], a[52:55], v[76:79]\n"
"buffer_load_dwordx4 v1, s[40:43], s61 offen offset:2048 lds\n"
"v_mfma_f32_16x16x32_bf16 v[72:75], a[120:123], a[24:27], v[72:75]\n"
"ds_read_b128 a[104:107], v253 offset:10240\n"
"v_mfma_f32_16x16x32_bf16 v[76:79], a[120:123], a[56:59], v[76:79]\n"
"v_exp_f32 v61, v61\n"
"v_mfma_f32_16x16x32_bf16 v[72:75], a[124:127], a[28:31], v[72:75]\n"
"ds_read_b128 a[108:111], v253 offset:11264\n"
"s_waitcnt lgkmcnt(8)\n"
"v_mfma_f32_16x16x32_bf16 v[76:79], a[124:127], a[60:63], v[76:79]\n"
"v_exp_f32 v62, v62\n"
"s_nop 3\n"
"v_exp_f32 v63, v63\n"
"v_exp_f32 v64, v64\n"
"v_exp_f32 v65, v65\n"
"v_exp_f32 v66, v66\n"
"v_exp_f32 v67, v67\n"
"v_exp_f32 v68, v68\n"
"v_exp_f32 v69, v69\n"
"v_exp_f32 v70, v70\n"
"v_exp_f32 v71, v71\n"
"v_exp_f32 v72, v72\n"
"v_exp_f32 v73, v73\n"
"v_exp_f32 v74, v74\n"
"v_exp_f32 v75, v75\n"
"v_exp_f32 v76, v76\n"
"v_exp_f32 v77, v77\n"
"v_exp_f32 v78, v78\n"
"v_exp_f32 v79, v79\n"
"s_nop 0\n"
"v_cvt_pk_bf16_f32 v90, v60, v61\n"
"v_cvt_pk_bf16_f32 v91, v62, v63\n"
"v_cvt_pk_bf16_f32 v84, v64, v65\n"
"v_cvt_pk_bf16_f32 v85, v66, v67\n"
"v_cvt_pk_bf16_f32 v92, v68, v69\n"
"v_cvt_pk_bf16_f32 v93, v70, v71\n"
"v_cvt_pk_bf16_f32 v86, v72, v73\n"
"v_cvt_pk_bf16_f32 v87, v74, v75\n"
"v_cvt_pk_bf16_f32 v94, v76, v77\n"
"v_cvt_pk_bf16_f32 v95, v78, v79\n"
"s_cmp_ge_i32 s63, s77\n"
"s_cbranch_scc1 LEV1%=\n"
"LA1%=:\n"
"v_mfma_f32_16x16x32_bf16 v[48:51], a[64:67], a[0:3], v[16:19]\n"
"ds_read_b128 a[112:115], v7 offset:12288\n"
"v_mfma_f32_16x16x32_bf16 v[52:55], a[64:67], a[32:35], v[20:23]\n"
"v_mfma_f32_16x16x32_bf16 v[48:51], a[68:71], a[4:7], v[48:51]\n"
"ds_read_b128 a[116:119], v7 offset:13312\n"
"s_add_u32 s59, s59, 32768\n"
"v_mfma_f32_16x16x32_bf16 v[52:55], a[68:71], a[36:39], v[52:55]\n"
"buffer_load_dwordx4 v1, s[40:43], s61 offen offset:3072 lds\n"
"v_mfma_f32_16x16x32_bf16 v[48:51], a[72:75], a[8:11], v[48:51]\n"
"ds_read_b128 a[120:123], v7 offset:14336\n"
"s_add_u32 m0, m0, 0x1000\n"
"v_mfma_f32_16x16x32_bf16 v[52:55], a[72:75], a[40:43], v[52:55]\n"
"v_mfma_f32_16x16x32_bf16 v[48:51], a[76:79], a[12:15], v[48:51]\n"
"ds_read_b128 a[124:127], v7 offset:15360\n"
"s_waitcnt lgkmcnt(8)\n"
"v_mfma_f32_16x16x32_bf16 v[52:55], a[76:79], a[44:47], v[52:55]\n"
"s_cmp_eq_u32 s59, 163840\n"
"s_cselect_b32 s59, 0, s59\n"
"v_mfma_f32_16x16x32_bf16 v[48:51], a[80:83], a[16:19], v[48:51]\n"
"ds_read_b128 a[64:67], v7 offset:16384\n"
"v_add_u32 v253, s59, v0\n"
"v_mfma_f32_16x16x32_bf16 v[52:55], a[80:83], a[48:51], v[52:55]\n"
"s_add_u32 s63, s63, 64\n"
"v_mfma_f32_16x16x32_bf16 v[48:51], a[84:87], a[20:23], v[48:51]\n"
"ds_read_b128 a[68:71], v7 offset:17408\n"
"v_mfma_f32_16x16x32_bf16 v[52:55], a[84:87], a[52:55], v[52:55]\n"
"buffer_load_dwordx4 v6, s[40:43], s61 offen offset:0 lds\n"
"v_mfma_f32_16x16x32_bf16 v[48:51], a[88:91], a[24:27], v[48:51]\n"
"ds_read_b128 a[72:75], v7 offset:18432\n"
"v_mfma_f32_16x16x32_bf16 v[52:55], a[88:91], a[56:59], v[52:55]\n"
"v_mfma_f32_16x16x32_bf16 v[48:51], a[92:95], a[28:31], v[48:51]\n"
"ds_read_b128 a[76:79], v7 offset:19456\n"
"s_waitcnt lgkmcnt(8)\n"
"v_mfma_f32_16x16x32_bf16 v[52:55], a[92:95], a[60:63], v[52:55]\n"
"v_mfma_f32_16x16x32_bf16 v[56:59], a[96:99], a[0:3], v[24:27]\n"
"ds_read_b128 a[80:83], v7 offset:20480\n"
"v_mfma_f32_16x16x32_bf16 v[60:63], a[96:99], a[32:35], v[28:31]\n"
"v_mfma_f32_16x16x32_bf16 v[56:59], a[100:103], a[4:7], v[56:59]\n"
"ds_read_b128 a[84:87], v7 offset:21504\n"
"v_mfma_f32_16x16x32_bf16 v[60:63], a[100:103], a[36:39], v[60:63]\n"
"buffer_load_dwordx4 v6, s[40:43], s61 offen offset:1024 lds\n"
"v_mfma_f32_16x16x32_bf16 v[56:59], a[104:107], a[8:11], v[56:59]\n"
"ds_read_b128 a[88:91], v7 offset:22528\n"
"v_mfma_f32_16x16x32_bf16 v[60:63], a[104:107], a[40:43], v[60:63]\n"
"v_exp_f32 v48, v48\n"
"v_mfma_f32_16x16x32_bf16 v[56:59], a[108:111], a[12:15], v[56:59]\n"
"ds_read_b128 a[92:95], v7 offset:23552\n"
"s_waitcnt lgkmcnt(8)\n"
"v_mfma_f32_16x16x32_bf16 v[60:63], a[108:111], a[44:47], v[60:63]\n"
"v_exp_f32 v49, v49\n"
"v_mfma_f32_16x16x32_bf16 v[56:59], a[112:115], a[16:19], v[56:59]\n"
"ds_read_b128 a[96:99], v7 offset:24576\n"
"v_mfma_f32_16x16x32_bf16 v[60:63], a[112:115], a[48:51], v[60:63]\n"
"v_exp_f32 v50, v50\n"
"v_mfma_f32_16x16x32_bf16 v[56:59], a[116:119], a[20:23], v[56:59]\n"
"ds_read_b128 a[100:103], v7 offset:25600\n"
"v_mfma_f32_16x16x32_bf16 v[60:63], a[116:119], a[52:55], v[60:63]\n"
"buffer_load_dwordx4 v6, s[40:43], s61 offen offset:2048 lds\n"
"v_mfma_f32_16x16x32_bf16 v[56:59], a[120:123], a[24:27], v[56:59]\n"
"ds_read_b128 a[104:107], v7 offset:26624\n"
"v_mfma_f32_16x16x32_bf16 v[60:63], a[120:123], a[56:59], v[60:63]\n"
"v_exp_f32 v51, v51\n"
"v_mfma_f32_16x16x32_bf16 v[56:59], a[124:127], a[28:31], v[56:59]\n"
"ds_read_b128 a[108:111], v7 offset:27648\n"
"s_waitcnt lgkmcnt(8)\n"
"v_mfma_f32_16x16x32_bf16 v[60:63], a[124:127], a[60:63], v[60:63]\n"
"v_exp_f32 v52, v52\n"
"v_mfma_f32_16x16x32_bf16 v[64:67], a[64:67], a[0:3], v[32:35]\n"
"ds_read_b128 a[112:115], v7 offset:28672\n"
"v_cvt_pk_bf16_f32 v96, v48, v49\n"
"v_mfma_f32_16x16x32_bf16 v[68:71], a[64:67], a[32:35], v[36:39]\n"
"v_exp_f32 v53, v53\n"
"v_mfma_f32_16x16x32_bf16 v[64:67], a[68:71], a[4:7], v[64:67]\n"
"ds_read_b128 a[116:119], v7 offset:29696\n"
"v_cvt_pk_bf16_f32 v97, v50, v51\n"
"v_mfma_f32_16x16x32_bf16 v[68:71], a[68:71], a[36:39], v[68:71]\n"
"buffer_load_dwordx4 v6, s[40:43], s61 offen offset:3072 lds\n"
"s_add_u32 m0, s60, s62\n"
"v_mfma_f32_16x16x32_bf16 v[64:67], a[72:75], a[8:11], v[64:67]\n"
"ds_read_b128 a[120:123], v7 offset:30720\n"
"v_readlane_b32 s61, v2, s58\n"
"v_mfma_f32_16x16x32_bf16 v[68:71], a[72:75], a[40:43], v[68:71]\n"
"v_exp_f32 v54, v54\n"
"v_mfma_f32_16x16x32_bf16 v[64:67], a[76:79], a[12:15], v[64:67]\n"
"ds_read_b128 a[124:127], v7 offset:31744\n"
"s_waitcnt lgkmcnt(8)\n"
"v_mfma_f32_16x16x32_bf16 v[68:71], a[76:79], a[44:47], v[68:71]\n"
"s_waitcnt vmcnt(17)\n"
"s_barrier\n"
"v_mfma_f32_16x16x32_bf16 v[64:67], a[80:83], a[16:19], v[64:67]\n"
"ds_read_b128 a[64:67], v253 offset:0\n"
"s_cmp_lt_u32 s58, 34\n"
"v_mfma_f32_16x16x32_bf16 v[68:71], a[80:83], a[48:51], v[68:71]\n"
"v_exp_f32 v55, v55\n"
"v_mfma_f32_16x16x32_bf16 v[64:67], a[84:87], a[20:23], v[64:67]\n"
"ds_read_b128 a[68:71], v253 offset:1024\n"
"s_cselect_b32 s42, s71, 0\n"
"v_mfma_f32_16x16x32_bf16 v[68:71], a[84:87], a[52:55], v[68:71]\n"
"buffer_load_dwordx4 v1, s[40:43], s61 offen offset:0 lds\n"
"v_cvt_pk_bf16_f32 v104, v52, v53\n"
"v_mfma_f32_16x16x32_bf16 v[64:67], a[88:91], a[24:27], v[64:67]\n"
"ds_read_b128 a[72:75], v253 offset:2048\n"
"s_add_u32 s60, s60, 32768\n"
"v_mfma_f32_16x16x32_bf16 v[68:71], a[88:91], a[56:59], v[68:71]\n"
"v_exp_f32 v56, v56\n"
"v_mfma_f32_16x16x32_bf16 v[64:67], a[92:95], a[28:31], v[64:67]\n"
"ds_read_b128 a[76:79], v253 offset:3072\n"
"s_waitcnt lgkmcnt(8)\n"
"v_mfma_f32_16x16x32_bf16 v[68:71], a[92:95], a[60:63], v[68:71]\n"
"s_cmp_eq_u32 s60, 163840\n"
"s_cselect_b32 s60, 0, s60\n"
"v_mfma_f32_16x16x32_bf16 v[72:75], a[96:99], a[0:3], v[40:43]\n"
"ds_read_b128 a[80:83], v253 offset:4096\n"
"s_add_u32 s58, s58, 1\n"
"v_mfma_f32_16x16x32_bf16 v[76:79], a[96:99], a[32:35], v[44:47]\n"
"v_exp_f32 v57, v57\n"
"v_mfma_f32_16x16x32_bf16 v[72:75], a[100:103], a[4:7], v[72:75]\n"
"ds_read_b128 a[84:87], v253 offset:5120\n"
"v_mov_b32 v7, v253\n"
"v_mfma_f32_16x16x32_bf16 v[76:79], a[100:103], a[36:39], v[76:79]\n"
"buffer_load_dwordx4 v1, s[40:43], s61 offen offset:1024 lds\n"
"v_cvt_pk_bf16_f32 v105, v54, v55\n"
"v_mfma_f32_16x16x32_bf16 v[72:75], a[104:107], a[8:11], v[72:75]\n"
"ds_read_b128 a[88:91], v253 offset:6144\n"
"v_mfma_f32_16x16x32_bf16 v[76:79], a[104:107], a[40:43], v[76:79]\n"
"v_exp_f32 v58, v58\n"
"v_mfma_f32_16x16x32_bf16 v[72:75], a[108:111], a[12:15], v[72:75]\n"
"ds_read_b128 a[92:95], v253 offset:7168\n"
"s_waitcnt lgkmcnt(8)\n"
"v_mfma_f32_16x16x32_bf16 v[76:79], a[108:111], a[44:47], v[76:79]\n"
"v_mfma_f32_16x16x32_bf16 v[72:75], a[112:115], a[16:19], v[72:75]\n"
"ds_read_b128 a[96:99], v253 offset:8192\n"
"v_mfma_f32_16x16x32_bf16 v[76:79], a[112:115], a[48:51], v[76:79]\n"
"v_exp_f32 v59, v59\n"
"v_mfma_f32_16x16x32_bf16 v[72:75], a[116:119], a[20:23], v[72:75]\n"
"ds_read_b128 a[100:103], v253 offset:9216\n"
"v_cvt_pk_bf16_f32 v98, v56, v57\n"
"v_mfma_f32_16x16x32_bf16 v[76:79], a[116:119], a[52:55], v[76:79]\n"
"buffer_load_dwordx4 v1, s[40:43], s61 offen offset:2048 lds\n"
"v_cvt_pk_bf16_f32 v99, v58, v59\n"
"v_mfma_f32_16x16x32_bf16 v[72:75], a[120:123], a[24:27], v[72:75]\n"
"ds_read_b128 a[104:107], v253 offset:10240\n"
"v_mfma_f32_16x16x32_bf16 v[76:79], a[120:123], a[56:59], v[76:79]\n"
"v_exp_f32 v60, v60\n"
"v_mfma_f32_16x16x32_bf16 v[72:75], a[124:127], a[28:31], v[72:75]\n"
"ds_read_b128 a[108:111], v253 offset:11264\n"
"s_waitcnt lgkmcnt(8)\n"
"v_mfma_f32_16x16x32_bf16 v[76:79], a[124:127], a[60:63], v[76:79]\n"
"v_exp_f32 v61, v61\n"
"LB1%=:\n"
"v_mfma_f32_16x16x32_bf16 v[112:115], a[64:67], v[80:83], v[112:115]\n"
"ds_read_b128 a[112:115], v7 offset:12288\n"
"v_mfma_f32_16x16x32_bf16 v[116:119], a[64:67], v[88:91], v[116:119]\n"
"v_exp_f32 v62, v62\n"
"v_mfma_f32_16x16x32_bf16 v[112:115], a[68:71], v[84:87], v[112:115]\n"
"ds_read_b128 a[116:119], v7 offset:13312\n"
"s_add_u32 s59, s59, 32768\n"
"v_mfma_f32_16x16x32_bf16 v[116:119], a[68:71], v[92:95], v[116:119]\n"
"buffer_load_dwordx4 v1, s[40:43], s61 offen offset:3072 lds\n"
"v_mfma_f32_16x16x32_bf16 v[120:123], a[72:75], v[80:83], v[120:123]\n"
"ds_read_b128 a[120:123], v7 offset:14336\n"
"s_add_u32 m0, m0, 0x1000\n"
"v_mfma_f32_16x16x32_bf16 v[124:127], a[72:75], v[88:91], v[124:127]\n"
"v_exp_f32 v63, v63\n"
"v_mfma_f32_16x16x32_bf16 v[120:123], a[76:79], v[84:87], v[120:123]\n"
"ds_read_b128 a[124:127], v7 offset:15360\n"
"s_waitcnt lgkmcnt(8)\n"
"v_mfma_f32_16x16x32_bf16 v[124:127], a[76:79], v[92:95], v[124:127]\n"
"s_cmp_eq_u32 s59, 163840\n"
"s_cselect_b32 s59, 0, s59\n"
"v_mfma_f32_16x16x32_bf16 v[128:131], a[80:83], v[80:83], v[128:131]\n"
"ds_read_b128 a[64:67], v7 offset:16384\n"
"v_add_u32 v253, s59, v0\n"
"v_mfma_f32_16x16x32_bf16 v[132:135], a[80:83], v[88:91], v[132:135]\n"
"v_exp_f32 v64, v64\n"
"v_mfma_f32_16x16x32_bf16 v[128:131], a[84:87], v[84:87], v[128:131]\n"
"ds_read_b128 a[68:71], v7 offset:17408\n"
"v_cvt_pk_bf16_f32 v106, v60, v61\n"
"v_mfma_f32_16x16x32_bf16 v[132:135], a[84:87], v[92:95], v[132:135]\n"
"buffer_load_dwordx4 v6, s[40:43], s61 offen offset:0 lds\n"
"v_cvt_pk_bf16_f32 v107, v62, v63\n"
"v_mfma_f32_16x16x32_bf16 v[136:139], a[88:91], v[80:83], v[136:139]\n"
"ds_read_b128 a[72:75], v7 offset:18432\n"
"s_waitcnt vmcnt(16)\n"
"buffer_load_dword v247, v246, s[52:55], 0 offen sc0 sc1\n"
"v_mfma_f32_16x16x32_bf16 v[140:143], a[88:91], v[88:91], v[140:143]\n"
"v_exp_f32 v65, v65\n"
"v_mfma_f32_16x16x32_bf16 v[136:139], a[92:95], v[84:87], v[136:139]\n"
"ds_read_b128 a[76:79], v7 offset:19456\n"
"s_waitcnt lgkmcnt(8)\n"
"v_mfma_f32_16x16x32_bf16 v[140:143], a[92:95], v[92:95], v[140:143]\n"
"v_exp_f32 v66, v66\n"
"v_mfma_f32_16x16x32_bf16 v[144:147], a[96:99], v[80:83], v[144:147]\n"
"ds_read_b128 a[80:83], v7 offset:20480\n"
"v_mfma_f32_16x16x32_bf16 v[148:151], a[96:99], v[88:91], v[148:151]\n"
"v_exp_f32 v67, v67\n"
"v_mfma_f32_16x16x32_bf16 v[144:147], a[100:103], v[84:87], v[144:147]\n"
"ds_read_b128 a[84:87], v7 offset:21504\n"
"v_cvt_pk_bf16_f32 v100, v64, v65\n"
"v_mfma_f32_16x16x32_bf16 v[148:151], a[100:103], v[92:95], v[148:151]\n"
"buffer_load_dwordx4 v6, s[40:43], s61 offen offset:1024 lds\n"
"v_cvt_pk_bf16_f32 v101, v66, v67\n"
"v_mfma_f32_16x16x32_bf16 v[152:155], a[104:107], v[80:83], v[152:155]\n"
"ds_read_b128 a[88:91], v7 offset:22528\n"
"v_mfma_f32_16x16x32_bf16 v[156:159], a[104:107], v[88:91], v[156:159]\n"
"v_exp_f32 v68, v68\n"
"v_mfma_f32_16x16x32_bf16 v[152:155], a[108:111], v[84:87], v[152:155]\n"
"ds_read_b128 a[92:95], v7 offset:23552\n"
"s_waitcnt lgkmcnt(8)\n"
"v_mfma_f32_16x16x32_bf16 v[156:159], a[108:111], v[92:95], v[156:159]\n"
"v_mfma_f32_16x16x32_bf16 v[160:163], a[112:115], v[80:83], v[160:163]\n"
"ds_read_b128 a[96:99], v7 offset:24576\n"
"v_mfma_f32_16x16x32_bf16 v[164:167], a[112:115], v[88:91], v[164:167]\n"
"v_exp_f32 v69, v69\n"
"v_mfma_f32_16x16x32_bf16 v[160:163], a[116:119], v[84:87], v[160:163]\n"
"ds_read_b128 a[100:103], v7 offset:25600\n"
"v_mfma_f32_16x16x32_bf16 v[164:167], a[116:119], v[92:95], v[164:167]\n"
"buffer_load_dwordx4 v6, s[40:43], s61 offen offset:2048 lds\n"
"v_mfma_f32_16x16x32_bf16 v[168:171], a[120:123], v[80:83], v[168:171]\n"
"ds_read_b128 a[104:107], v7 offset:26624\n"
"v_mfma_f32_16x16x32_bf16 v[172:175], a[120:123], v[88:91], v[172:175]\n"
"v_exp_f32 v70, v70\n"
"v_mfma_f32_16x16x32_bf16 v[168:171], a[124:127], v[84:87], v[168:171]\n"
"ds_read_b128 a[108:111], v7 offset:27648\n"
"s_waitcnt lgkmcnt(8)\n"
"v_mfma_f32_16x16x32_bf16 v[172:175], a[124:127], v[92:95], v[172:175]\n"
"v_exp_f32 v71, v71\n"
"v_mfma_f32_16x16x32_bf16 v[176:179], a[64:67], v[80:83], v[176:179]\n"
"ds_read_b128 a[112:115], v7 offset:28672\n"
"v_cvt_pk_bf16_f32 v108, v68, v69\n"
"v_mfma_f32_16x16x32_bf16 v[180:183], a[64:67], v[88:91], v[180:183]\n"
"v_exp_f32 v72, v72\n"
"v_mfma_f32_16x16x32_bf16 v[176:179], a[68:71], v[84:87], v[176:179]\n"
"ds_read_b128 a[116:119], v7 offset:29696\n"
"v_cvt_pk_bf16_f32 v109, v70, v71\n"
"v_mfma_f32_16x16x32_bf16 v[180:183], a[68:71], v[92:95], v[180:183]\n"
"buffer_load_dwordx4 v6, s[40:43], s61 offen offset:3072 lds\n"
"s_add_u32 m0, s60, s62\n"
"v_mfma_f32_16x16x32_bf16 v[184:187], a[72:75], v[80:83], v[184:187]\n"
"ds_read_b128 a[120:123], v7 offset:30720\n"
"v_readlane_b32 s61, v2, s58\n"
"v_mfma_f32_16x16x32_bf16 v[188:191], a[72:75], v[88:91], v[188:191]\n"
"v_exp_f32 v73, v73\n"
"v_mfma_f32_16x16x32_bf16 v[184:187], a[76:79], v[84:87], v[184:187]\n"
"ds_read_b128 a[124:127], v7 offset:31744\n"
"s_waitcnt lgkmcnt(8)\n"
"v_mfma_f32_16x16x32_bf16 v[188:191], a[76:79], v[92:95], v[188:191]\n"
"s_waitcnt vmcnt(17)\n"
"s_barrier\n"
"v_mfma_f32_16x16x32_bf16 v[192:195], a[80:83], v[80:83], v[192:195]\n"
"ds_read_b128 a[64:67], v253 offset:0\n"
"s_cmp_lt_u32 s58, 34\n"
"v_mfma_f32_16x16x32_bf16 v[196:199], a[80:83], v[88:91], v[196:199]\n"
"v_exp_f32 v74, v74\n"
"v_mfma_f32_16x16x32_bf16 v[192:195], a[84:87], v[84:87], v[192:195]\n"
"ds_read_b128 a[68:71], v253 offset:1024\n"
"s_cselect_b32 s42, s71, 0\n"
"v_mfma_f32_16x16x32_bf16 v[196:199], a[84:87], v[92:95], v[196:199]\n"
"buffer_load_dwordx4 v1, s[40:43], s61 offen offset:0 lds\n"
"v_mfma_f32_16x16x32_bf16 v[200:203], a[88:91], v[80:83], v[200:203]\n"
"ds_read_b128 a[72:75], v253 offset:2048\n"
"s_add_u32 s60, s60, 32768\n"
"v_mfma_f32_16x16x32_bf16 v[204:207], a[88:91], v[88:91], v[204:207]\n"
"v_exp_f32 v75, v75\n"
"v_mfma_f32_16x16x32_bf16 v[200:203], a[92:95], v[84:87], v[200:203]\n"
"ds_read_b128 a[76:79], v253 offset:3072\n"
"s_waitcnt lgkmcnt(8)\n"
"v_mfma_f32_16x16x32_bf16 v[204:207], a[92:95], v[92:95], v[204:207]\n"
"s_cmp_eq_u32 s60, 163840\n"
"s_cselect_b32 s60, 0, s60\n"
"v_mfma_f32_16x16x32_bf16 v[208:211], a[96:99], v[80:83], v[208:211]\n"
"ds_read_b128 a[80:83], v253 offset:4096\n"
"s_add_u32 s58, s58, 1\n"
"v_mfma_f32_16x16x32_bf16 v[212:215], a[96:99], v[88:91], v[212:215]\n"
"v_exp_f32 v76, v76\n"
"v_mfma_f32_16x16x32_bf16 v[208:211], a[100:103], v[84:87], v[208:211]\n"
"ds_read_b128 a[84:87], v253 offset:5120\n"
"v_mov_b32 v7, v253\n"
"v_mfma_f32_16x16x32_bf16 v[212:215], a[100:103], v[92:95], v[212:215]\n"
"buffer_load_dwordx4 v1, s[40:43], s61 offen offset:1024 lds\n"
"v_cvt_pk_bf16_f32 v102, v72, v73\n"
"v_mfma_f32_16x16x32_bf16 v[216:219], a[104:107], v[80:83], v[216:219]\n"
"ds_read_b128 a[88:91], v253 offset:6144\n"
"v_cvt_pk_bf16_f32 v103, v74, v75\n"
"v_mfma_f32_16x16x32_bf16 v[220:223], a[104:107], v[88:91], v[220:223]\n"
"v_exp_f32 v77, v77\n"
"v_mfma_f32_16x16x32_bf16 v[216:219], a[108:111], v[84:87], v[216:219]\n"
"ds_read_b128 a[92:95], v253 offset:7168\n"
"s_waitcnt lgkmcnt(8)\n"
"v_mfma_f32_16x16x32_bf16 v[220:223], a[108:111], v[92:95], v[220:223]\n"
"v_mfma_f32_16x16x32_bf16 v[224:227], a[112:115], v[80:83], v[224:227]\n"
"ds_read_b128 a[96:99], v253 offset:8192\n"
"v_mfma_f32_16x16x32_bf16 v[228:231], a[112:115], v[88:91], v[228:231]\n"
"v_exp_f32 v78, v78\n"
"v_mfma_f32_16x16x32_bf16 v[224:227], a[116:119], v[84:87], v[224:227]\n"
"ds_read_b128 a[100:103], v253 offset:9216\n"
"v_mfma_f32_16x16x32_bf16 v[228:231], a[116:119], v[92:95], v[228:231]\n"
"buffer_load_dwordx4 v1, s[40:43], s61 offen offset:2048 lds\n"
"v_mfma_f32_16x16x32_bf16 v[232:235], a[120:123], v[80:83], v[232:235]\n"
"ds_read_b128 a[104:107], v253 offset:10240\n"
"v_mfma_f32_16x16x32_bf16 v[236:239], a[120:123], v[88:91], v[236:239]\n"
"v_exp_f32 v79, v79\n"
"v_mfma_f32_16x16x32_bf16 v[232:235], a[124:127], v[84:87], v[232:235]\n"
"ds_read_b128 a[108:111], v253 offset:11264\n"
"s_waitcnt lgkmcnt(8)\n"
"v_mfma_f32_16x16x32_bf16 v[236:239], a[124:127], v[92:95], v[236:239]\n"
"v_cvt_pk_bf16_f32 v110, v76, v77\n"
"v_cvt_pk_bf16_f32 v111, v78, v79\n"
"v_mfma_f32_16x16x32_bf16 a[128:131], a[136:139], v[80:83], a[128:131]\n"
"v_mfma_f32_16x16x32_bf16 a[128:131], a[136:139], v[84:87], a[128:131]\n"
"v_mfma_f32_16x16x32_bf16 a[132:135], a[136:139], v[88:91], a[132:135]\n"
"s_cmp_ge_i32 s63, s77\n"
"v_mfma_f32_16x16x32_bf16 a[132:135], a[136:139], v[92:95], a[132:135]\n"
"s_cbranch_scc1 LEV0%=\n"
"LA0%=:\n"
"v_mfma_f32_16x16x32_bf16 v[48:51], a[64:67], a[0:3], v[16:19]\n"
"ds_read_b128 a[112:115], v7 offset:12288\n"
"v_mfma_f32_16x16x32_bf16 v[52:55], a[64:67], a[32:35], v[20:23]\n"
"v_mfma_f32_16x16x32_bf16 v[48:51], a[68:71], a[4:7], v[48:51]\n"
"ds_read_b128 a[116:119], v7 offset:13312\n"
"s_add_u32 s59, s59, 32768\n"
"v_mfma_f32_16x16x32_bf16 v[52:55], a[68:71], a[36:39], v[52:55]\n"
"buffer_load_dwordx4 v1, s[40:43], s61 offen offset:3072 lds\n"
"v_mfma_f32_16x16x32_bf16 v[48:51], a[72:75], a[8:11], v[48:51]\n"
"ds_read_b128 a[120:123], v7 offset:14336\n"
"s_add_u32 m0, m0, 0x1000\n"
"v_mfma_f32_16x16x32_bf16 v[52:55], a[72:75], a[40:43], v[52:55]\n"
"v_mfma_f32_16x16x32_bf16 v[48:51], a[76:79], a[12:15], v[48:51]\n"
"ds_read_b128 a[124:127], v7 offset:15360\n"
"s_waitcnt lgkmcnt(8)\n"
"v_mfma_f32_16x16x32_bf16 v[52:55], a[76:79], a[44:47], v[52:55]\n"
"s_cmp_eq_u32 s59, 163840\n"
"s_cselect_b32 s59, 0, s59\n"
"v_mfma_f32_16x16x32_bf16 v[48:51], a[80:83], a[16:19], v[48:51]\n"
"ds_read_b128 a[64:67], v7 offset:16384\n"
"v_add_u32 v253, s59, v0\n"
"v_mfma_f32_16x16x32_bf16 v[52:55], a[80:83], a[48:51], v[52:55]\n"
"s_add_u32 s63, s63, 64\n"
"v_mfma_f32_16x16x32_bf16 v[48:51], a[84:87], a[20:23], v[48:51]\n"
"ds_read_b128 a[68:71], v7 offset:17408\n"
"v_mfma_f32_16x16x32_bf16 v[52:55], a[84:87], a[52:55], v[52:55]\n"
"buffer_load_dwordx4 v6, s[40:43], s61 offen offset:0 lds\n"
"v_mfma_f32_16x16x32_bf16 v[48:51], a[88:91], a[24:27], v[48:51]\n"
"ds_read_b128 a[72:75], v7 offset:18432\n"
"v_mfma_f32_16x16x32_bf16 v[52:55], a[88:91], a[56:59], v[52:55]\n"
"v_mfma_f32_16x16x32_bf16 v[48:51], a[92:95], a[28:31], v[48:51]\n"
"ds_read_b128 a[76:79], v7 offset:19456\n"
"s_waitcnt lgkmcnt(8)\n"
"v_mfma_f32_16x16x32_bf16 v[52:55], a[92:95], a[60:63], v[52:55]\n"
"v_mfma_f32_16x16x32_bf16 v[56:59], a[96:99], a[0:3], v[24:27]\n"
"ds_read_b128 a[80:83], v7 offset:20480\n"
"v_mfma_f32_16x16x32_bf16 v[60:63], a[96:99], a[32:35], v[28:31]\n"
"v_mfma_f32_16x16x32_bf16 v[56:59], a[100:103], a[4:7], v[56:59]\n"
"ds_read_b128 a[84:87], v7 offset:21504\n"
"v_mfma_f32_16x16x32_bf16 v[60:63], a[100:103], a[36:39], v[60:63]\n"
"buffer_load_dwordx4 v6, s[40:43], s61 offen offset:1024 lds\n"
"v_mfma_f32_16x16x32_bf16 v[56:59], a[104:107], a[8:11], v[56:59]\n"
"ds_read_b128 a[88:91], v7 offset:22528\n"
"v_mfma_f32_16x16x32_bf16 v[60:63], a[104:107], a[40:43], v[60:63]\n"
"v_exp_f32 v48, v48\n"
"v_mfma_f32_16x16x32_bf16 v[56:59], a[108:111], a[12:15], v[56:59]\n"
"ds_read_b128 a[92:95], v7 offset:23552\n"
"s_waitcnt lgkmcnt(8)\n"
"v_mfma_f32_16x16x32_bf16 v[60:63], a[108:111], a[44:47], v[60:63]\n"
"v_exp_f32 v49, v49\n"
"v_mfma_f32_16x16x32_bf16 v[56:59], a[112:115], a[16:19], v[56:59]\n"
"ds_read_b128 a[96:99], v7 offset:24576\n"
"v_mfma_f32_16x16x32_bf16 v[60:63], a[112:115], a[48:51], v[60:63]\n"
"v_exp_f32 v50, v50\n"
"v_mfma_f32_16x16x32_bf16 v[56:59], a[116:119], a[20:23], v[56:59]\n"
"ds_read_b128 a[100:103], v7 offset:25600\n"
"v_mfma_f32_16x16x32_bf16 v[60:63], a[116:119], a[52:55], v[60:63]\n"
"buffer_load_dwordx4 v6, s[40:43], s61 offen offset:2048 lds\n"
"v_mfma_f32_16x16x32_bf16 v[56:59], a[120:123], a[24:27], v[56:59]\n"
"ds_read_b128 a[104:107], v7 offset:26624\n"
"v_mfma_f32_16x16x32_bf16 v[60:63], a[120:123], a[56:59], v[60:63]\n"
"v_exp_f32 v51, v51\n"
"v_mfma_f32_16x16x32_bf16 v[56:59], a[124:127], a[28:31], v[56:59]\n"
"ds_read_b128 a[108:111], v7 offset:27648\n"
"s_waitcnt lgkmcnt(8)\n"
"v_mfma_f32_16x16x32_bf16 v[60:63], a[124:127], a[60:63], v[60:63]\n"
"v_exp_f32 v52, v52\n"
"v_mfma_f32_16x16x32_bf16 v[64:67], a[64:67], a[0:3], v[32:35]\n"
"ds_read_b128 a[112:115], v7 offset:28672\n"
"v_cvt_pk_bf16_f32 v80, v48, v49\n"
"v_mfma_f32_16x16x32_bf16 v[68:71], a[64:67], a[32:35], v[36:39]\n"
"v_exp_f32 v53, v53\n"
"v_mfma_f32_16x16x32_bf16 v[64:67], a[68:71], a[4:7], v[64:67]\n"
"ds_read_b128 a[116:119], v7 offset:29696\n"
"v_cvt_pk_bf16_f32 v81, v50, v51\n"
"v_mfma_f32_16x16x32_bf16 v[68:71], a[68:71], a[36:39], v[68:71]\n"
"buffer_load_dwordx4 v6, s[40:43], s61 offen offset:3072 lds\n"
"s_add_u32 m0, s60, s62\n"
"v_mfma_f32_16x16x32_bf16 v[64:67], a[72:75], a[8:11], v[64:67]\n"
"ds_read_b128 a[120:123], v7 offset:30720\n"
"v_readlane_b32 s61, v2, s58\n"
"v_mfma_f32_16x16x32_bf16 v[68:71], a[72:75], a[40:43], v[68:71]\n"
"v_exp_f32 v54, v54\n"
"v_mfma_f32_16x16x32_bf16 v[64:67], a[76:79], a[12:15], v[64:67]\n"
"ds_read_b128 a[124:127], v7 offset:31744\n"
"s_waitcnt lgkmcnt(8)\n"
"v_mfma_f32_16x16x32_bf16 v[68:71], a[76:79], a[44:47], v[68:71]\n"
"s_waitcnt vmcnt(17)\n"
"s_barrier\n"
"v_mfma_f32_16x16x32_bf16 v[64:67], a[80:83], a[16:19], v[64:67]\n"
"ds_read_b128 a[64:67], v253 offset:0\n"
"s_cmp_lt_u32 s58, 34\n"
"v_mfma_f32_16x16x32_bf16 v[68:71], a[80:83], a[48:51], v[68:71]\n"
"v_exp_f32 v55, v55\n"
"v_mfma_f32_16x16x32_bf16 v[64:67], a[84:87], a[20:23], v[64:67]\n"
"ds_read_b128 a[68:71], v253 offset:1024\n"
"s_cselect_b32 s42, s71, 0\n"
"v_mfma_f32_16x16x32_bf16 v[68:71], a[84:87], a[52:55], v[68:71]\n"
"buffer_load_dwordx4 v1, s[40:43], s61 offen offset:0 lds\n"
"v_cvt_pk_bf16_f32 v88, v52, v53\n"
"v_mfma_f32_16x16x32_bf16 v[64:67], a[88:91], a[24:27], v[64:67]\n"
"ds_read_b128 a[72:75], v253 offset:2048\n"
"s_add_u32 s60, s60, 32768\n"
"v_mfma_f32_16x16x32_bf16 v[68:71], a[88:91], a[56:59], v[68:71]\n"
"v_exp_f32 v56, v56\n"
"v_mfma_f32_16x16x32_bf16 v[64:67], a[92:95], a[28:31], v[64:67]\n"
"ds_read_b128 a[76:79], v253 offset:3072\n"
"s_waitcnt lgkmcnt(8)\n"
"v_mfma_f32_16x16x32_bf16 v[68:71], a[92:95], a[60:63], v[68:71]\n"
"s_cmp_eq_u32 s60, 163840\n"
"s_cselect_b32 s60, 0, s60\n"
"v_mfma_f32_16x16x32_bf16 v[72:75], a[96:99], a[0:3], v[40:43]\n"
"ds_read_b128 a[80:83], v253 offset:4096\n"
"s_add_u32 s58, s58, 1\n"
"v_mfma_f32_16x16x32_bf16 v[76:79], a[96:99], a[32:35], v[44:47]\n"
"v_exp_f32 v57, v57\n"
"v_mfma_f32_16x16x32_bf16 v[72:75], a[100:103], a[4:7], v[72:75]\n"
"ds_read_b128 a[84:87], v253 offset:5120\n"
"v_mov_b32 v7, v253\n"
"v_mfma_f32_16x16x32_bf16 v[76:79], a[100:103], a[36:39], v[76:79]\n"
"buffer_load_dwordx4 v1, s[40:43], s61 offen offset:1024 lds\n"
"v_cvt_pk_bf16_f32 v89, v54, v55\n"
"v_mfma_f32_16x16x32_bf16 v[72:75], a[104:107], a[8:11], v[72:75]\n"
"ds_read_b128 a[88:91], v253 offset:6144\n"
"v_mfma_f32_16x16x32_bf16 v[76:79], a[104:107], a[40:43], v[76:79]\n"
"v_exp_f32 v58, v58\n"
"v_mfma_f32_16x16x32_bf16 v[72:75], a[108:111], a[12:15], v[72:75]\n"
"ds_read_b128 a[92:95], v253 offset:7168\n"
"s_waitcnt lgkmcnt(8)\n"
"v_mfma_f32_16x16x32_bf16 v[76:79], a[108:111], a[44:47], v[76:79]\n"
"v_mfma_f32_16x16x32_bf16 v[72:75], a[112:115], a[16:19], v[72:75]\n"
"ds_read_b128 a[96:99], v253 offset:8192\n"
"v_mfma_f32_16x16x32_bf16 v[76:79], a[112:115], a[48:51], v[76:79]\n"
"v_exp_f32 v59, v59\n"
"v_mfma_f32_16x16x32_bf16 v[72:75], a[116:119], a[20:23], v[72:75]\n"
"ds_read_b128 a[100:103], v253 offset:9216\n"
"v_cvt_pk_bf16_f32 v82, v56, v57\n"
"v_mfma_f32_16x16x32_bf16 v[76:79], a[116:119], a[52:55], v[76:79]\n"
"buffer_load_dwordx4 v1, s[40:43], s61 offen offset:2048 lds\n"
"v_cvt_pk_bf16_f32 v83, v58, v59\n"
"v_mfma_f32_16x16x32_bf16 v[72:75], a[120:123], a[24:27], v[72:75]\n"
"ds_read_b128 a[104:107], v253 offset:10240\n"
"v_mfma_f32_16x16x32_bf16 v[76:79], a[120:123], a[56:59], v[76:79]\n"
"v_exp_f32 v60, v60\n"
"v_mfma_f32_16x16x32_bf16 v[72:75], a[124:127], a[28:31], v[72:75]\n"
"ds_read_b128 a[108:111], v253 offset:11264\n"
"s_waitcnt lgkmcnt(8)\n"
"v_mfma_f32_16x16x32_bf16 v[76:79], a[124:127], a[60:63], v[76:79]\n"
"v_exp_f32 v61, v61\n"
"v_mfma_f32_16x16x32_bf16 v[112:115], a[64:67], v[96:99], v[112:115]\n"
"ds_read_b128 a[112:115], v7 offset:12288\n"
"v_mfma_f32_16x16x32_bf16 v[116:119], a[64:67], v[104:107], v[116:119]\n"
"v_exp_f32 v62, v62\n"
"v_mfma_f32_16x16x32_bf16 v[112:115], a[68:71], v[100:103], v[112:115]\n"
"ds_read_b128 a[116:119], v7 offset:13312\n"
"s_add_u32 s59, s59, 32768\n"
"v_mfma_f32_16x16x32_bf16 v[116:119], a[68:71], v[108:111], v[116:119]\n"
"buffer_load_dwordx4 v1, s[40:43], s61 offen offset:3072 lds\n"
"v_mfma_f32_16x16x32_bf16 v[120:123], a[72:75], v[96:99], v[120:123]\n"
"ds_read_b128 a[120:123], v7 offset:14336\n"
"s_add_u32 m0, m0, 0x1000\n"
"v_mfma_f32_16x16x32_bf16 v[124:127], a[72:75], v[104:107], v[124:127]\n"
"v_exp_f32 v63, v63\n"
"v_mfma_f32_16x16x32_bf16 v[120:123], a[76:79], v[100:103], v[120:123]\n"
"ds_read_b128 a[124:127], v7 offset:15360\n"
"s_waitcnt lgkmcnt(8)\n"
"v_mfma_f32_16x16x32_bf16 v[124:127], a[76:79], v[108:111], v[124:127]\n"
"s_cmp_eq_u32 s59, 163840\n"
"s_cselect_b32 s59, 0, s59\n"
"v_mfma_f32_16x16x32_bf16 v[128:131], a[80:83], v[96:99], v[128:131]\n"
"ds_read_b128 a[64:67], v7 offset:16384\n"
"v_add_u32 v253, s59, v0\n"
"v_mfma_f32_16x16x32_bf16 v[132:135], a[80:83], v[104:107], v[132:135]\n"
"v_exp_f32 v64, v64\n"
"v_mfma_f32_16x16x32_bf16 v[128:131], a[84:87], v[100:103], v[128:131]\n"
"ds_read_b128 a[68:71], v7 offset:17408\n"
"v_cvt_pk_bf16_f32 v90, v60, v61\n"
"v_mfma_f32_16x16x32_bf16 v[132:135], a[84:87], v[108:111], v[132:135]\n"
"buffer_load_dwordx4 v6, s[40:43], s61 offen offset:0 lds\n"
"v_cvt_pk_bf16_f32 v91, v62, v63\n"
"v_mfma_f32_16x16x32_bf16 v[136:139], a[88:91], v[96:99], v[136:139]\n"
"ds_read_b128 a[72:75], v7 offset:18432\n"
"s_waitcnt vmcnt(16)\n"
"buffer_load_dword v247, v246, s[52:55], 0 offen sc0 sc1\n"
"v_mfma_f32_16x16x32_bf16 v[140:143], a[88:91], v[104:107], v[140:143]\n"
"v_exp_f32 v65, v65\n"
"v_mfma_f32_16x16x32_bf16 v[136:139], a[92:95], v[100:103], v[136:139]\n"
"ds_read_b128 a[76:79], v7 offset:19456\n"
"s_waitcnt lgkmcnt(8)\n"
"v_mfma_f32_16x16x32_bf16 v[140:143], a[92:95], v[108:111], v[140:143]\n"
"v_exp_f32 v66, v66\n"
"v_mfma_f32_16x16x32_bf16 v[144:147], a[96:99], v[96:99], v[144:147]\n"
"ds_read_b128 a[80:83], v7 offset:20480\n"
"v_mfma_f32_16x16x32_bf16 v[148:151], a[96:99], v[104:107], v[148:151]\n"
"v_exp_f32 v67, v67\n"
"v_mfma_f32_16x16x32_bf16 v[144:147], a[100:103], v[100:103], v[144:147]\n"
"ds_read_b128 a[84:87], v7 offset:21504\n"
"v_cvt_pk_bf16_f32 v84, v64, v65\n"
"v_mfma_f32_16x16x32_bf16 v[148:151], a[100:103], v[108:111], v[148:151]\n"
"buffer_load_dwordx4 v6, s[40:43], s61 offen offset:1024 lds\n"
"v_cvt_pk_bf16_f32 v85, v66, v67\n"
"v_mfma_f32_16x16x32_bf16 v[152:155], a[104:107], v[96:99], v[152:155]\n"
"ds_read_b128 a[88:91], v7 offset:22528\n"
"v_mfma_f32_16x16x32_bf16 v[156:159], a[104:107], v[104:107], v[156:159]\n"
"v_exp_f32 v68, v68\n"
"v_mfma_f32_16x16x32_bf16 v[152:155], a[108:111], v[100:103], v[152:155]\n"
"ds_read_b128 a[92:95], v7 offset:23552\n"
"s_waitcnt lgkmcnt(8)\n"
"v_mfma_f32_16x16x32_bf16 v[156:159], a[108:111], v[108:111], v[156:159]\n"
"v_mfma_f32_16x16x32_bf16 v[160:163], a[112:115], v[96:99], v[160:163]\n"
"ds_read_b128 a[96:99], v7 offset:24576\n"
"v_mfma_f32_16x16x32_bf16 v[164:167], a[112:115], v[104:107], v[164:167]\n"
"v_exp_f32 v69, v69\n"
"v_mfma_f32_16x16x32_bf16 v[160:163], a[116:119], v[100:103], v[160:163]\n"
"ds_read_b128 a[100:103], v7 offset:25600\n"
"v_mfma_f32_16x16x32_bf16 v[164:167], a[116:119], v[108:111], v[164:167]\n"
"buffer_load_dwordx4 v6, s[40:43], s61 offen offset:2048 lds\n"
"v_mfma_f32_16x16x32_bf16 v[168:171], a[120:123], v[96:99], v[168:171]\n"
"ds_read_b128 a[104:107], v7 offset:26624\n"
"v_mfma_f32_16x16x32_bf16 v[172:175], a[120:123], v[104:107], v[172:175]\n"
"v_exp_f32 v70, v70\n"
"v_mfma_f32_16x16x32_bf16 v[168:171], a[124:127], v[100:103], v[168:171]\n"
"ds_read_b128 a[108:111], v7 offset:27648\n"
"s_waitcnt lgkmcnt(8)\n"
"v_mfma_f32_16x16x32_bf16 v[172:175], a[124:127], v[108:111], v[172:175]\n"
"v_exp_f32 v71, v71\n"
"v_mfma_f32_16x16x32_bf16 v[176:179], a[64:67], v[96:99], v[176:179]\n"
"ds_read_b128 a[112:115], v7 offset:28672\n"
"v_cvt_pk_bf16_f32 v92, v68, v69\n"
"v_mfma_f32_16x16x32_bf16 v[180:183], a[64:67], v[104:107], v[180:183]\n"
"v_exp_f32 v72, v72\n"
"v_mfma_f32_16x16x32_bf16 v[176:179], a[68:71], v[100:103], v[176:179]\n"
"ds_read_b128 a[116:119], v7 offset:29696\n"
"v_cvt_pk_bf16_f32 v93, v70, v71\n"
"v_mfma_f32_16x16x32_bf16 v[180:183], a[68:71], v[108:111], v[180:183]\n"
"buffer_load_dwordx4 v6, s[40:43], s61 offen offset:3072 lds\n"
"s_add_u32 m0, s60, s62\n"
"v_mfma_f32_16x16x32_bf16 v[184:187], a[72:75], v[96:99], v[184:187]\n"
"ds_read_b128 a[120:123], v7 offset:30720\n"
"v_readlane_b32 s61, v2, s58\n"
"v_mfma_f32_16x16x32_bf16 v[188:191], a[72:75], v[104:107], v[188:191]\n"
"v_exp_f32 v73, v73\n"
"v_mfma_f32_16x16x32_bf16 v[184:187], a[76:79], v[100:103], v[184:187]\n"
"ds_read_b128 a[124:127], v7 offset:31744\n"
"s_waitcnt lgkmcnt(8)\n"
"v_mfma_f32_16x16x32_bf16 v[188:191], a[76:79], v[108:111], v[188:191]\n"
"s_waitcnt vmcnt(17)\n"
"s_barrier\n"
"v_mfma_f32_16x16x32_bf16 v[192:195], a[80:83], v[96:99], v[192:195]\n"
"ds_read_b128 a[64:67], v253 offset:0\n"
"s_cmp_lt_u32 s58, 34\n"
"v_mfma_f32_16x16x32_bf16 v[196:199], a[80:83], v[104:107], v[196:199]\n"
"v_exp_f32 v74, v74\n"
"v_mfma_f32_16x16x32_bf16 v[192:195], a[84:87], v[100:103], v[192:195]\n"
"ds_read_b128 a[68:71], v253 offset:1024\n"
"s_cselect_b32 s42, s71, 0\n"
"v_mfma_f32_16x16x32_bf16 v[196:199], a[84:87], v[108:111], v[196:199]\n"
"buffer_load_dwordx4 v1, s[40:43], s61 offen offset:0 lds\n"
"v_mfma_f32_16x16x32_bf16 v[200:203], a[88:91], v[96:99], v[200:203]\n"
"ds_read_b128 a[72:75], v253 offset:2048\n"
"s_add_u32 s60, s60, 32768\n"
"v_mfma_f32_16x16x32_bf16 v[204:207], a[88:91], v[104:107], v[204:207]\n"
"v_exp_f32 v75, v75\n"
"v_mfma_f32_16x16x32_bf16 v[200:203], a[92:95], v[100:103], v[200:203]\n"
"ds_read_b128 a[76:79], v253 offset:3072\n"
"s_waitcnt lgkmcnt(8)\n"
"v_mfma_f32_16x16x32_bf16 v[204:207], a[92:95], v[108:111], v[204:207]\n"
"s_cmp_eq_u32 s60, 163840\n"
"s_cselect_b32 s60, 0, s60\n"
"v_mfma_f32_16x16x32_bf16 v[208:211], a[96:99], v[96:99], v[208:211]\n"
"ds_read_b128 a[80:83], v253 offset:4096\n"
"s_add_u32 s58, s58, 1\n"
"v_mfma_f32_16x16x32_bf16 v[212:215], a[96:99], v[104:107], v[212:215]\n"
"v_exp_f32 v76, v76\n"
"v_mfma_f32_16x16x32_bf16 v[208:211], a[100:103], v[100:103], v[208:211]\n"
"ds_read_b128 a[84:87], v253 offset:5120\n"
"v_mov_b32 v7, v253\n"
"v_mfma_f32_16x16x32_bf16 v[212:215], a[100:103], v[108:111], v[212:215]\n"
"buffer_load_dwordx4 v1, s[40:43], s61 offen offset:1024 lds\n"
"v_cvt_pk_bf16_f32 v86, v72, v73\n"
"v_mfma_f32_16x16x32_bf16 v[216:219], a[104:107], v[96:99], v[216:219]\n"
"ds_read_b128 a[88:91], v253 offset:6144\n"
"v_cvt_pk_bf16_f32 v87, v74, v75\n"
"v_mfma_f32_16x16x32_bf16 v[220:223], a[104:107], v[104:107], v[220:223]\n"
"v_exp_f32 v77, v77\n"
"v_mfma_f32_16x16x32_bf16 v[216:219], a[108:111], v[100:103], v[216:219]\n"
"ds_read_b128 a[92:95], v253 offset:7168\n"
"s_waitcnt lgkmcnt(8)\n"
"v_mfma_f32_16x16x32_bf16 v[220:223], a[108:111], v[108:111], v[220:223]\n"
"v_mfma_f32_16x16x32_bf16 v[224:227], a[112:115], v[96:99], v[224:227]\n"
"ds_read_b128 a[96:99], v253 offset:8192\n"
"v_mfma_f32_16x16x32_bf16 v[228:231], a[112:115], v[104:107], v[228:231]\n"
"v_exp_f32 v78, v78\n"
"v_mfma_f32_16x16x32_bf16 v[224:227], a[116:119], v[100:103], v[224:227]\n"
"ds_read_b128 a[100:103], v253 offset:9216\n"
"v_mfma_f32_16x16x32_bf16 v[228:231], a[116:119], v[108:111], v[228:231]\n"
"buffer_load_dwordx4 v1, s[40:43], s61 offen offset:2048 lds\n"
"v_mfma_f32_16x16x32_bf16 v[232:235], a[120:123], v[96:99], v[232:235]\n"
"ds_read_b128 a[104:107], v253 offset:10240\n"
"v_mfma_f32_16x16x32_bf16 v[236:239], a[120:123], v[104:107], v[236:239]\n"
"v_exp_f32 v79, v79\n"
"v_mfma_f32_16x16x32_bf16 v[232:235], a[124:127], v[100:103], v[232:235]\n"
"ds_read_b128 a[108:111], v253 offset:11264\n"
"s_waitcnt lgkmcnt(8)\n"
"v_mfma_f32_16x16x32_bf16 v[236:239], a[124:127], v[108:111], v[236:239]\n"
"v_cvt_pk_bf16_f32 v94, v76, v77\n"
"v_cvt_pk_bf16_f32 v95, v78, v79\n"
"v_mfma_f32_16x16x32_bf16 a[128:131], a[136:139], v[96:99], a[128:131]\n"
"v_mfma_f32_16x16x32_bf16 a[128:131], a[136:139], v[100:103], a[128:131]\n"
"v_mfma_f32_16x16x32_bf16 a[132:135], a[136:139], v[104:107], a[132:135]\n"
"s_cmp_ge_i32 s63, s77\n"
"v_mfma_f32_16x16x32_bf16 a[132:135], a[136:139], v[108:111], a[132:135]\n"
"s_cbranch_scc0 LA1%=\n"
"LEV1%=:\n"
"s_cmp_eq_u32 s63, s78\n"
"s_cbranch_scc1 LE1%=\n"
"s_cmp_lt_i32 s63, 0\n"
"s_cbranch_scc1 LM2%=\n"
"v_cvt_f32_i32 v12, s63\n"
"v_add_f32 v12, v12, v3\n"
"v_mul_f32 v12, 0xf1c9f2ca, v12\n"
"v_add_f32 v16, 0x7149f2ca, v12\n"
"v_add_f32 v17, 0xf149f2ca, v12\n"
"v_add_f32 v18, 0xf2177617, v12\n"
"v_add_f32 v19, 0xf27c6f7c, v12\n"
"v_add_f32 v20, 0x74cb86af, v12\n"
"v_add_f32 v21, 0x74c85ee4, v12\n"
"v_add_f32 v22, 0x74c53719, v12\n"
"v_add_f32 v23, 0x74c20f4e, v12\n"
"v_add_f32 v24, 0xf3c3a333, v12\n"
"v_add_f32 v25, 0xf3d04260, v12\n"
"v_add_f32 v26, 0xf3dce18d, v12\n"
"v_add_f32 v27, 0xf3e980b9, v12\n"
"v_add_f32 v28, 0x749909fd, v12\n"
"v_add_f32 v29, 0x7495e232, v12\n"
"v_add_f32 v30, 0x7492ba67, v12\n"
"v_add_f32 v31, 0x748f929b, v12\n"
"v_add_f32 v32, 0xf446caff, v12\n"
"v_add_f32 v33, 0xf44d1a95, v12\n"
"v_add_f32 v34, 0xf4536a2b, v12\n"
"v_add_f32 v35, 0xf459b9c2, v12\n"
"v_add_f32 v36, 0x744d1a95, v12\n"
"v_add_f32 v37, 0x7446caff, v12\n"
"v_add_f32 v38, 0x74407b68, v12\n"
"v_add_f32 v39, 0x743a2bd2, v12\n"
"v_add_f32 v40, 0xf495e232, v12\n"
"v_add_f32 v41, 0xf49909fd, v12\n"
"v_add_f32 v42, 0xf49c31c8, v12\n"
"v_add_f32 v43, 0xf49f5993, v12\n"
"v_add_f32 v44, 0x73d04260, v12\n"
"v_add_f32 v45, 0x73c3a333, v12\n"
"v_add_f32 v46, 0x73b70407, v12\n"
"v_add_f32 v47, 0x73aa64da, v12\n"
"v_min_f32 v16, v16, v8\n"
"v_min_f32 v17, v17, v8\n"
"v_min_f32 v18, v18, v8\n"
"v_min_f32 v19, v19, v8\n"
"v_min_f32 v20, v20, v9\n"
"v_min_f32 v21, v21, v9\n"
"v_min_f32 v22, v22, v9\n"
"v_min_f32 v23, v23, v9\n"
"v_min_f32 v24, v24, v8\n"
"v_min_f32 v25, v25, v8\n"
"v_min_f32 v26, v26, v8\n"
"v_min_f32 v27, v27, v8\n"
"v_min_f32 v28, v28, v9\n"
"v_min_f32 v29, v29, v9\n"
"v_min_f32 v30, v30, v9\n"
"v_min_f32 v31, v31, v9\n"
"v_min_f32 v32, v32, v8\n"
"v_min_f32 v33, v33, v8\n"
"v_min_f32 v34, v34, v8\n"
"v_min_f32 v35, v35, v8\n"
"v_min_f32 v36, v36, v9\n"
"v_min_f32 v37, v37, v9\n"
"v_min_f32 v38, v38, v9\n"
"v_min_f32 v39, v39, v9\n"
"v_min_f32 v40, v40, v8\n"
"v_min_f32 v41, v41, v8\n"
"v_min_f32 v42, v42, v8\n"
"v_min_f32 v43, v43, v8\n"
"v_min_f32 v44, v44, v9\n"
"v_min_f32 v45, v45, v9\n"
"v_min_f32 v46, v46, v9\n"
"v_min_f32 v47, v47, v9\n"
"s_nop 1\n"
"LM2%=:\n"
"s_cmp_eq_u32 s63, s89\n"
"s_cbranch_scc0 LA1%=\n"
"s_min_i32 s77, s78, 0\n"
"v_mfma_f32_16x16x32_bf16 v[48:51], a[64:67], a[0:3], v[16:19]\n"
"ds_read_b128 a[112:115], v7 offset:12288\n"
"buffer_load_dwordx4 a[176:179], v0, s[52:55], s67 offen offset:0\n"
"v_mfma_f32_16x16x32_bf16 v[52:55], a[64:67], a[32:35], v[20:23]\n"
"v_mfma_f32_16x16x32_bf16 v[48:51], a[68:71], a[4:7], v[48:51]\n"
"ds_read_b128 a[116:119], v7 offset:13312\n"
"s_add_u32 s59, s59, 32768\n"
"v_mfma_f32_16x16x32_bf16 v[52:55], a[68:71], a[36:39], v[52:55]\n"
"buffer_load_dwordx4 v1, s[40:43], s61 offen offset:3072 lds\n"
"v_mfma_f32_16x16x32_bf16 v[48:51], a[72:75], a[8:11], v[48:51]\n"
"ds_read_b128 a[120:123], v7 offset:14336\n"
"s_add_u32 m0, m0, 0x1000\n"
"v_mfma_f32_16x16x32_bf16 v[52:55], a[72:75], a[40:43], v[52:55]\n"
"buffer_load_dwordx4 a[180:183], v0, s[52:55], s67 offen offset:1024\n"
"v_mfma_f32_16x16x32_bf16 v[48:51], a[76:79], a[12:15], v[48:51]\n"
"ds_read_b128 a[124:127], v7 offset:15360\n"
"s_waitcnt lgkmcnt(8)\n"
"v_mfma_f32_16x16x32_bf16 v[52:55], a[76:79], a[44:47], v[52:55]\n"
"s_cmp_eq_u32 s59, 163840\n"
"s_cselect_b32 s59, 0, s59\n"
"v_mfma_f32_16x16x32_bf16 v[48:51], a[80:83], a[16:19], v[48:51]\n"
"ds_read_b128 a[64:67], v7 offset:16384\n"
"v_add_u32 v253, s59, v0\n"
"buffer_load_dwordx4 a[184:187], v0, s[52:55], s67 offen offset:2048\n"
"v_mfma_f32_16x16x32_bf16 v[52:55], a[80:83], a[48:51], v[52:55]\n"
"s_add_u32 s63, s63, 64\n"
"v_mfma_f32_16x16x32_bf16 v[48:51], a[84:87], a[20:23], v[48:51]\n"
"ds_read_b128 a[68:71], v7 offset:17408\n"
"v_mfma_f32_16x16x32_bf16 v[52:55], a[84:87], a[52:55], v[52:55]\n"
"buffer_load_dwordx4 v6, s[40:43], s61 offen offset:0 lds\n"
"v_mfma_f32_16x16x32_bf16 v[48:51], a[88:91], a[24:27], v[48:51]\n"
"ds_read_b128 a[72:75], v7 offset:18432\n"
"v_mfma_f32_16x16x32_bf16 v[52:55], a[88:91], a[56:59], v[52:55]\n"
"buffer_load_dwordx4 a[188:191], v0, s[52:55], s67 offen offset:3072\n"
"v_mfma_f32_16x16x32_bf16 v[48:51], a[92:95], a[28:31], v[48:51]\n"
"ds_read_b128 a[76:79], v7 offset:19456\n"
"s_waitcnt lgkmcnt(8)\n"
"v_mfma_f32_16x16x32_bf16 v[52:55], a[92:95], a[60:63], v[52:55]\n"
"v_mfma_f32_16x16x32_bf16 v[56:59], a[96:99], a[0:3], v[24:27]\n"
"ds_read_b128 a[80:83], v7 offset:20480\n"
"buffer_load_dwordx4 a[192:195], v0, s[52:55], s65 offen offset:0\n"
"v_mfma_f32_16x16x32_bf16 v[60:63], a[96:99], a[32:35], v[28:31]\n"
"v_mfma_f32_16x16x32_bf16 v[56:59], a[100:103], a[4:7], v[56:59]\n"
"ds_read_b128 a[84:87], v7 offset:21504\n"
"v_mfma_f32_16x16x32_bf16 v[60:63], a[100:103], a[36:39], v[60:63]\n"
"buffer_load_dwordx4 v6, s[40:43], s61 offen offset:1024 lds\n"
"v_mfma_f32_16x16x32_bf16 v[56:59], a[104:107], a[8:11], v[56:59]\n"
"ds_read_b128 a[88:91], v7 offset:22528\n"
"v_mfma_f32_16x16x32_bf16 v[60:63], a[104:107], a[40:43], v[60:63]\n"
"v_exp_f32 v48, v48\n"
"buffer_load_dwordx4 a[196:199], v0, s[52:55], s65 offen offset:1024\n"
"v_mfma_f32_16x16x32_bf16 v[56:59], a[108:111], a[12:15], v[56:59]\n"
"ds_read_b128 a[92:95], v7 offset:23552\n"
"s_waitcnt lgkmcnt(8)\n"
"v_mfma_f32_16x16x32_bf16 v[60:63], a[108:111], a[44:47], v[60:63]\n"
"v_exp_f32 v49, v49\n"
"v_mfma_f32_16x16x32_bf16 v[56:59], a[112:115], a[16:19], v[56:59]\n"
"ds_read_b128 a[96:99], v7 offset:24576\n"
"buffer_load_dwordx4 a[200:203], v0, s[52:55], s65 offen offset:2048\n"
"v_mfma_f32_16x16x32_bf16 v[60:63], a[112:115], a[48:51], v[60:63]\n"
"v_exp_f32 v50, v50\n"
"v_mfma_f32_16x16x32_bf16 v[56:59], a[116:119], a[20:23], v[56:59]\n"
"ds_read_b128 a[100:103], v7 offset:25600\n"
"v_mfma_f32_16x16x32_bf16 v[60:63], a[116:119], a[52:55], v[60:63]\n"
"buffer_load_dwordx4 v6, s[40:43], s61 offen offset:2048 lds\n"
"v_mfma_f32_16x16x32_bf16 v[56:59], a[120:123], a[24:27], v[56:59]\n"
"ds_read_b128 a[104:107], v7 offset:26624\n"
"v_mfma_f32_16x16x32_bf16 v[60:63], a[120:123], a[56:59], v[60:63]\n"
"v_exp_f32 v51, v51\n"
"buffer_load_dwordx4 a[204:207], v0, s[52:55], s65 offen offset:3072\n"
"v_mfma_f32_16x16x32_bf16 v[56:59], a[124:127], a[28:31], v[56:59]\n"
"ds_read_b128 a[108:111], v7 offset:27648\n"
"s_waitcnt lgkmcnt(8)\n"
"v_mfma_f32_16x16x32_bf16 v[60:63], a[124:127], a[60:63], v[60:63]\n"
"v_exp_f32 v52, v52\n"
"v_mfma_f32_16x16x32_bf16 v[64:67], a[64:67], a[0:3], v[32:35]\n"
"ds_read_b128 a[112:115], v7 offset:28672\n"
"v_cvt_pk_bf16_f32 v96, v48, v49\n"
"buffer_load_dwordx4 a[208:211], v0, s[52:55], s66 offen offset:0\n"
"v_mfma_f32_16x16x32_bf16 v[68:71], a[64:67], a[32:35], v[36:39]\n"
"v_exp_f32 v53, v53\n"
"v_mfma_f32_16x16x32_bf16 v[64:67], a[68:71], a[4:7], v[64:67]\n"
"ds_read_b128 a[116:119], v7 offset:29696\n"
"v_cvt_pk_bf16_f32 v97, v50, v51\n"
"v_mfma_f32_16x16x32_bf16 v[68:71], a[68:71], a[36:39], v[68:71]\n"
"buffer_load_dwordx4 v6, s[40:43], s61 offen offset:3072 lds\n"
"s_add_u32 m0, s60, s62\n"
"v_mfma_f32_16x16x32_bf16 v[64:67], a[72:75], a[8:11], v[64:67]\n"
"ds_read_b128 a[120:123], v7 offset:30720\n"
"v_readlane_b32 s61, v2, s58\n"
"v_mfma_f32_16x16x32_bf16 v[68:71], a[72:75], a[40:43], v[68:71]\n"
"v_exp_f32 v54, v54\n"
"buffer_load_dwordx4 a[212:215], v0, s[52:55], s66 offen offset:1024\n"
"v_mfma_f32_16x16x32_bf16 v[64:67], a[76:79], a[12:15], v[64:67]\n"
"ds_read_b128 a[124:127], v7 offset:31744\n"
"s_waitcnt lgkmcnt(8)\n"
"v_mfma_f32_16x16x32_bf16 v[68:71], a[76:79], a[44:47], v[68:71]\n"
"s_waitcnt vmcnt(27)\n"
"s_barrier\n"
"v_mfma_f32_16x16x32_bf16 v[64:67], a[80:83], a[16:19], v[64:67]\n"
"ds_read_b128 a[64:67], v253 offset:0\n"
"s_cmp_lt_u32 s58, 34\n"
"buffer_load_dwordx4 a[216:219], v0, s[52:55], s66 offen offset:2048\n"
"v_mfma_f32_16x16x32_bf16 v[68:71], a[80:83], a[48:51], v[68:71]\n"
"v_exp_f32 v55, v55\n"
"v_mfma_f32_16x16x32_bf16 v[64:67], a[84:87], a[20:23], v[64:67]\n"
"ds_read_b128 a[68:71], v253 offset:1024\n"
"s_cselect_b32 s42, s71, 0\n"
"v_mfma_f32_16x16x32_bf16 v[68:71], a[84:87], a[52:55], v[68:71]\n"
"buffer_load_dwordx4 v1, s[40:43], s61 offen offset:0 lds\n"
"v_cvt_pk_bf16_f32 v104, v52, v53\n"
"v_mfma_f32_16x16x32_bf16 v[64:67], a[88:91], a[24:27], v[64:67]\n"
"ds_read_b128 a[72:75], v253 offset:2048\n"
"s_add_u32 s60, s60, 32768\n"
"v_mfma_f32_16x16x32_bf16 v[68:71], a[88:91], a[56:59], v[68:71]\n"
"v_exp_f32 v56, v56\n"
"buffer_load_dwordx4 a[220:223], v0, s[52:55], s66 offen offset:3072\n"
"v_mfma_f32_16x16x32_bf16 v[64:67], a[92:95], a[28:31], v[64:67]\n"
"ds_read_b128 a[76:79], v253 offset:3072\n"
"s_waitcnt lgkmcnt(8)\n"
"v_mfma_f32_16x16x32_bf16 v[68:71], a[92:95], a[60:63], v[68:71]\n"
"s_cmp_eq_u32 s60, 163840\n"
"s_cselect_b32 s60, 0, s60\n"
"v_mfma_f32_16x16x32_bf16 v[72:75], a[96:99], a[0:3], v[40:43]\n"
"ds_read_b128 a[80:83], v253 offset:4096\n"
"s_add_u32 s58, s58, 1\n"
"buffer_load_dwordx4 a[224:227], v0, s[52:55], s70 offen offset:0\n"
"v_mfma_f32_16x16x32_bf16 v[76:79], a[96:99], a[32:35], v[44:47]\n"
"v_exp_f32 v57, v57\n"
"v_mfma_f32_16x16x32_bf16 v[72:75], a[100:103], a[4:7], v[72:75]\n"
"ds_read_b128 a[84:87], v253 offset:5120\n"
"v_mov_b32 v7, v253\n"
"v_mfma_f32_16x16x32_bf16 v[76:79], a[100:103], a[36:39], v[76:79]\n"
"buffer_load_dwordx4 v1, s[40:43], s61 offen offset:1024 lds\n"
"v_cvt_pk_bf16_f32 v105, v54, v55\n"
"v_mfma_f32_16x16x32_bf16 v[72:75], a[104:107], a[8:11], v[72:75]\n"
"ds_read_b128 a[88:91], v253 offset:6144\n"
"v_mfma_f32_16x16x32_bf16 v[76:79], a[104:107], a[40:43], v[76:79]\n"
"v_exp_f32 v58, v58\n"
"buffer_load_dwordx4 a[228:231], v0, s[52:55], s70 offen offset:1024\n"
"v_mfma_f32_16x16x32_bf16 v[72:75], a[108:111], a[12:15], v[72:75]\n"
"ds_read_b128 a[92:95], v253 offset:7168\n"
"s_waitcnt lgkmcnt(8)\n"
"v_mfma_f32_16x16x32_bf16 v[76:79], a[108:111], a[44:47], v[76:79]\n"
"v_mfma_f32_16x16x32_bf16 v[72:75], a[112:115], a[16:19], v[72:75]\n"
"ds_read_b128 a[96:99], v253 offset:8192\n"
"buffer_load_dwordx4 a[232:235], v0, s[52:55], s70 offen offset:2048\n"
"v_mfma_f32_16x16x32_bf16 v[76:79], a[112:115], a[48:51], v[76:79]\n"
"v_exp_f32 v59, v59\n"
"v_mfma_f32_16x16x32_bf16 v[72:75], a[116:119], a[20:23], v[72:75]\n"
"ds_read_b128 a[100:103], v253 offset:9216\n"
"v_cvt_pk_bf16_f32 v98, v56, v57\n"
"v_mfma_f32_16x16x32_bf16 v[76:79], a[116:119], a[52:55], v[76:79]\n"
"buffer_load_dwordx4 v1, s[40:43], s61 offen offset:2048 lds\n"
"v_cvt_pk_bf16_f32 v99, v58, v59\n"
"v_mfma_f32_16x16x32_bf16 v[72:75], a[120:123], a[24:27], v[72:75]\n"
"ds_read_b128 a[104:107], v253 offset:10240\n"
"v_mfma_f32_16x16x32_bf16 v[76:79], a[120:123], a[56:59], v[76:79]\n"
"v_exp_f32 v60, v60\n"
"buffer_load_dwordx4 a[236:239], v0, s[52:55], s70 offen offset:3072\n"
"v_mfma_f32_16x16x32_bf16 v[72:75], a[124:127], a[28:31], v[72:75]\n"
"ds_read_b128 a[108:111], v253 offset:11264\n"
"s_waitcnt lgkmcnt(8)\n"
"v_mfma_f32_16x16x32_bf16 v[76:79], a[124:127], a[60:63], v[76:79]\n"
"v_exp_f32 v61, v61\n"
"s_branch LB1%=\n"
"LEV0%=:\n"
"s_cmp_eq_u32 s63, s78\n"
"s_cbranch_scc1 LBF0%=\n"
"v_cvt_f32_i32 v12, s63\n"
"v_add_f32 v12, v12, v3\n"
"v_mul_f32 v12, 0xf1c9f2ca, v12\n"
"v_add_f32 v16, 0x7149f2ca, v12\n"
"v_add_f32 v17, 0xf149f2ca, v12\n"
"v_add_f32 v18, 0xf2177617, v12\n"
"v_add_f32 v19, 0xf27c6f7c, v12\n"
"v_add_f32 v20, 0x74cb86af, v12\n"
"v_add_f32 v21, 0x74c85ee4, v12\n"
"v_add_f32 v22, 0x74c53719, v12\n"
"v_add_f32 v23, 0x74c20f4e, v12\n"
"v_add_f32 v24, 0xf3c3a333, v12\n"
"v_add_f32 v25, 0xf3d04260, v12\n"
"v_add_f32 v26, 0xf3dce18d, v12\n"
"v_add_f32 v27, 0xf3e980b9, v12\n"
"v_add_f32 v28, 0x749909fd, v12\n"
"v_add_f32 v29, 0x7495e232, v12\n"
"v_add_f32 v30, 0x7492ba67, v12\n"
"v_add_f32 v31, 0x748f929b, v12\n"
"v_add_f32 v32, 0xf446caff, v12\n"
"v_add_f32 v33, 0xf44d1a95, v12\n"
"v_add_f32 v34, 0xf4536a2b, v12\n"
"v_add_f32 v35, 0xf459b9c2, v12\n"
"v_add_f32 v36, 0x744d1a95, v12\n"
"v_add_f32 v37, 0x7446caff, v12\n"
"v_add_f32 v38, 0x74407b68, v12\n"
"v_add_f32 v39, 0x743a2bd2, v12\n"
"v_add_f32 v40, 0xf495e232, v12\n"
"v_add_f32 v41, 0xf49909fd, v12\n"
"v_add_f32 v42, 0xf49c31c8, v12\n"
"v_add_f32 v43, 0xf49f5993, v12\n"
"v_add_f32 v44, 0x73d04260, v12\n"
"v_add_f32 v45, 0x73c3a333, v12\n"
"v_add_f32 v46, 0x73b70407, v12\n"
"v_add_f32 v47, 0x73aa64da, v12\n"
"v_min_f32 v16, v16, v8\n"
"v_min_f32 v17, v17, v8\n"
"v_min_f32 v18, v18, v8\n"
"v_min_f32 v19, v19, v8\n"
"v_min_f32 v20, v20, v9\n"
"v_min_f32 v21, v21, v9\n"
"v_min_f32 v22, v22, v9\n"
"v_min_f32 v23, v23, v9\n"
"v_min_f32 v24, v24, v8\n"
"v_min_f32 v25, v25, v8\n"
"v_min_f32 v26, v26, v8\n"
"v_min_f32 v27, v27, v8\n"
"v_min_f32 v28, v28, v9\n"
"v_min_f32 v29, v29, v9\n"
"v_min_f32 v30, v30, v9\n"
"v_min_f32 v31, v31, v9\n"
"v_min_f32 v32, v32, v8\n"
"v_min_f32 v33, v33, v8\n"
"v_min_f32 v34, v34, v8\n"
"v_min_f32 v35, v35, v8\n"
"v_min_f32 v36, v36, v9\n"
"v_min_f32 v37, v37, v9\n"
"v_min_f32 v38, v38, v9\n"
"v_min_f32 v39, v39, v9\n"
"v_min_f32 v40, v40, v8\n"
"v_min_f32 v41, v41, v8\n"
"v_min_f32 v42, v42, v8\n"
"v_min_f32 v43, v43, v8\n"
"v_min_f32 v44, v44, v9\n"
"v_min_f32 v45, v45, v9\n"
"v_min_f32 v46, v46, v9\n"
"v_min_f32 v47, v47, v9\n"
"s_branch LA0%=\n"
"LE1%=:\n"
"s_cmp_eq_u32 s64, 0\n"
"s_cbranch_scc1 LF1%=\n"
"v_mfma_f32_16x16x32_bf16 a[128:131], a[136:139], v[80:83], a[128:131]\n"
"ds_read_b128 a[112:115], v7 offset:12288\n"
"s_waitcnt vmcnt(23)\n"
"v_accvgpr_mov_b32 a0, a176\n"
"v_accvgpr_mov_b32 a1, a177\n"
"v_mfma_f32_16x16x32_bf16 a[128:131], a[136:139], v[84:87], a[128:131]\n"
"v_accvgpr_mov_b32 a2, a178\n"
"v_accvgpr_mov_b32 a3, a179\n"
"s_waitcnt vmcnt(22)\n"
"v_accvgpr_mov_b32 a4, a180\n"
"v_mfma_f32_16x16x32_bf16 a[132:135], a[136:139], v[88:91], a[132:135]\n"
"ds_read_b128 a[116:119], v7 offset:13312\n"
"s_add_u32 s59, s59, 32768\n"
"v_accvgpr_mov_b32 a5, a181\n"
"v_mfma_f32_16x16x32_bf16 a[132:135], a[136:139], v[92:95], a[132:135]\n"
"buffer_load_dwordx4 v1, s[40:43], s61 offen offset:3072 lds\n"
"v_accvgpr_mov_b32 a6, a182\n"
"v_accvgpr_mov_b32 a7, a183\n"
"v_mfma_f32_16x16x32_bf16 v[112:115], a[64:67], v[80:83], v[112:115]\n"
"ds_read_b128 a[120:123], v7 offset:14336\n"
"s_add_u32 m0, m0, 0x1000\n"
"s_waitcnt vmcnt(22)\n"
"v_accvgpr_mov_b32 a8, a184\n"
"v_mfma_f32_16x16x32_bf16 v[116:119], a[64:67], v[88:91], v[116:119]\n"
"v_accvgpr_mov_b32 a9, a185\n"
"v_accvgpr_mov_b32 a10, a186\n"
"v_accvgpr_mov_b32 a11, a187\n"
"v_mfma_f32_16x16x32_bf16 v[112:115], a[68:71], v[84:87], v[112:115]\n"
"ds_read_b128 a[124:127], v7 offset:15360\n"
"s_waitcnt vmcnt(21) lgkmcnt(8)\n"
"v_accvgpr_mov_b32 a12, a188\n"
"v_mfma_f32_16x16x32_bf16 v[116:119], a[68:71], v[92:95], v[116:119]\n"
"s_cmp_eq_u32 s59, 163840\n"
"s_cselect_b32 s59, 0, s59\n"
"v_accvgpr_mov_b32 a13, a189\n"
"v_mfma_f32_16x16x32_bf16 v[120:123], a[72:75], v[80:83], v[120:123]\n"
"ds_read_b128 a[64:67], v7 offset:16384\n"
"v_add_u32 v253, s59, v0\n"
"v_accvgpr_read_b32 v12, a128\n"
"v_mfma_f32_16x16x32_bf16 v[124:127], a[72:75], v[88:91], v[124:127]\n"
"v_accvgpr_read_b32 v13, a132\n"
"v_accvgpr_mov_b32 a14, a190\n"
"v_accvgpr_mov_b32 a15, a191\n"
"v_mfma_f32_16x16x32_bf16 v[120:123], a[76:79], v[84:87], v[120:123]\n"
"ds_read_b128 a[68:71], v7 offset:17408\n"
"v_add_f32 v14, s79, v12\n"
"s_waitcnt vmcnt(20)\n"
"v_accvgpr_mov_b32 a16, a192\n"
"v_mfma_f32_16x16x32_bf16 v[124:127], a[76:79], v[92:95], v[124:127]\n"
"buffer_load_dwordx4 v6, s[40:43], s61 offen offset:0 lds\n"
"v_add_f32 v15, v12, v13\n"
"v_accvgpr_mov_b32 a17, a193\n"
"v_mfma_f32_16x16x32_bf16 v[128:131], a[80:83], v[80:83], v[128:131]\n"
"ds_read_b128 a[72:75], v7 offset:18432\n"
"v_min_f32 v14, v14, v13\n"
"v_accvgpr_mov_b32 a18, a194\n"
"v_mfma_f32_16x16x32_bf16 v[132:135], a[80:83], v[88:91], v[132:135]\n"
"v_cmp_nle_f32 s[80:81], v15, v240\n"
"v_accvgpr_mov_b32 a19, a195\n"
"s_waitcnt vmcnt(20)\n"
"v_accvgpr_mov_b32 a20, a196\n"
"v_mfma_f32_16x16x32_bf16 v[128:131], a[84:87], v[84:87], v[128:131]\n"
"ds_read_b128 a[76:79], v7 offset:19456\n"
"s_waitcnt lgkmcnt(8)\n"
"v_cmp_lt_f32 vcc, v14, v241\n"
"v_mfma_f32_16x16x32_bf16 v[132:135], a[84:87], v[92:95], v[132:135]\n"
"s_or_b64 s[80:81], s[80:81], vcc\n"
"v_cvt_pk_bf16_f32 v112, v112, v113\n"
"v_cvt_pk_bf16_f32 v113, v114, v115\n"
"v_mfma_f32_16x16x32_bf16 v[136:139], a[88:91], v[80:83], v[136:139]\n"
"ds_read_b128 a[80:83], v7 offset:20480\n"
"v_mov_b64 v[248:249], 0\n"
"v_mov_b64 v[250:251], v[12:13]\n"
"v_mfma_f32_16x16x32_bf16 v[140:143], a[88:91], v[88:91], v[140:143]\n"
"s_nop 0\n"
"buffer_store_dwordx4 v[248:251], v0, s[52:55], s88 offen sc0 sc1\n"
"v_cvt_pk_bf16_f32 v114, v120, v121\n"
"v_mfma_f32_16x16x32_bf16 v[136:139], a[92:95], v[84:87], v[136:139]\n"
"ds_read_b128 a[84:87], v7 offset:21504\n"
"v_cvt_pk_bf16_f32 v115, v122, v123\n"
"buffer_store_dwordx4 v[112:115], v0, s[52:55], s84 offen offset:0 sc0 sc1\n"
"v_mfma_f32_16x16x32_bf16 v[140:143], a[92:95], v[92:95], v[140:143]\n"
"buffer_load_dwordx4 v6, s[40:43], s61 offen offset:1024 lds\n"
"v_cvt_pk_bf16_f32 v116, v116, v117\n"
"v_cvt_pk_bf16_f32 v117, v118, v119\n"
"v_mfma_f32_16x16x32_bf16 v[144:147], a[96:99], v[80:83], v[144:147]\n"
"ds_read_b128 a[88:91], v7 offset:22528\n"
"v_cvt_pk_bf16_f32 v118, v124, v125\n"
"v_cvt_pk_bf16_f32 v119, v126, v127\n"
"v_mfma_f32_16x16x32_bf16 v[148:151], a[96:99], v[88:91], v[148:151]\n"
"buffer_store_dwordx4 v[116:119], v0, s[52:55], s84 offen offset:1024 sc0 sc1\n"
"v_accvgpr_mov_b32 a21, a197\n"
"v_accvgpr_mov_b32 a22, a198\n"
"v_mfma_f32_16x16x32_bf16 v[144:147], a[100:103], v[84:87], v[144:147]\n"
"ds_read_b128 a[92:95], v7 offset:23552\n"
"s_waitcnt lgkmcnt(8)\n"
"v_cvt_pk_bf16_f32 v128, v128, v129\n"
"v_mfma_f32_16x16x32_bf16 v[148:151], a[100:103], v[92:95], v[148:151]\n"
"v_cvt_pk_bf16_f32 v129, v130, v131\n"
"v_cvt_pk_bf16_f32 v130, v136, v137\n"
"v_cvt_pk_bf16_f32 v131, v138, v139\n"
"v_mfma_f32_16x16x32_bf16 v[152:155], a[104:107], v[80:83], v[152:155]\n"
"ds_read_b128 a[96:99], v7 offset:24576\n"
"buffer_store_dwordx4 v[128:131], v0, s[52:55], s84 offen offset:2048 sc0 sc1\n"
"v_cvt_pk_bf16_f32 v132, v132, v133\n"
"v_mfma_f32_16x16x32_bf16 v[156:159], a[104:107], v[88:91], v[156:159]\n"
"v_cvt_pk_bf16_f32 v133, v134, v135\n"
"v_cvt_pk_bf16_f32 v134, v140, v141\n"
"v_cvt_pk_bf16_f32 v135, v142, v143\n"
"v_mfma_f32_16x16x32_bf16 v[152:155], a[108:111], v[84:87], v[152:155]\n"
"ds_read_b128 a[100:103], v7 offset:25600\n"
"buffer_store_dwordx4 v[132:135], v0, s[52:55], s84 offen offset:3072 sc0 sc1\n"
"v_accvgpr_mov_b32 a23, a199\n"
"v_mfma_f32_16x16x32_bf16 v[156:159], a[108:111], v[92:95], v[156:159]\n"
"buffer_load_dwordx4 v6, s[40:43], s61 offen offset:2048 lds\n"
"s_waitcnt vmcnt(26)\n"
"v_accvgpr_mov_b32 a24, a200\n"
"v_accvgpr_mov_b32 a25, a201\n"
"v_mfma_f32_16x16x32_bf16 v[160:163], a[112:115], v[80:83], v[160:163]\n"
"ds_read_b128 a[104:107], v7 offset:26624\n"
"v_accvgpr_mov_b32 a26, a202\n"
"v_accvgpr_mov_b32 a27, a203\n"
"v_mfma_f32_16x16x32_bf16 v[164:167], a[112:115], v[88:91], v[164:167]\n"
"s_waitcnt vmcnt(25)\n"
"v_accvgpr_mov_b32 a28, a204\n"
"v_accvgpr_mov_b32 a29, a205\n"
"v_accvgpr_mov_b32 a30, a206\n"
"v_mfma_f32_16x16x32_bf16 v[160:163], a[116:119], v[84:87], v[160:163]\n"
"ds_read_b128 a[108:111], v7 offset:27648\n"
"s_waitcnt lgkmcnt(8)\n"
"v_cvt_pk_bf16_f32 v144, v144, v145\n"
"v_mfma_f32_16x16x32_bf16 v[164:167], a[116:119], v[92:95], v[164:167]\n"
"v_cvt_pk_bf16_f32 v145, v146, v147\n"
"v_cvt_pk_bf16_f32 v146, v152, v153\n"
"v_cvt_pk_bf16_f32 v147, v154, v155\n"
"v_mfma_f32_16x16x32_bf16 v[168:171], a[120:123], v[80:83], v[168:171]\n"
"ds_read_b128 a[112:115], v7 offset:28672\n"
"buffer_store_dwordx4 v[144:147], v0, s[52:55], s85 offen offset:0 sc0 sc1\n"
"v_cvt_pk_bf16_f32 v148, v148, v149\n"
"v_mfma_f32_16x16x32_bf16 v[172:175], a[120:123], v[88:91], v[172:175]\n"
"v_cvt_pk_bf16_f32 v149, v150, v151\n"
"v_cvt_pk_bf16_f32 v150, v156, v157\n"
"v_cvt_pk_bf16_f32 v151, v158, v159\n"
"v_mfma_f32_16x16x32_bf16 v[168:171], a[124:127], v[84:87], v[168:171]\n"
"ds_read_b128 a[116:119], v7 offset:29696\n"
"buffer_store_dwordx4 v[148:151], v0, s[52:55], s85 offen offset:1024 sc0 sc1\n"
"v_accvgpr_mov_b32 a31, a207\n"
"v_mfma_f32_16x16x32_bf16 v[172:175], a[124:127], v[92:95], v[172:175]\n"
"buffer_load_dwordx4 v6, s[40:43], s61 offen offset:3072 lds\n"
"s_add_u32 m0, s60, s62\n"
"s_waitcnt vmcnt(27)\n"
"v_accvgpr_mov_b32 a32, a208\n"
"v_mfma_f32_16x16x32_bf16 v[176:179], a[64:67], v[80:83], v[176:179]\n"
"ds_read_b128 a[120:123], v7 offset:30720\n"
"v_readlane_b32 s61, v2, s58\n"
"v_accvgpr_mov_b32 a33, a209\n"
"v_mfma_f32_16x16x32_bf16 v[180:183], a[64:67], v[88:91], v[180:183]\n"
"v_accvgpr_mov_b32 a34, a210\n"
"v_accvgpr_mov_b32 a35, a211\n"
"s_waitcnt vmcnt(26)\n"
"v_accvgpr_mov_b32 a36, a212\n"
"v_mfma_f32_16x16x32_bf16 v[176:179], a[68:71], v[84:87], v[176:179]\n"
"ds_read_b128 a[124:127], v7 offset:31744\n"
"s_waitcnt lgkmcnt(8)\n"
"v_cvt_pk_bf16_f32 v160, v160, v161\n"
"v_mfma_f32_16x16x32_bf16 v[180:183], a[68:71], v[92:95], v[180:183]\n"
"s_waitcnt vmcnt(24)\n"
"s_barrier\n"
"v_cvt_pk_bf16_f32 v161, v162, v163\n"
"v_mfma_f32_16x16x32_bf16 v[184:187], a[72:75], v[80:83], v[184:187]\n"
"ds_read_b128 a[64:67], v253 offset:0\n"
"s_cmp_lt_u32 s58, 34\n"
"v_cvt_pk_bf16_f32 v162, v168, v169\n"
"v_mfma_f32_16x16x32_bf16 v[188:191], a[72:75], v[88:91], v[188:191]\n"
"v_cvt_pk_bf16_f32 v163, v170, v171\n"
"buffer_store_dwordx4 v[160:163], v0, s[52:55], s85 offen offset:2048 sc0 sc1\n"
"v_cvt_pk_bf16_f32 v164, v164, v165\n"
"v_mfma_f32_16x16x32_bf16 v[184:187], a[76:79], v[84:87], v[184:187]\n"
"ds_read_b128 a[68:71], v253 offset:1024\n"
"s_cselect_b32 s42, s71, 0\n"
"v_cvt_pk_bf16_f32 v165, v166, v167\n"
"v_mfma_f32_16x16x32_bf16 v[188:191], a[76:79], v[92:95], v[188:191]\n"
"buffer_load_dwordx4 v1, s[40:43], s61 offen offset:0 lds\n"
"v_cvt_pk_bf16_f32 v166, v172, v173\n"
"v_cvt_pk_bf16_f32 v167, v174, v175\n"
"v_mfma_f32_16x16x32_bf16 v[192:195], a[80:83], v[80:83], v[192:195]\n"
"ds_read_b128 a[72:75], v253 offset:2048\n"
"s_add_u32 s60, s60, 32768\n"
"buffer_store_dwordx4 v[164:167], v0, s[52:55], s85 offen offset:3072 sc0 sc1\n"
"v_mfma_f32_16x16x32_bf16 v[196:199], a[80:83], v[88:91], v[196:199]\n"
"v_accvgpr_mov_b32 a37, a213\n"
"v_accvgpr_mov_b32 a38, a214\n"
"v_accvgpr_mov_b32 a39, a215\n"
"v_mfma_f32_16x16x32_bf16 v[192:195], a[84:87], v[84:87], v[192:195]\n"
"ds_read_b128 a[76:79], v253 offset:3072\n"
"s_waitcnt lgkmcnt(8)\n"
"v_cvt_pk_bf16_f32 v176, v176, v177\n"
"v_mfma_f32_16x16x32_bf16 v[196:199], a[84:87], v[92:95], v[196:199]\n"
"s_cmp_eq_u32 s60, 163840\n"
"s_cselect_b32 s60, 0, s60\n"
"v_cvt_pk_bf16_f32 v177, v178, v179\n"
"v_mfma_f32_16x16x32_bf16 v[200:203], a[88:91], v[80:83], v[200:203]\n"
"ds_read_b128 a[80:83], v253 offset:4096\n"
"s_add_u32 s58, s58, 1\n"
"v_cvt_pk_bf16_f32 v178, v184, v185\n"
"v_mfma_f32_16x16x32_bf16 v[204:207], a[88:91], v[88:91], v[204:207]\n"
"v_cvt_pk_bf16_f32 v179, v186, v187\n"
"buffer_store_dwordx4 v[176:179], v0, s[52:55], s86 offen offset:0 sc0 sc1\n"
"v_cvt_pk_bf16_f32 v180, v180, v181\n"
"v_mfma_f32_16x16x32_bf16 v[200:203], a[92:95], v[84:87], v[200:203]\n"
"ds_read_b128 a[84:87], v253 offset:5120\n"
"v_mov_b32 v7, v253\n"
"v_cvt_pk_bf16_f32 v181, v182, v183\n"
"v_mfma_f32_16x16x32_bf16 v[204:207], a[92:95], v[92:95], v[204:207]\n"
"buffer_load_dwordx4 v1, s[40:43], s61 offen offset:1024 lds\n"
"v_cvt_pk_bf16_f32 v182, v188, v189\n"
"v_cvt_pk_bf16_f32 v183, v190, v191\n"
"v_mfma_f32_16x16x32_bf16 v[208:211], a[96:99], v[80:83], v[208:211]\n"
"ds_read_b128 a[88:91], v253 offset:6144\n"
"buffer_store_dwordx4 v[180:183], v0, s[52:55], s86 offen offset:1024 sc0 sc1\n"
"s_waitcnt vmcnt(31)\n"
"v_accvgpr_mov_b32 a40, a216\n"
"v_mfma_f32_16x16x32_bf16 v[212:215], a[96:99], v[88:91], v[212:215]\n"
"v_accvgpr_mov_b32 a41, a217\n"
"v_accvgpr_mov_b32 a42, a218\n"
"v_accvgpr_mov_b32 a43, a219\n"
"v_mfma_f32_16x16x32_bf16 v[208:211], a[100:103], v[84:87], v[208:211]\n"
"ds_read_b128 a[92:95], v253 offset:7168\n"
"s_waitcnt lgkmcnt(8)\n"
"v_cvt_pk_bf16_f32 v192, v192, v193\n"
"v_mfma_f32_16x16x32_bf16 v[212:215], a[100:103], v[92:95], v[212:215]\n"
"v_cvt_pk_bf16_f32 v193, v194, v195\n"
"v_cvt_pk_bf16_f32 v194, v200, v201\n"
"v_cvt_pk_bf16_f32 v195, v202, v203\n"
"v_mfma_f32_16x16x32_bf16 v[216:219], a[104:107], v[80:83], v[216:219]\n"
"ds_read_b128 a[96:99], v253 offset:8192\n"
"buffer_store_dwordx4 v[192:195], v0, s[52:55], s86 offen offset:2048 sc0 sc1\n"
"v_cvt_pk_bf16_f32 v196, v196, v197\n"
"v_mfma_f32_16x16x32_bf16 v[220:223], a[104:107], v[88:91], v[220:223]\n"
"v_cvt_pk_bf16_f32 v197, v198, v199\n"
"v_cvt_pk_bf16_f32 v198, v204, v205\n"
"v_cvt_pk_bf16_f32 v199, v206, v207\n"
"v_mfma_f32_16x16x32_bf16 v[216:219], a[108:111], v[84:87], v[216:219]\n"
"ds_read_b128 a[100:103], v253 offset:9216\n"
"buffer_store_dwordx4 v[196:199], v0, s[52:55], s86 offen offset:3072 sc0 sc1\n"
"s_waitcnt vmcnt(32)\n"
"v_accvgpr_mov_b32 a44, a220\n"
"v_mfma_f32_16x16x32_bf16 v[220:223], a[108:111], v[92:95], v[220:223]\n"
"buffer_load_dwordx4 v1, s[40:43], s61 offen offset:2048 lds\n"
"v_accvgpr_mov_b32 a45, a221\n"
"v_accvgpr_mov_b32 a46, a222\n"
"v_mfma_f32_16x16x32_bf16 v[224:227], a[112:115], v[80:83], v[224:227]\n"
"ds_read_b128 a[104:107], v253 offset:10240\n"
"v_accvgpr_mov_b32 a47, a223\n"
"s_waitcnt vmcnt(32)\n"
"v_accvgpr_mov_b32 a48, a224\n"
"v_mfma_f32_16x16x32_bf16 v[228:231], a[112:115], v[88:91], v[228:231]\n"
"v_accvgpr_mov_b32 a49, a225\n"
"v_accvgpr_mov_b32 a50, a226\n"
"v_accvgpr_mov_b32 a51, a227\n"
"v_mfma_f32_16x16x32_bf16 v[224:227], a[116:119], v[84:87], v[224:227]\n"
"ds_read_b128 a[108:111], v253 offset:11264\n"
"s_waitcnt lgkmcnt(8)\n"
"v_cvt_pk_bf16_f32 v208, v208, v209\n"
"v_mfma_f32_16x16x32_bf16 v[228:231], a[116:119], v[92:95], v[228:231]\n"
"v_cvt_pk_bf16_f32 v209, v210, v211\n"
"v_cvt_pk_bf16_f32 v210, v216, v217\n"
"v_cvt_pk_bf16_f32 v211, v218, v219\n"
"v_mfma_f32_16x16x32_bf16 v[232:235], a[120:123], v[80:83], v[232:235]\n"
"buffer_store_dwordx4 v[208:211], v0, s[52:55], s87 offen offset:0 sc0 sc1\n"
"v_cvt_pk_bf16_f32 v212, v212, v213\n"
"v_cvt_pk_bf16_f32 v213, v214, v215\n"
"v_mfma_f32_16x16x32_bf16 v[236:239], a[120:123], v[88:91], v[236:239]\n"
"v_cvt_pk_bf16_f32 v214, v220, v221\n"
"v_cvt_pk_bf16_f32 v215, v222, v223\n"
"buffer_store_dwordx4 v[212:215], v0, s[52:55], s87 offen offset:1024 sc0 sc1\n"
"v_mfma_f32_16x16x32_bf16 v[232:235], a[124:127], v[84:87], v[232:235]\n"
"s_waitcnt vmcnt(33)\n"
"v_accvgpr_mov_b32 a52, a228\n"
"v_accvgpr_mov_b32 a53, a229\n"
"v_accvgpr_mov_b32 a54, a230\n"
"v_mfma_f32_16x16x32_bf16 v[236:239], a[124:127], v[92:95], v[236:239]\n"
"v_accvgpr_mov_b32 a55, a231\n"
"s_waitcnt vmcnt(32)\n"
"v_accvgpr_mov_b32 a56, a232\n"
"v_accvgpr_mov_b32 a57, a233\n"
"s_nop 7\n"
"v_cvt_pk_bf16_f32 v224, v224, v225\n"
"v_cvt_pk_bf16_f32 v225, v226, v227\n"
"v_cvt_pk_bf16_f32 v226, v232, v233\n"
"v_cvt_pk_bf16_f32 v227, v234, v235\n"
"buffer_store_dwordx4 v[224:227], v0, s[52:55], s87 offen offset:2048 sc0 sc1\n"
"v_cvt_pk_bf16_f32 v228, v228, v229\n"
"v_cvt_pk_bf16_f32 v229, v230, v231\n"
"v_cvt_pk_bf16_f32 v230, v236, v237\n"
"v_cvt_pk_bf16_f32 v231, v238, v239\n"
"buffer_store_dwordx4 v[228:231], v0, s[52:55], s87 offen offset:3072 sc0 sc1\n"
"v_accvgpr_mov_b32 a58, a234\n"
"v_accvgpr_mov_b32 a59, a235\n"
"s_waitcnt vmcnt(33)\n"
"v_accvgpr_mov_b32 a60, a236\n"
"v_accvgpr_mov_b32 a61, a237\n"
"v_accvgpr_mov_b32 a62, a238\n"
"v_accvgpr_mov_b32 a63, a239\n"
"v_mov_b64 v[16:17], 0\n"
"v_mov_b64 v[18:19], 0\n"
"v_mov_b64 v[20:21], 0\n"
"v_mov_b64 v[22:23], 0\n"
"v_mov_b64 v[24:25], 0\n"
"v_mov_b64 v[26:27], 0\n"
"v_mov_b64 v[28:29], 0\n"
"v_mov_b64 v[30:31], 0\n"
"v_mov_b64 v[32:33], 0\n"
"v_mov_b64 v[34:35], 0\n"
"v_mov_b64 v[36:37], 0\n"
"v_mov_b64 v[38:39], 0\n"
"v_mov_b64 v[40:41], 0\n"
"v_mov_b64 v[42:43], 0\n"
"v_mov_b64 v[44:45], 0\n"
"v_mov_b64 v[46:47], 0\n"
"s_cmp_lg_u64 s[80:81], 0\n"
"s_cbranch_scc1 LSLOW%=\n"
"LSN%=:\n"
"s_mov_b32 s89, 0x7fffffff\n"
"s_mov_b32 s56, s64\n"
"s_mov_b32 s64, 0\n"
"s_mov_b32 s63, %9\n"
"s_mov_b32 s68, 1\n"
"v_mov_b64 v[112:113], 0\n"
"v_mov_b64 v[114:115], 0\n"
"v_mov_b64 v[116:117], 0\n"
"v_mov_b64 v[118:119], 0\n"
"v_mov_b64 v[120:121], 0\n"
"v_mov_b64 v[122:123], 0\n"
"v_mov_b64 v[124:125], 0\n"
"v_mov_b64 v[126:127], 0\n"
"v_mov_b64 v[128:129], 0\n"
"v_mov_b64 v[130:131], 0\n"
"v_mov_b64 v[132:133], 0\n"
"v_mov_b64 v[134:135], 0\n"
"v_mov_b64 v[136:137], 0\n"
"v_mov_b64 v[138:139], 0\n"
"v_mov_b64 v[140:141], 0\n"
"v_mov_b64 v[142:143], 0\n"
"v_mov_b64 v[144:145], 0\n"
"v_mov_b64 v[146:147], 0\n"
"v_mov_b64 v[148:149], 0\n"
"v_mov_b64 v[150:151], 0\n"
"v_mov_b64 v[152:153], 0\n"
"v_mov_b64 v[154:155], 0\n"
"v_mov_b64 v[156:157], 0\n"
"v_mov_b64 v[158:159], 0\n"
"v_mov_b64 v[160:161], 0\n"
"v_mov_b64 v[162:163], 0\n"
"v_mov_b64 v[164:165], 0\n"
"v_mov_b64 v[166:167], 0\n"
"v_mov_b64 v[168:169], 0\n"
"v_mov_b64 v[170:171], 0\n"
"v_mov_b64 v[172:173], 0\n"
"v_mov_b64 v[174:175], 0\n"
"v_mov_b64 v[176:177], 0\n"
"v_mov_b64 v[178:179], 0\n"
"v_mov_b64 v[180:181], 0\n"
"v_mov_b64 v[182:183], 0\n"
"v_mov_b64 v[184:185], 0\n"
"v_mov_b64 v[186:187], 0\n"
"v_mov_b64 v[188:189], 0\n"
"v_mov_b64 v[190:191], 0\n"
"v_mov_b64 v[192:193], 0\n"
"v_mov_b64 v[194:195], 0\n"
"v_mov_b64 v[196:197], 0\n"
"v_mov_b64 v[198:199], 0\n"
"v_mov_b64 v[200:201], 0\n"
"v_mov_b64 v[202:203], 0\n"
"v_mov_b64 v[204:205], 0\n"
"v_mov_b64 v[206:207], 0\n"
"v_mov_b64 v[8:9], 0\n"
"v_accvgpr_write_b32 a128, 0\n"
"v_accvgpr_write_b32 a129, 0\n"
"v_accvgpr_write_b32 a130, 0\n"
"v_accvgpr_write_b32 a131, 0\n"
"v_accvgpr_write_b32 a132, 0\n"
"v_accvgpr_write_b32 a133, 0\n"
"v_accvgpr_write_b32 a134, 0\n"
"v_accvgpr_write_b32 a135, 0\n"
"s_lshl_b32 s78, s56, 6\n"
"s_add_u32 s78, s78, s63\n"
"s_min_i32 s77, s78, 0\n"
"s_cmp_gt_i32 s63, 0\n"
"s_cselect_b32 s79, 1.0, 0\n"
"s_cmp_lt_i32 s63, 0\n"
"s_cbranch_scc1 LM1%=\n"
"v_cvt_f32_i32 v12, s63\n"
"v_add_f32 v12, v12, v3\n"
"v_mul_f32 v12, 0xf1c9f2ca, v12\n"
"v_add_f32 v16, 0x7149f2ca, v12\n"
"v_add_f32 v17, 0xf149f2ca, v12\n"
"v_add_f32 v18, 0xf2177617, v12\n"
"v_add_f32 v19, 0xf27c6f7c, v12\n"
"v_add_f32 v20, 0x74cb86af, v12\n"
"v_add_f32 v21, 0x74c85ee4, v12\n"
"v_add_f32 v22, 0x74c53719, v12\n"
"v_add_f32 v23, 0x74c20f4e, v12\n"
"v_add_f32 v24, 0xf3c3a333, v12\n"
"v_add_f32 v25, 0xf3d04260, v12\n"
"v_add_f32 v26, 0xf3dce18d, v12\n"
"v_add_f32 v27, 0xf3e980b9, v12\n"
"v_add_f32 v28, 0x749909fd, v12\n"
"v_add_f32 v29, 0x7495e232, v12\n"
"v_add_f32 v30, 0x7492ba67, v12\n"
"v_add_f32 v31, 0x748f929b, v12\n"
"v_add_f32 v32, 0xf446caff, v12\n"
"v_add_f32 v33, 0xf44d1a95, v12\n"
"v_add_f32 v34, 0xf4536a2b, v12\n"
"v_add_f32 v35, 0xf459b9c2, v12\n"
"v_add_f32 v36, 0x744d1a95, v12\n"
"v_add_f32 v37, 0x7446caff, v12\n"
"v_add_f32 v38, 0x74407b68, v12\n"
"v_add_f32 v39, 0x743a2bd2, v12\n"
"v_add_f32 v40, 0xf495e232, v12\n"
"v_add_f32 v41, 0xf49909fd, v12\n"
"v_add_f32 v42, 0xf49c31c8, v12\n"
"v_add_f32 v43, 0xf49f5993, v12\n"
"v_add_f32 v44, 0x73d04260, v12\n"
"v_add_f32 v45, 0x73c3a333, v12\n"
"v_add_f32 v46, 0x73b70407, v12\n"
"v_add_f32 v47, 0x73aa64da, v12\n"
"v_min_f32 v16, v16, v8\n"
"v_min_f32 v17, v17, v8\n"
"v_min_f32 v18, v18, v8\n"
"v_min_f32 v19, v19, v8\n"
"v_min_f32 v20, v20, v9\n"
"v_min_f32 v21, v21, v9\n"
"v_min_f32 v22, v22, v9\n"
"v_min_f32 v23, v23, v9\n"
"v_min_f32 v24, v24, v8\n"
"v_min_f32 v25, v25, v8\n"
"v_min_f32 v26, v26, v8\n"
"v_min_f32 v27, v27, v8\n"
"v_min_f32 v28, v28, v9\n"
"v_min_f32 v29, v29, v9\n"
"v_min_f32 v30, v30, v9\n"
"v_min_f32 v31, v31, v9\n"
"v_min_f32 v32, v32, v8\n"
"v_min_f32 v33, v33, v8\n"
"v_min_f32 v34, v34, v8\n"
"v_min_f32 v35, v35, v8\n"
"v_min_f32 v36, v36, v9\n"
"v_min_f32 v37, v37, v9\n"
"v_min_f32 v38, v38, v9\n"
"v_min_f32 v39, v39, v9\n"
"v_min_f32 v40, v40, v8\n"
"v_min_f32 v41, v41, v8\n"
"v_min_f32 v42, v42, v8\n"
"v_min_f32 v43, v43, v8\n"
"v_min_f32 v44, v44, v9\n"
"v_min_f32 v45, v45, v9\n"
"v_min_f32 v46, v46, v9\n"
"v_min_f32 v47, v47, v9\n"
"s_nop 1\n"
"LM1%=:\n"
"v_mfma_f32_16x16x32_bf16 v[48:51], a[64:67], a[0:3], v[16:19]\n"
"ds_read_b128 a[112:115], v7 offset:12288\n"
"buffer_load_dwordx4 a[140:143], v0, s[52:55], %12 offen offset:0\n"
"v_mfma_f32_16x16x32_bf16 v[52:55], a[64:67], a[32:35], v[20:23]\n"
"s_add_u32 s63, s63, 64\n"
"v_mov_b64 v[208:209], 0\n"
"v_mfma_f32_16x16x32_bf16 v[48:51], a[68:71], a[4:7], v[48:51]\n"
"ds_read_b128 a[116:119], v7 offset:13312\n"
"s_add_u32 s59, s59, 32768\n"
"v_mfma_f32_16x16x32_bf16 v[52:55], a[68:71], a[36:39], v[52:55]\n"
"buffer_load_dwordx4 v1, s[40:43], s61 offen offset:3072 lds\n"
"v_mov_b64 v[210:211], 0\n"
"v_mfma_f32_16x16x32_bf16 v[48:51], a[72:75], a[8:11], v[48:51]\n"
"ds_read_b128 a[120:123], v7 offset:14336\n"
"s_add_u32 m0, m0, 0x1000\n"
"v_mfma_f32_16x16x32_bf16 v[52:55], a[72:75], a[40:43], v[52:55]\n"
"v_mov_b64 v[212:213], 0\n"
"v_mov_b64 v[214:215], 0\n"
"v_mfma_f32_16x16x32_bf16 v[48:51], a[76:79], a[12:15], v[48:51]\n"
"ds_read_b128 a[124:127], v7 offset:15360\n"
"s_waitcnt lgkmcnt(8)\n"
"v_mfma_f32_16x16x32_bf16 v[52:55], a[76:79], a[44:47], v[52:55]\n"
"s_cmp_eq_u32 s59, 163840\n"
"s_cselect_b32 s59, 0, s59\n"
"v_mfma_f32_16x16x32_bf16 v[48:51], a[80:83], a[16:19], v[48:51]\n"
"ds_read_b128 a[64:67], v7 offset:16384\n"
"v_add_u32 v253, s59, v0\n"
"v_mfma_f32_16x16x32_bf16 v[52:55], a[80:83], a[48:51], v[52:55]\n"
"v_mov_b64 v[216:217], 0\n"
"v_mov_b64 v[218:219], 0\n"
"v_mfma_f32_16x16x32_bf16 v[48:51], a[84:87], a[20:23], v[48:51]\n"
"ds_read_b128 a[68:71], v7 offset:17408\n"
"buffer_load_dwordx4 a[144:147], v0, s[52:55], %12 offen offset:1024\n"
"v_mfma_f32_16x16x32_bf16 v[52:55], a[84:87], a[52:55], v[52:55]\n"
"buffer_load_dwordx4 v6, s[40:43], s61 offen offset:0 lds\n"
"v_mov_b64 v[220:221], 0\n"
"v_mfma_f32_16x16x32_bf16 v[48:51], a[88:91], a[24:27], v[48:51]\n"
"ds_read_b128 a[72:75], v7 offset:18432\n"
"v_mov_b64 v[222:223], 0\n"
"v_mfma_f32_16x16x32_bf16 v[52:55], a[88:91], a[56:59], v[52:55]\n"
"v_mov_b64 v[224:225], 0\n"
"v_mov_b64 v[226:227], 0\n"
"v_mfma_f32_16x16x32_bf16 v[48:51], a[92:95], a[28:31], v[48:51]\n"
"ds_read_b128 a[76:79], v7 offset:19456\n"
"s_waitcnt lgkmcnt(8)\n"
"v_mfma_f32_16x16x32_bf16 v[52:55], a[92:95], a[60:63], v[52:55]\n"
"v_mov_b64 v[228:229], 0\n"
"v_mov_b64 v[230:231], 0\n"
"v_mfma_f32_16x16x32_bf16 v[56:59], a[96:99], a[0:3], v[24:27]\n"
"ds_read_b128 a[80:83], v7 offset:20480\n"
"buffer_load_dwordx4 a[148:151], v0, s[52:55], %12 offen offset:2048\n"
"v_mfma_f32_16x16x32_bf16 v[60:63], a[96:99], a[32:35], v[28:31]\n"
"v_mov_b64 v[232:233], 0\n"
"v_mov_b64 v[234:235], 0\n"
"v_mfma_f32_16x16x32_bf16 v[56:59], a[100:103], a[4:7], v[56:59]\n"
"ds_read_b128 a[84:87], v7 offset:21504\n"
"v_mov_b64 v[236:237], 0\n"
"v_mfma_f32_16x16x32_bf16 v[60:63], a[100:103], a[36:39], v[60:63]\n"
"buffer_load_dwordx4 v6, s[40:43], s61 offen offset:1024 lds\n"
"v_mov_b64 v[238:239], 0\n"
"v_mfma_f32_16x16x32_bf16 v[56:59], a[104:107], a[8:11], v[56:59]\n"
"ds_read_b128 a[88:91], v7 offset:22528\n"
"v_mfma_f32_16x16x32_bf16 v[60:63], a[104:107], a[40:43], v[60:63]\n"
"v_exp_f32 v48, v48\n"
"v_mfma_f32_16x16x32_bf16 v[56:59], a[108:111], a[12:15], v[56:59]\n"
"ds_read_b128 a[92:95], v7 offset:23552\n"
"s_waitcnt lgkmcnt(8)\n"
"v_mfma_f32_16x16x32_bf16 v[60:63], a[108:111], a[44:47], v[60:63]\n"
"v_exp_f32 v49, v49\n"
"v_mfma_f32_16x16x32_bf16 v[56:59], a[112:115], a[16:19], v[56:59]\n"
"ds_read_b128 a[96:99], v7 offset:24576\n"
"buffer_load_dwordx4 a[152:155], v0, s[52:55], %12 offen offset:3072\n"
"v_mfma_f32_16x16x32_bf16 v[60:63], a[112:115], a[48:51], v[60:63]\n"
"v_exp_f32 v50, v50\n"
"v_mfma_f32_16x16x32_bf16 v[56:59], a[116:119], a[20:23], v[56:59]\n"
"ds_read_b128 a[100:103], v7 offset:25600\n"
"v_mfma_f32_16x16x32_bf16 v[60:63], a[116:119], a[52:55], v[60:63]\n"
"buffer_load_dwordx4 v6, s[40:43], s61 offen offset:2048 lds\n"
"v_mfma_f32_16x16x32_bf16 v[56:59], a[120:123], a[24:27], v[56:59]\n"
"ds_read_b128 a[104:107], v7 offset:26624\n"
"v_mfma_f32_16x16x32_bf16 v[60:63], a[120:123], a[56:59], v[60:63]\n"
"v_exp_f32 v51, v51\n"
"v_mfma_f32_16x16x32_bf16 v[56:59], a[124:127], a[28:31], v[56:59]\n"
"ds_read_b128 a[108:111], v7 offset:27648\n"
"s_waitcnt lgkmcnt(8)\n"
"v_mfma_f32_16x16x32_bf16 v[60:63], a[124:127], a[60:63], v[60:63]\n"
"v_exp_f32 v52, v52\n"
"v_mfma_f32_16x16x32_bf16 v[64:67], a[64:67], a[0:3], v[32:35]\n"
"ds_read_b128 a[112:115], v7 offset:28672\n"
"buffer_load_dwordx4 a[156:159], v0, s[52:55], s82 offen offset:0\n"
"v_mfma_f32_16x16x32_bf16 v[68:71], a[64:67], a[32:35], v[36:39]\n"
"v_exp_f32 v53, v53\n"
"v_mfma_f32_16x16x32_bf16 v[64:67], a[68:71], a[4:7], v[64:67]\n"
"ds_read_b128 a[116:119], v7 offset:29696\n"
"v_cvt_pk_bf16_f32 v80, v48, v49\n"
"v_mfma_f32_16x16x32_bf16 v[68:71], a[68:71], a[36:39], v[68:71]\n"
"buffer_load_dwordx4 v6, s[40:43], s61 offen offset:3072 lds\n"
"s_add_u32 m0, s60, s62\n"
"v_mfma_f32_16x16x32_bf16 v[64:67], a[72:75], a[8:11], v[64:67]\n"
"ds_read_b128 a[120:123], v7 offset:30720\n"
"v_readlane_b32 s61, v2, s58\n"
"v_mfma_f32_16x16x32_bf16 v[68:71], a[72:75], a[40:43], v[68:71]\n"
"v_exp_f32 v54, v54\n"
"v_mfma_f32_16x16x32_bf16 v[64:67], a[76:79], a[12:15], v[64:67]\n"
"ds_read_b128 a[124:127], v7 offset:31744\n"
"s_waitcnt lgkmcnt(8)\n"
"v_mfma_f32_16x16x32_bf16 v[68:71], a[76:79], a[44:47], v[68:71]\n"
"s_waitcnt vmcnt(38)\n"
"s_barrier\n"
"v_mfma_f32_16x16x32_bf16 v[64:67], a[80:83], a[16:19], v[64:67]\n"
"ds_read_b128 a[64:67], v253 offset:0\n"
"s_cmp_lt_u32 s58, 34\n"
"v_mfma_f32_16x16x32_bf16 v[68:71], a[80:83], a[48:51], v[68:71]\n"
"v_exp_f32 v55, v55\n"
"v_mfma_f32_16x16x32_bf16 v[64:67], a[84:87], a[20:23], v[64:67]\n"
"ds_read_b128 a[68:71], v253 offset:1024\n"
"s_cselect_b32 s42, s71, 0\n"
"v_mfma_f32_16x16x32_bf16 v[68:71], a[84:87], a[52:55], v[68:71]\n"
"buffer_load_dwordx4 v1, s[40:43], s61 offen offset:0 lds\n"
"v_cvt_pk_bf16_f32 v81, v50, v51\n"
"v_mfma_f32_16x16x32_bf16 v[64:67], a[88:91], a[24:27], v[64:67]\n"
"ds_read_b128 a[72:75], v253 offset:2048\n"
"s_add_u32 s60, s60, 32768\n"
"v_mfma_f32_16x16x32_bf16 v[68:71], a[88:91], a[56:59], v[68:71]\n"
"v_exp_f32 v56, v56\n"
"v_mfma_f32_16x16x32_bf16 v[64:67], a[92:95], a[28:31], v[64:67]\n"
"ds_read_b128 a[76:79], v253 offset:3072\n"
"s_waitcnt lgkmcnt(8)\n"
"buffer_load_dwordx4 a[160:163], v0, s[52:55], s82 offen offset:1024\n"
"v_mfma_f32_16x16x32_bf16 v[68:71], a[92:95], a[60:63], v[68:71]\n"
"s_cmp_eq_u32 s60, 163840\n"
"s_cselect_b32 s60, 0, s60\n"
"v_mfma_f32_16x16x32_bf16 v[72:75], a[96:99], a[0:3], v[40:43]\n"
"ds_read_b128 a[80:83], v253 offset:4096\n"
"s_add_u32 s58, s58, 1\n"
"v_mfma_f32_16x16x32_bf16 v[76:79], a[96:99], a[32:35], v[44:47]\n"
"v_exp_f32 v57, v57\n"
"v_mfma_f32_16x16x32_bf16 v[72:75], a[100:103], a[4:7], v[72:75]\n"
"ds_read_b128 a[84:87], v253 offset:5120\n"
"v_mov_b32 v7, v253\n"
"v_mfma_f32_16x16x32_bf16 v[76:79], a[100:103], a[36:39], v[76:79]\n"
"buffer_load_dwordx4 v1, s[40:43], s61 offen offset:1024 lds\n"
"v_cvt_pk_bf16_f32 v88, v52, v53\n"
"v_mfma_f32_16x16x32_bf16 v[72:75], a[104:107], a[8:11], v[72:75]\n"
"ds_read_b128 a[88:91], v253 offset:6144\n"
"buffer_load_dwordx4 a[164:167], v0, s[52:55], s82 offen offset:2048\n"
"v_mfma_f32_16x16x32_bf16 v[76:79], a[104:107], a[40:43], v[76:79]\n"
"v_exp_f32 v58, v58\n"
"v_mfma_f32_16x16x32_bf16 v[72:75], a[108:111], a[12:15], v[72:75]\n"
"ds_read_b128 a[92:95], v253 offset:7168\n"
"s_waitcnt lgkmcnt(8)\n"
"v_mfma_f32_16x16x32_bf16 v[76:79], a[108:111], a[44:47], v[76:79]\n"
"v_exp_f32 v59, v59\n"
"v_mfma_f32_16x16x32_bf16 v[72:75], a[112:115], a[16:19], v[72:75]\n"
"ds_read_b128 a[96:99], v253 offset:8192\n"
"buffer_load_dwordx4 a[168:171], v0, s[52:55], s82 offen offset:3072\n"
"v_mfma_f32_16x16x32_bf16 v[76:79], a[112:115], a[48:51], v[76:79]\n"
"v_exp_f32 v60, v60\n"
"v_mfma_f32_16x16x32_bf16 v[72:75], a[116:119], a[20:23], v[72:75]\n"
"ds_read_b128 a[100:103], v253 offset:9216\n"
"v_cvt_pk_bf16_f32 v89, v54, v55\n"
"v_mfma_f32_16x16x32_bf16 v[76:79], a[116:119], a[52:55], v[76:79]\n"
"buffer_load_dwordx4 v1, s[40:43], s61 offen offset:2048 lds\n"
"v_cvt_pk_bf16_f32 v82, v56, v57\n"
"v_mfma_f32_16x16x32_bf16 v[72:75], a[120:123], a[24:27], v[72:75]\n"
"ds_read_b128 a[104:107], v253 offset:10240\n"
"v_cvt_pk_bf16_f32 v83, v58, v59\n"
"v_mfma_f32_16x16x32_bf16 v[76:79], a[120:123], a[56:59], v[76:79]\n"
"v_exp_f32 v61, v61\n"
"v_mfma_f32_16x16x32_bf16 v[72:75], a[124:127], a[28:31], v[72:75]\n"
"ds_read_b128 a[108:111], v253 offset:11264\n"
"s_waitcnt lgkmcnt(8)\n"
"v_mfma_f32_16x16x32_bf16 v[76:79], a[124:127], a[60:63], v[76:79]\n"
"v_exp_f32 v62, v62\n"
"s_nop 3\n"
"v_exp_f32 v63, v63\n"
"v_exp_f32 v64, v64\n"
"v_exp_f32 v65, v65\n"
"v_exp_f32 v66, v66\n"
"v_exp_f32 v67, v67\n"
"v_exp_f32 v68, v68\n"
"v_exp_f32 v69, v69\n"
"v_exp_f32 v70, v70\n"
"v_exp_f32 v71, v71\n"
"v_exp_f32 v72, v72\n"
"v_exp_f32 v73, v73\n"
"v_exp_f32 v74, v74\n"
"v_exp_f32 v75, v75\n"
"v_exp_f32 v76, v76\n"
"v_exp_f32 v77, v77\n"
"v_exp_f32 v78, v78\n"
"v_exp_f32 v79, v79\n"
"s_nop 0\n"
"v_cvt_pk_bf16_f32 v90, v60, v61\n"
"v_cvt_pk_bf16_f32 v91, v62, v63\n"
"v_cvt_pk_bf16_f32 v84, v64, v65\n"
"v_cvt_pk_bf16_f32 v85, v66, v67\n"
"v_cvt_pk_bf16_f32 v92, v68, v69\n"
"v_cvt_pk_bf16_f32 v93, v70, v71\n"
"v_cvt_pk_bf16_f32 v86, v72, v73\n"
"v_cvt_pk_bf16_f32 v87, v74, v75\n"
"v_cvt_pk_bf16_f32 v94, v76, v77\n"
"v_cvt_pk_bf16_f32 v95, v78, v79\n"
"s_waitcnt vmcnt(16)\n"
"v_mov_b32 v12, 1\n"
"buffer_store_dword v12, v246, s[52:55], 0 offen sc0 sc1\n"
"s_cmp_ge_i32 s63, s77\n"
"s_cbranch_scc0 LA1%=\n"
"s_branch LEV1%=\n"
"LSLOW%=:\n"
"s_lshl_b32 s65, s56, 6\n"
"s_sub_u32 s63, s63, s65\n"
"s_mov_b32 s73, 0\n"
"v_mov_b32 v8, 0\n"
"v_mov_b32 v9, 0\n"
"v_mov_b32 v10, 0xff61b1e6\n"
"v_mov_b32 v11, 0xff61b1e6\n"
"s_lshl_b32 s75, %4, 1\n"
"s_cmp_eq_u32 s68, 1\n"
"s_cselect_b32 s67, %7, %6\n"
"s_cselect_b32 s75, s75, 0\n"
"v_readlane_b32 s76, v2, s75\n"
"s_waitcnt vmcnt(0) ;keep\n"
"s_add_u32 s65, s67, 4096\n"
"s_add_u32 s66, s67, 32768\n"
"s_add_u32 s70, s67, 36864\n"
"buffer_load_dwordx4 a[0:3], v0, s[52:55], s67 offen offset:0\n"
"buffer_load_dwordx4 a[4:7], v0, s[52:55], s67 offen offset:1024\n"
"buffer_load_dwordx4 a[8:11], v0, s[52:55], s67 offen offset:2048\n"
"buffer_load_dwordx4 a[12:15], v0, s[52:55], s67 offen offset:3072\n"
"buffer_load_dwordx4 a[16:19], v0, s[52:55], s65 offen offset:0\n"
"buffer_load_dwordx4 a[20:23], v0, s[52:55], s65 offen offset:1024\n"
"buffer_load_dwordx4 a[24:27], v0, s[52:55], s65 offen offset:2048\n"
"buffer_load_dwordx4 a[28:31], v0, s[52:55], s65 offen offset:3072\n"
"buffer_load_dwordx4 a[32:35], v0, s[52:55], s66 offen offset:0\n"
"buffer_load_dwordx4 a[36:39], v0, s[52:55], s66 offen offset:1024\n"
"buffer_load_dwordx4 a[40:43], v0, s[52:55], s66 offen offset:2048\n"
"buffer_load_dwordx4 a[44:47], v0, s[52:55], s66 offen offset:3072\n"
"buffer_load_dwordx4 a[48:51], v0, s[52:55], s70 offen offset:0\n"
"buffer_load_dwordx4 a[52:55], v0, s[52:55], s70 offen offset:1024\n"
"buffer_load_dwordx4 a[56:59], v0, s[52:55], s70 offen offset:2048\n"
"buffer_load_dwordx4 a[60:63], v0, s[52:55], s70 offen offset:3072\n"
"LS0%=:\n"
"s_mov_b32 s57, 0\n"
"s_mov_b32 s74, s76\n"
"LS1%=:\n"
"v_cvt_f32_i32 v12, s63\n"
"v_add_f32 v12, v12, v3\n"
"v_mul_f32 v12, 0xf1c9f2ca, v12\n"
"v_add_f32 v16, 0x7149f2ca, v12\n"
"v_add_f32 v17, 0xf149f2ca, v12\n"
"v_add_f32 v18, 0xf2177617, v12\n"
"v_add_f32 v19, 0xf27c6f7c, v12\n"
"v_add_f32 v20, 0x74cb86af, v12\n"
"v_add_f32 v21, 0x74c85ee4, v12\n"
"v_add_f32 v22, 0x74c53719, v12\n"
"v_add_f32 v23, 0x74c20f4e, v12\n"
"v_add_f32 v24, 0xf3c3a333, v12\n"
"v_add_f32 v25, 0xf3d04260, v12\n"
"v_add_f32 v26, 0xf3dce18d, v12\n"
"v_add_f32 v27, 0xf3e980b9, v12\n"
"v_add_f32 v28, 0x749909fd, v12\n"
"v_add_f32 v29, 0x7495e232, v12\n"
"v_add_f32 v30, 0x7492ba67, v12\n"
"v_add_f32 v31, 0x748f929b, v12\n"
"v_add_f32 v32, 0xf446caff, v12\n"
"v_add_f32 v33, 0xf44d1a95, v12\n"
"v_add_f32 v34, 0xf4536a2b, v12\n"
"v_add_f32 v35, 0xf459b9c2, v12\n"
"v_add_f32 v36, 0x744d1a95, v12\n"
"v_add_f32 v37, 0x7446caff, v12\n"
"v_add_f32 v38, 0x74407b68, v12\n"
"v_add_f32 v39, 0x743a2bd2, v12\n"
"v_add_f32 v40, 0xf495e232, v12\n"
"v_add_f32 v41, 0xf49909fd, v12\n"
"v_add_f32 v42, 0xf49c31c8, v12\n"
"v_add_f32 v43, 0xf49f5993, v12\n"
"v_add_f32 v44, 0x73d04260, v12\n"
"v_add_f32 v45, 0x73c3a333, v12\n"
"v_add_f32 v46, 0x73b70407, v12\n"
"v_add_f32 v47, 0x73aa64da, v12\n"
"v_min_f32 v16, v16, v8\n"
"v_min_f32 v17, v17, v8\n"
"v_min_f32 v18, v18, v8\n"
"v_min_f32 v19, v19, v8\n"
"v_min_f32 v20, v20, v9\n"
"v_min_f32 v21, v21, v9\n"
"v_min_f32 v22, v22, v9\n"
"v_min_f32 v23, v23, v9\n"
"v_min_f32 v24, v24, v8\n"
"v_min_f32 v25, v25, v8\n"
"v_min_f32 v26, v26, v8\n"
"v_min_f32 v27, v27, v8\n"
"v_min_f32 v28, v28, v9\n"
"v_min_f32 v29, v29, v9\n"
"v_min_f32 v30, v30, v9\n"
"v_min_f32 v31, v31, v9\n"
"v_min_f32 v32, v32, v8\n"
"v_min_f32 v33, v33, v8\n"
"v_min_f32 v34, v34, v8\n"
"v_min_f32 v35, v35, v8\n"
"v_min_f32 v36, v36, v9\n"
"v_min_f32 v37, v37, v9\n"
"v_min_f32 v38, v38, v9\n"
"v_min_f32 v39, v39, v9\n"
"v_min_f32 v40, v40, v8\n"
"v_min_f32 v41, v41, v8\n"
"v_min_f32 v42, v42, v8\n"
"v_min_f32 v43, v43, v8\n"
"v_min_f32 v44, v44, v9\n"
"v_min_f32 v45, v45, v9\n"
"v_min_f32 v46, v46, v9\n"
"v_min_f32 v47, v47, v9\n"
"s_add_u32 s65, s74, 0\n"
"buffer_load_dwordx4 a[64:67], v0, s[52:55], s65 offen offset:0\n"
"buffer_load_dwordx4 a[68:71], v0, s[52:55], s65 offen offset:1024\n"
"buffer_load_dwordx4 a[72:75], v0, s[52:55], s65 offen offset:2048\n"
"buffer_load_dwordx4 a[76:79], v0, s[52:55], s65 offen offset:3072\n"
"s_add_u32 s65, s74, 4096\n"
"buffer_load_dwordx4 a[80:83], v0, s[52:55], s65 offen offset:0\n"
"buffer_load_dwordx4 a[84:87], v0, s[52:55], s65 offen offset:1024\n"
"buffer_load_dwordx4 a[88:91], v0, s[52:55], s65 offen offset:2048\n"
"buffer_load_dwordx4 a[92:95], v0, s[52:55], s65 offen offset:3072\n"
"s_add_u32 s65, s74, 8192\n"
"buffer_load_dwordx4 a[96:99], v0, s[52:55], s65 offen offset:0\n"
"buffer_load_dwordx4 a[100:103], v0, s[52:55], s65 offen offset:1024\n"
"buffer_load_dwordx4 a[104:107], v0, s[52:55], s65 offen offset:2048\n"
"buffer_load_dwordx4 a[108:111], v0, s[52:55], s65 offen offset:3072\n"
"s_add_u32 s65, s74, 12288\n"
"buffer_load_dwordx4 a[112:115], v0, s[52:55], s65 offen offset:0\n"
"buffer_load_dwordx4 a[116:119], v0, s[52:55], s65 offen offset:1024\n"
"buffer_load_dwordx4 a[120:123], v0, s[52:55], s65 offen offset:2048\n"
"buffer_load_dwordx4 a[124:127], v0, s[52:55], s65 offen offset:3072\n"
"s_waitcnt vmcnt(0) ;keep\n"
"v_mfma_f32_16x16x32_bf16 v[48:51], a[64:67], a[0:3], v[16:19]\n"
"v_mfma_f32_16x16x32_bf16 v[48:51], a[68:71], a[4:7], v[48:51]\n"
"v_mfma_f32_16x16x32_bf16 v[48:51], a[72:75], a[8:11], v[48:51]\n"
"v_mfma_f32_16x16x32_bf16 v[48:51], a[76:79], a[12:15], v[48:51]\n"
"v_mfma_f32_16x16x32_bf16 v[48:51], a[80:83], a[16:19], v[48:51]\n"
"v_mfma_f32_16x16x32_bf16 v[48:51], a[84:87], a[20:23], v[48:51]\n"
"v_mfma_f32_16x16x32_bf16 v[48:51], a[88:91], a[24:27], v[48:51]\n"
"v_mfma_f32_16x16x32_bf16 v[48:51], a[92:95], a[28:31], v[48:51]\n"
"v_mfma_f32_16x16x32_bf16 v[52:55], a[64:67], a[32:35], v[20:23]\n"
"v_mfma_f32_16x16x32_bf16 v[52:55], a[68:71], a[36:39], v[52:55]\n"
"v_mfma_f32_16x16x32_bf16 v[52:55], a[72:75], a[40:43], v[52:55]\n"
"v_mfma_f32_16x16x32_bf16 v[52:55], a[76:79], a[44:47], v[52:55]\n"
"v_mfma_f32_16x16x32_bf16 v[52:55], a[80:83], a[48:51], v[52:55]\n"
"v_mfma_f32_16x16x32_bf16 v[52:55], a[84:87], a[52:55], v[52:55]\n"
"v_mfma_f32_16x16x32_bf16 v[52:55], a[88:91], a[56:59], v[52:55]\n"
"v_mfma_f32_16x16x32_bf16 v[52:55], a[92:95], a[60:63], v[52:55]\n"
"v_mfma_f32_16x16x32_bf16 v[56:59], a[96:99], a[0:3], v[24:27]\n"
"v_mfma_f32_16x16x32_bf16 v[56:59], a[100:103], a[4:7], v[56:59]\n"
"v_mfma_f32_16x16x32_bf16 v[56:59], a[104:107], a[8:11], v[56:59]\n"
"v_mfma_f32_16x16x32_bf16 v[56:59], a[108:111], a[12:15], v[56:59]\n"
"v_mfma_f32_16x16x32_bf16 v[56:59], a[112:115], a[16:19], v[56:59]\n"
"v_mfma_f32_16x16x32_bf16 v[56:59], a[116:119], a[20:23], v[56:59]\n"
"v_mfma_f32_16x16x32_bf16 v[56:59], a[120:123], a[24:27], v[56:59]\n"
"v_mfma_f32_16x16x32_bf16 v[56:59], a[124:127], a[28:31], v[56:59]\n"
"v_mfma_f32_16x16x32_bf16 v[60:63], a[96:99], a[32:35], v[28:31]\n"
"v_mfma_f32_16x16x32_bf16 v[60:63], a[100:103], a[36:39], v[60:63]\n"
"v_mfma_f32_16x16x32_bf16 v[60:63], a[104:107], a[40:43], v[60:63]\n"
"v_mfma_f32_16x16x32_bf16 v[60:63], a[108:111], a[44:47], v[60:63]\n"
"v_mfma_f32_16x16x32_bf16 v[60:63], a[112:115], a[48:51], v[60:63]\n"
"v_mfma_f32_16x16x32_bf16 v[60:63], a[116:119], a[52:55], v[60:63]\n"
"v_mfma_f32_16x16x32_bf16 v[60:63], a[120:123], a[56:59], v[60:63]\n"
"v_mfma_f32_16x16x32_bf16 v[60:63], a[124:127], a[60:63], v[60:63]\n"
"s_add_u32 s65, s74, 16384\n"
"buffer_load_dwordx4 a[64:67], v0, s[52:55], s65 offen offset:0\n"
"buffer_load_dwordx4 a[68:71], v0, s[52:55], s65 offen offset:1024\n"
"buffer_load_dwordx4 a[72:75], v0, s[52:55], s65 offen offset:2048\n"
"buffer_load_dwordx4 a[76:79], v0, s[52:55], s65 offen offset:3072\n"
"s_add_u32 s65, s74, 20480\n"
"buffer_load_dwordx4 a[80:83], v0, s[52:55], s65 offen offset:0\n"
"buffer_load_dwordx4 a[84:87], v0, s[52:55], s65 offen offset:1024\n"
"buffer_load_dwordx4 a[88:91], v0, s[52:55], s65 offen offset:2048\n"
"buffer_load_dwordx4 a[92:95], v0, s[52:55], s65 offen offset:3072\n"
"s_add_u32 s65, s74, 24576\n"
"buffer_load_dwordx4 a[96:99], v0, s[52:55], s65 offen offset:0\n"
"buffer_load_dwordx4 a[100:103], v0, s[52:55], s65 offen offset:1024\n"
"buffer_load_dwordx4 a[104:107], v0, s[52:55], s65 offen offset:2048\n"
"buffer_load_dwordx4 a[108:111], v0, s[52:55], s65 offen offset:3072\n"
"s_add_u32 s65, s74, 28672\n"
"buffer_load_dwordx4 a[112:115], v0, s[52:55], s65 offen offset:0\n"
"buffer_load_dwordx4 a[116:119], v0, s[52:55], s65 offen offset:1024\n"
"buffer_load_dwordx4 a[120:123], v0, s[52:55], s65 offen offset:2048\n"
"buffer_load_dwordx4 a[124:127], v0, s[52:55], s65 offen offset:3072\n"
"s_waitcnt vmcnt(0) ;keep\n"
"v_mfma_f32_16x16x32_bf16 v[64:67], a[64:67], a[0:3], v[32:35]\n"
"v_mfma_f32_16x16x32_bf16 v[64:67], a[68:71], a[4:7], v[64:67]\n"
"v_mfma_f32_16x16x32_bf16 v[64:67], a[72:75], a[8:11], v[64:67]\n"
"v_mfma_f32_16x16x32_bf16 v[64:67], a[76:79], a[12:15], v[64:67]\n"
"v_mfma_f32_16x16x32_bf16 v[64:67], a[80:83], a[16:19], v[64:67]\n"
"v_mfma_f32_16x16x32_bf16 v[64:67], a[84:87], a[20:23], v[64:67]\n"
"v_mfma_f32_16x16x32_bf16 v[64:67], a[88:91], a[24:27], v[64:67]\n"
"v_mfma_f32_16x16x32_bf16 v[64:67], a[92:95], a[28:31], v[64:67]\n"
"v_mfma_f32_16x16x32_bf16 v[68:71], a[64:67], a[32:35], v[36:39]\n"
"v_mfma_f32_16x16x32_bf16 v[68:71], a[68:71], a[36:39], v[68:71]\n"
"v_mfma_f32_16x16x32_bf16 v[68:71], a[72:75], a[40:43], v[68:71]\n"
"v_mfma_f32_16x16x32_bf16 v[68:71], a[76:79], a[44:47], v[68:71]\n"
"v_mfma_f32_16x16x32_bf16 v[68:71], a[80:83], a[48:51], v[68:71]\n"
"v_mfma_f32_16x16x32_bf16 v[68:71], a[84:87], a[52:55], v[68:71]\n"
"v_mfma_f32_16x16x32_bf16 v[68:71], a[88:91], a[56:59], v[68:71]\n"
"v_mfma_f32_16x16x32_bf16 v[68:71], a[92:95], a[60:63], v[68:71]\n"
"v_mfma_f32_16x16x32_bf16 v[72:75], a[96:99], a[0:3], v[40:43]\n"
"v_mfma_f32_16x16x32_bf16 v[72:75], a[100:103], a[4:7], v[72:75]\n"
"v_mfma_f32_16x16x32_bf16 v[72:75], a[104:107], a[8:11], v[72:75]\n"
"v_mfma_f32_16x16x32_bf16 v[72:75], a[108:111], a[12:15], v[72:75]\n"
"v_mfma_f32_16x16x32_bf16 v[72:75], a[112:115], a[16:19], v[72:75]\n"
"v_mfma_f32_16x16x32_bf16 v[72:75], a[116:119], a[20:23], v[72:75]\n"
"v_mfma_f32_16x16x32_bf16 v[72:75], a[120:123], a[24:27], v[72:75]\n"
"v_mfma_f32_16x16x32_bf16 v[72:75], a[124:127], a[28:31], v[72:75]\n"
"v_mfma_f32_16x16x32_bf16 v[76:79], a[96:99], a[32:35], v[44:47]\n"
"v_mfma_f32_16x16x32_bf16 v[76:79], a[100:103], a[36:39], v[76:79]\n"
"v_mfma_f32_16x16x32_bf16 v[76:79], a[104:107], a[40:43], v[76:79]\n"
"v_mfma_f32_16x16x32_bf16 v[76:79], a[108:111], a[44:47], v[76:79]\n"
"v_mfma_f32_16x16x32_bf16 v[76:79], a[112:115], a[48:51], v[76:79]\n"
"v_mfma_f32_16x16x32_bf16 v[76:79], a[116:119], a[52:55], v[76:79]\n"
"v_mfma_f32_16x16x32_bf16 v[76:79], a[120:123], a[56:59], v[76:79]\n"
"v_mfma_f32_16x16x32_bf16 v[76:79], a[124:127], a[60:63], v[76:79]\n"
"s_nop 7\n"
"s_cmp_eq_u32 s73, 1\n"
"s_cbranch_scc1 LS2%=\n"
"v_max3_f32 v14, v48, v49, v50\n"
"v_max3_f32 v15, v52, v53, v54\n"
"v_max3_f32 v14, v14, v51, v56\n"
"v_max3_f32 v15, v15, v55, v60\n"
"v_max3_f32 v14, v14, v57, v58\n"
"v_max3_f32 v15, v15, v61, v62\n"
"v_max3_f32 v14, v14, v59, v64\n"
"v_max3_f32 v15, v15, v63, v68\n"
"v_max3_f32 v14, v14, v65, v66\n"
"v_max3_f32 v15, v15, v69, v70\n"
"v_max3_f32 v14, v14, v67, v72\n"
"v_max3_f32 v15, v15, v71, v76\n"
"v_max3_f32 v14, v14, v73, v74\n"
"v_max3_f32 v15, v15, v77, v78\n"
"v_max_f32 v14, v14, v75\n"
"v_max_f32 v15, v15, v79\n"
"v_max_f32 v10, v10, v14\n"
"v_max_f32 v11, v11, v15\n"
"s_branch LS3%=\n"
"LS2%=:\n"
"v_exp_f32 v48, v48\n"
"v_exp_f32 v49, v49\n"
"v_exp_f32 v50, v50\n"
"v_exp_f32 v51, v51\n"
"v_exp_f32 v52, v52\n"
"v_exp_f32 v53, v53\n"
"v_exp_f32 v54, v54\n"
"v_exp_f32 v55, v55\n"
"v_exp_f32 v56, v56\n"
"v_exp_f32 v57, v57\n"
"v_exp_f32 v58, v58\n"
"v_exp_f32 v59, v59\n"
"v_exp_f32 v60, v60\n"
"v_exp_f32 v61, v61\n"
"v_exp_f32 v62, v62\n"
"v_exp_f32 v63, v63\n"
"v_exp_f32 v64, v64\n"
"v_exp_f32 v65, v65\n"
"v_exp_f32 v66, v66\n"
"v_exp_f32 v67, v67\n"
"v_exp_f32 v68, v68\n"
"v_exp_f32 v69, v69\n"
"v_exp_f32 v70, v70\n"
"v_exp_f32 v71, v71\n"
"v_exp_f32 v72, v72\n"
"v_exp_f32 v73, v73\n"
"v_exp_f32 v74, v74\n"
"v_exp_f32 v75, v75\n"
"v_exp_f32 v76, v76\n"
"v_exp_f32 v77, v77\n"
"v_exp_f32 v78, v78\n"
"v_exp_f32 v79, v79\n"
"s_nop 0\n"
"v_cvt_pk_bf16_f32 v80, v48, v49\n"
"v_cvt_pk_bf16_f32 v81, v50, v51\n"
"v_cvt_pk_bf16_f32 v88, v52, v53\n"
"v_cvt_pk_bf16_f32 v89, v54, v55\n"
"v_cvt_pk_bf16_f32 v82, v56, v57\n"
"v_cvt_pk_bf16_f32 v83, v58, v59\n"
"v_cvt_pk_bf16_f32 v90, v60, v61\n"
"v_cvt_pk_bf16_f32 v91, v62, v63\n"
"v_cvt_pk_bf16_f32 v84, v64, v65\n"
"v_cvt_pk_bf16_f32 v85, v66, v67\n"
"v_cvt_pk_bf16_f32 v92, v68, v69\n"
"v_cvt_pk_bf16_f32 v93, v70, v71\n"
"v_cvt_pk_bf16_f32 v86, v72, v73\n"
"v_cvt_pk_bf16_f32 v87, v74, v75\n"
"v_cvt_pk_bf16_f32 v94, v76, v77\n"
"v_cvt_pk_bf16_f32 v95, v78, v79\n"
"s_add_u32 s65, s74, 16777216\n"
"buffer_load_dwordx4 a[64:67], v0, s[52:55], s65 offen offset:0\n"
"buffer_load_dwordx4 a[68:71], v0, s[52:55], s65 offen offset:1024\n"
"buffer_load_dwordx4 a[72:75], v0, s[52:55], s65 offen offset:2048\n"
"buffer_load_dwordx4 a[76:79], v0, s[52:55], s65 offen offset:3072\n"
"s_add_u32 s65, s74, 16781312\n"
"buffer_load_dwordx4 a[80:83], v0, s[52:55], s65 offen offset:0\n"
"buffer_load_dwordx4 a[84:87], v0, s[52:55], s65 offen offset:1024\n"
"buffer_load_dwordx4 a[88:91], v0, s[52:55], s65 offen offset:2048\n"
"buffer_load_dwordx4 a[92:95], v0, s[52:55], s65 offen offset:3072\n"
"s_add_u32 s65, s74, 16785408\n"
"buffer_load_dwordx4 a[96:99], v0, s[52:55], s65 offen offset:0\n"
"buffer_load_dwordx4 a[100:103], v0, s[52:55], s65 offen offset:1024\n"
"buffer_load_dwordx4 a[104:107], v0, s[52:55], s65 offen offset:2048\n"
"buffer_load_dwordx4 a[108:111], v0, s[52:55], s65 offen offset:3072\n"
"s_add_u32 s65, s74, 16789504\n"
"buffer_load_dwordx4 a[112:115], v0, s[52:55], s65 offen offset:0\n"
"buffer_load_dwordx4 a[116:119], v0, s[52:55], s65 offen offset:1024\n"
"buffer_load_dwordx4 a[120:123], v0, s[52:55], s65 offen offset:2048\n"
"buffer_load_dwordx4 a[124:127], v0, s[52:55], s65 offen offset:3072\n"
"s_waitcnt vmcnt(0) ;keep\n"
"v_mfma_f32_16x16x32_bf16 v[112:115], a[64:67], v[80:83], v[112:115]\n"
"v_mfma_f32_16x16x32_bf16 v[112:115], a[68:71], v[84:87], v[112:115]\n"
"v_mfma_f32_16x16x32_bf16 v[116:119], a[64:67], v[88:91], v[116:119]\n"
"v_mfma_f32_16x16x32_bf16 v[116:119], a[68:71], v[92:95], v[116:119]\n"
"v_mfma_f32_16x16x32_bf16 v[120:123], a[72:75], v[80:83], v[120:123]\n"
"v_mfma_f32_16x16x32_bf16 v[120:123], a[76:79], v[84:87], v[120:123]\n"
"v_mfma_f32_16x16x32_bf16 v[124:127], a[72:75], v[88:91], v[124:127]\n"
"v_mfma_f32_16x16x32_bf16 v[124:127], a[76:79], v[92:95], v[124:127]\n"
"v_mfma_f32_16x16x32_bf16 v[128:131], a[80:83], v[80:83], v[128:131]\n"
"v_mfma_f32_16x16x32_bf16 v[128:131], a[84:87], v[84:87], v[128:131]\n"
"v_mfma_f32_16x16x32_bf16 v[132:135], a[80:83], v[88:91], v[132:135]\n"
"v_mfma_f32_16x16x32_bf16 v[132:135], a[84:87], v[92:95], v[132:135]\n"
"v_mfma_f32_16x16x32_bf16 v[136:139], a[88:91], v[80:83], v[136:139]\n"
"v_mfma_f32_16x16x32_bf16 v[136:139], a[92:95], v[84:87], v[136:139]\n"
"v_mfma_f32_16x16x32_bf16 v[140:143], a[88:91], v[88:91], v[140:143]\n"
"v_mfma_f32_16x16x32_bf16 v[140:143], a[92:95], v[92:95], v[140:143]\n"
"v_mfma_f32_16x16x32_bf16 v[144:147], a[96:99], v[80:83], v[144:147]\n"
"v_mfma_f32_16x16x32_bf16 v[144:147], a[100:103], v[84:87], v[144:147]\n"
"v_mfma_f32_16x16x32_bf16 v[148:151], a[96:99], v[88:91], v[148:151]\n"
"v_mfma_f32_16x16x32_bf16 v[148:151], a[100:103], v[92:95], v[148:151]\n"
"v_mfma_f32_16x16x32_bf16 v[152:155], a[104:107], v[80:83], v[152:155]\n"
"v_mfma_f32_16x16x32_bf16 v[152:155], a[108:111], v[84:87], v[152:155]\n"
"v_mfma_f32_16x16x32_bf16 v[156:159], a[104:107], v[88:91], v[156:159]\n"
"v_mfma_f32_16x16x32_bf16 v[156:159], a[108:111], v[92:95], v[156:159]\n"
"v_mfma_f32_16x16x32_bf16 v[160:163], a[112:115], v[80:83], v[160:163]\n"
"v_mfma_f32_16x16x32_bf16 v[160:163], a[116:119], v[84:87], v[160:163]\n"
"v_mfma_f32_16x16x32_bf16 v[164:167], a[112:115], v[88:91], v[164:167]\n"
"v_mfma_f32_16x16x32_bf16 v[164:167], a[116:119], v[92:95], v[164:167]\n"
"v_mfma_f32_16x16x32_bf16 v[168:171], a[120:123], v[80:83], v[168:171]\n"
"v_mfma_f32_16x16x32_bf16 v[168:171], a[124:127], v[84:87], v[168:171]\n"
"v_mfma_f32_16x16x32_bf16 v[172:175], a[120:123], v[88:91], v[172:175]\n"
"v_mfma_f32_16x16x32_bf16 v[172:175], a[124:127], v[92:95], v[172:175]\n"
"s_add_u32 s65, s74, 16793600\n"
"buffer_load_dwordx4 a[64:67], v0, s[52:55], s65 offen offset:0\n"
"buffer_load_dwordx4 a[68:71], v0, s[52:55], s65 offen offset:1024\n"
"buffer_load_dwordx4 a[72:75], v0, s[52:55], s65 offen offset:2048\n"
"buffer_load_dwordx4 a[76:79], v0, s[52:55], s65 offen offset:3072\n"
"s_add_u32 s65, s74, 16797696\n"
"buffer_load_dwordx4 a[80:83], v0, s[52:55], s65 offen offset:0\n"
"buffer_load_dwordx4 a[84:87], v0, s[52:55], s65 offen offset:1024\n"
"buffer_load_dwordx4 a[88:91], v0, s[52:55], s65 offen offset:2048\n"
"buffer_load_dwordx4 a[92:95], v0, s[52:55], s65 offen offset:3072\n"
"s_add_u32 s65, s74, 16801792\n"
"buffer_load_dwordx4 a[96:99], v0, s[52:55], s65 offen offset:0\n"
"buffer_load_dwordx4 a[100:103], v0, s[52:55], s65 offen offset:1024\n"
"buffer_load_dwordx4 a[104:107], v0, s[52:55], s65 offen offset:2048\n"
"buffer_load_dwordx4 a[108:111], v0, s[52:55], s65 offen offset:3072\n"
"s_add_u32 s65, s74, 16805888\n"
"buffer_load_dwordx4 a[112:115], v0, s[52:55], s65 offen offset:0\n"
"buffer_load_dwordx4 a[116:119], v0, s[52:55], s65 offen offset:1024\n"
"buffer_load_dwordx4 a[120:123], v0, s[52:55], s65 offen offset:2048\n"
"buffer_load_dwordx4 a[124:127], v0, s[52:55], s65 offen offset:3072\n"
"s_waitcnt vmcnt(0) ;keep\n"
"v_mfma_f32_16x16x32_bf16 v[176:179], a[64:67], v[80:83], v[176:179]\n"
"v_mfma_f32_16x16x32_bf16 v[176:179], a[68:71], v[84:87], v[176:179]\n"
"v_mfma_f32_16x16x32_bf16 v[180:183], a[64:67], v[88:91], v[180:183]\n"
"v_mfma_f32_16x16x32_bf16 v[180:183], a[68:71], v[92:95], v[180:183]\n"
"v_mfma_f32_16x16x32_bf16 v[184:187], a[72:75], v[80:83], v[184:187]\n"
"v_mfma_f32_16x16x32_bf16 v[184:187], a[76:79], v[84:87], v[184:187]\n"
"v_mfma_f32_16x16x32_bf16 v[188:191], a[72:75], v[88:91], v[188:191]\n"
"v_mfma_f32_16x16x32_bf16 v[188:191], a[76:79], v[92:95], v[188:191]\n"
"v_mfma_f32_16x16x32_bf16 v[192:195], a[80:83], v[80:83], v[192:195]\n"
"v_mfma_f32_16x16x32_bf16 v[192:195], a[84:87], v[84:87], v[192:195]\n"
"v_mfma_f32_16x16x32_bf16 v[196:199], a[80:83], v[88:91], v[196:199]\n"
"v_mfma_f32_16x16x32_bf16 v[196:199], a[84:87], v[92:95], v[196:199]\n"
"v_mfma_f32_16x16x32_bf16 v[200:203], a[88:91], v[80:83], v[200:203]\n"
"v_mfma_f32_16x16x32_bf16 v[200:203], a[92:95], v[84:87], v[200:203]\n"
"v_mfma_f32_16x16x32_bf16 v[204:207], a[88:91], v[88:91], v[204:207]\n"
"v_mfma_f32_16x16x32_bf16 v[204:207], a[92:95], v[92:95], v[204:207]\n"
"v_mfma_f32_16x16x32_bf16 v[208:211], a[96:99], v[80:83], v[208:211]\n"
"v_mfma_f32_16x16x32_bf16 v[208:211], a[100:103], v[84:87], v[208:211]\n"
"v_mfma_f32_16x16x32_bf16 v[212:215], a[96:99], v[88:91], v[212:215]\n"
"v_mfma_f32_16x16x32_bf16 v[212:215], a[100:103], v[92:95], v[212:215]\n"
"v_mfma_f32_16x16x32_bf16 v[216:219], a[104:107], v[80:83], v[216:219]\n"
"v_mfma_f32_16x16x32_bf16 v[216:219], a[108:111], v[84:87], v[216:219]\n"
"v_mfma_f32_16x16x32_bf16 v[220:223], a[104:107], v[88:91], v[220:223]\n"
"v_mfma_f32_16x16x32_bf16 v[220:223], a[108:111], v[92:95], v[220:223]\n"
"v_mfma_f32_16x16x32_bf16 v[224:227], a[112:115], v[80:83], v[224:227]\n"
"v_mfma_f32_16x16x32_bf16 v[224:227], a[116:119], v[84:87], v[224:227]\n"
"v_mfma_f32_16x16x32_bf16 v[228:231], a[112:115], v[88:91], v[228:231]\n"
"v_mfma_f32_16x16x32_bf16 v[228:231], a[116:119], v[92:95], v[228:231]\n"
"v_mfma_f32_16x16x32_bf16 v[232:235], a[120:123], v[80:83], v[232:235]\n"
"v_mfma_f32_16x16x32_bf16 v[232:235], a[124:127], v[84:87], v[232:235]\n"
"v_mfma_f32_16x16x32_bf16 v[236:239], a[120:123], v[88:91], v[236:239]\n"
"v_mfma_f32_16x16x32_bf16 v[236:239], a[124:127], v[92:95], v[236:239]\n"
"v_mfma_f32_16x16x32_bf16 a[128:131], a[136:139], v[80:83], a[128:131]\n"
"v_mfma_f32_16x16x32_bf16 a[128:131], a[136:139], v[84:87], a[128:131]\n"
"v_mfma_f32_16x16x32_bf16 a[132:135], a[136:139], v[88:91], a[132:135]\n"
"v_mfma_f32_16x16x32_bf16 a[132:135], a[136:139], v[92:95], a[132:135]\n"
"LS3%=:\n"
"s_nop 3\n"
"s_add_u32 s74, s74, 0x8000\n"
"s_add_u32 s63, s63, 64\n"
"s_add_u32 s57, s57, 1\n"
"s_cmp_lt_u32 s57, s56\n"
"s_cbranch_scc1 LS1%=\n"
"s_cmp_eq_u32 s73, 1\n"
"s_cbranch_scc1 LS4%=\n"
"v_mov_b32 v12, v10\n"
"v_mov_b32 v13, v11\n"
"s_nop 1\n"
"v_permlane32_swap_b32 v12, v10\n"
"v_permlane32_swap_b32 v13, v11\n"
"s_nop 0\n"
"v_max_f32 v10, v10, v12\n"
"v_max_f32 v11, v11, v13\n"
"v_mov_b32 v12, v10\n"
"v_mov_b32 v13, v11\n"
"s_nop 1\n"
"v_permlane16_swap_b32 v12, v10\n"
"v_permlane16_swap_b32 v13, v11\n"
"s_nop 0\n"
"v_max_f32 v10, v10, v12\n"
"v_max_f32 v11, v11, v13\n"
"v_max_f32 v10, 0xe0ad78ec, v10\n"
"v_max_f32 v11, 0xe0ad78ec, v11\n"
"v_sub_f32 v8, 0, v10\n"
"v_sub_f32 v9, 0, v11\n"
"v_mov_b32 v112, 0\n"
"v_mov_b32 v113, 0\n"
"v_mov_b32 v114, 0\n"
"v_mov_b32 v115, 0\n"
"v_mov_b32 v116, 0\n"
"v_mov_b32 v117, 0\n"
"v_mov_b32 v118, 0\n"
"v_mov_b32 v119, 0\n"
"v_mov_b32 v120, 0\n"
"v_mov_b32 v121, 0\n"
"v_mov_b32 v122, 0\n"
"v_mov_b32 v123, 0\n"
"v_mov_b32 v124, 0\n"
"v_mov_b32 v125, 0\n"
"v_mov_b32 v126, 0\n"
"v_mov_b32 v127, 0\n"
"v_mov_b32 v128, 0\n"
"v_mov_b32 v129, 0\n"
"v_mov_b32 v130, 0\n"
"v_mov_b32 v131, 0\n"
"v_mov_b32 v132, 0\n"
"v_mov_b32 v133, 0\n"
"v_mov_b32 v134, 0\n"
"v_mov_b32 v135, 0\n"
"v_mov_b32 v136, 0\n"
"v_mov_b32 v137, 0\n"
"v_mov_b32 v138, 0\n"
"v_mov_b32 v139, 0\n"
"v_mov_b32 v140, 0\n"
"v_mov_b32 v141, 0\n"
"v_mov_b32 v142, 0\n"
"v_mov_b32 v143, 0\n"
"v_mov_b32 v144, 0\n"
"v_mov_b32 v145, 0\n"
"v_mov_b32 v146, 0\n"
"v_mov_b32 v147, 0\n"
"v_mov_b32 v148, 0\n"
"v_mov_b32 v149, 0\n"
"v_mov_b32 v150, 0\n"
"v_mov_b32 v151, 0\n"
"v_mov_b32 v152, 0\n"
"v_mov_b32 v153, 0\n"
"v_mov_b32 v154, 0\n"
"v_mov_b32 v155, 0\n"
"v_mov_b32 v156, 0\n"
"v_mov_b32 v157, 0\n"
"v_mov_b32 v158, 0\n"
"v_mov_b32 v159, 0\n"
"v_mov_b32 v160, 0\n"
"v_mov_b32 v161, 0\n"
"v_mov_b32 v162, 0\n"
"v_mov_b32 v163, 0\n"
"v_mov_b32 v164, 0\n"
"v_mov_b32 v165, 0\n"
"v_mov_b32 v166, 0\n"
"v_mov_b32 v167, 0\n"
"v_mov_b32 v168, 0\n"
"v_mov_b32 v169, 0\n"
"v_mov_b32 v170, 0\n"
"v_mov_b32 v171, 0\n"
"v_mov_b32 v172, 0\n"
"v_mov_b32 v173, 0\n"
"v_mov_b32 v174, 0\n"
"v_mov_b32 v175, 0\n"
"v_mov_b32 v176, 0\n"
"v_mov_b32 v177, 0\n"
"v_mov_b32 v178, 0\n"
"v_mov_b32 v179, 0\n"
"v_mov_b32 v180, 0\n"
"v_mov_b32 v181, 0\n"
"v_mov_b32 v182, 0\n"
"v_mov_b32 v183, 0\n"
"v_mov_b32 v184, 0\n"
"v_mov_b32 v185, 0\n"
"v_mov_b32 v186, 0\n"
"v_mov_b32 v187, 0\n"
"v_mov_b32 v188, 0\n"
"v_mov_b32 v189, 0\n"
"v_mov_b32 v190, 0\n"
"v_mov_b32 v191, 0\n"
"v_mov_b32 v192, 0\n"
"v_mov_b32 v193, 0\n"
"v_mov_b32 v194, 0\n"
"v_mov_b32 v195, 0\n"
"v_mov_b32 v196, 0\n"
"v_mov_b32 v197, 0\n"
"v_mov_b32 v198, 0\n"
"v_mov_b32 v199, 0\n"
"v_mov_b32 v200, 0\n"
"v_mov_b32 v201, 0\n"
"v_mov_b32 v202, 0\n"
"v_mov_b32 v203, 0\n"
"v_mov_b32 v204, 0\n"
"v_mov_b32 v205, 0\n"
"v_mov_b32 v206, 0\n"
"v_mov_b32 v207, 0\n"
"v_mov_b32 v208, 0\n"
"v_mov_b32 v209, 0\n"
"v_mov_b32 v210, 0\n"
"v_mov_b32 v211, 0\n"
"v_mov_b32 v212, 0\n"
"v_mov_b32 v213, 0\n"
"v_mov_b32 v214, 0\n"
"v_mov_b32 v215, 0\n"
"v_mov_b32 v216, 0\n"
"v_mov_b32 v217, 0\n"
"v_mov_b32 v218, 0\n"
"v_mov_b32 v219, 0\n"
"v_mov_b32 v220, 0\n"
"v_mov_b32 v221, 0\n"
"v_mov_b32 v222, 0\n"
"v_mov_b32 v223, 0\n"
"v_mov_b32 v224, 0\n"
"v_mov_b32 v225, 0\n"
"v_mov_b32 v226, 0\n"
"v_mov_b32 v227, 0\n"
"v_mov_b32 v228, 0\n"
"v_mov_b32 v229, 0\n"
"v_mov_b32 v230, 0\n"
"v_mov_b32 v231, 0\n"
"v_mov_b32 v232, 0\n"
"v_mov_b32 v233, 0\n"
"v_mov_b32 v234, 0\n"
"v_mov_b32 v235, 0\n"
"v_mov_b32 v236, 0\n"
"v_mov_b32 v237, 0\n"
"v_mov_b32 v238, 0\n"
"v_mov_b32 v239, 0\n"
"v_accvgpr_write_b32 a128, 0\n"
"v_accvgpr_write_b32 a129, 0\n"
"v_accvgpr_write_b32 a130, 0\n"
"v_accvgpr_write_b32 a131, 0\n"
"v_accvgpr_write_b32 a132, 0\n"
"v_accvgpr_write_b32 a133, 0\n"
"v_accvgpr_write_b32 a134, 0\n"
"v_accvgpr_write_b32 a135, 0\n"
"s_lshl_b32 s65, s56, 6\n"
"s_sub_u32 s63, s63, s65\n"
"s_mov_b32 s73, 1\n"
"s_branch LS0%=\n"
"LS4%=:\n"
"s_cmp_eq_u32 s64, 0\n"
"s_cbranch_scc1 LS5%=\n"
"s_nop 7\n"
"v_accvgpr_mov_b32 a0, a176\n"
"v_accvgpr_mov_b32 a1, a177\n"
"v_accvgpr_mov_b32 a2, a178\n"
"v_accvgpr_mov_b32 a3, a179\n"
"v_accvgpr_mov_b32 a4, a180\n"
"v_accvgpr_mov_b32 a5, a181\n"
"v_accvgpr_mov_b32 a6, a182\n"
"v_accvgpr_mov_b32 a7, a183\n"
"v_accvgpr_mov_b32 a8, a184\n"
"v_accvgpr_mov_b32 a9, a185\n"
"v_accvgpr_mov_b32 a10, a186\n"
"v_accvgpr_mov_b32 a11, a187\n"
"v_accvgpr_mov_b32 a12, a188\n"
"v_accvgpr_mov_b32 a13, a189\n"
"v_accvgpr_mov_b32 a14, a190\n"
"v_accvgpr_mov_b32 a15, a191\n"
"v_accvgpr_mov_b32 a16, a192\n"
"v_accvgpr_mov_b32 a17, a193\n"
"v_accvgpr_mov_b32 a18, a194\n"
"v_accvgpr_mov_b32 a19, a195\n"
"v_accvgpr_mov_b32 a20, a196\n"
"v_accvgpr_mov_b32 a21, a197\n"
"v_accvgpr_mov_b32 a22, a198\n"
"v_accvgpr_mov_b32 a23, a199\n"
"v_accvgpr_mov_b32 a24, a200\n"
"v_accvgpr_mov_b32 a25, a201\n"
"v_accvgpr_mov_b32 a26, a202\n"
"v_accvgpr_mov_b32 a27, a203\n"
"v_accvgpr_mov_b32 a28, a204\n"
"v_accvgpr_mov_b32 a29, a205\n"
"v_accvgpr_mov_b32 a30, a206\n"
"v_accvgpr_mov_b32 a31, a207\n"
"v_accvgpr_mov_b32 a32, a208\n"
"v_accvgpr_mov_b32 a33, a209\n"
"v_accvgpr_mov_b32 a34, a210\n"
"v_accvgpr_mov_b32 a35, a211\n"
"v_accvgpr_mov_b32 a36, a212\n"
"v_accvgpr_mov_b32 a37, a213\n"
"v_accvgpr_mov_b32 a38, a214\n"
"v_accvgpr_mov_b32 a39, a215\n"
"v_accvgpr_mov_b32 a40, a216\n"
"v_accvgpr_mov_b32 a41, a217\n"
"v_accvgpr_mov_b32 a42, a218\n"
"v_accvgpr_mov_b32 a43, a219\n"
"v_accvgpr_mov_b32 a44, a220\n"
"v_accvgpr_mov_b32 a45, a221\n"
"v_accvgpr_mov_b32 a46, a222\n"
"v_accvgpr_mov_b32 a47, a223\n"
"v_accvgpr_mov_b32 a48, a224\n"
"v_accvgpr_mov_b32 a49, a225\n"
"v_accvgpr_mov_b32 a50, a226\n"
"v_accvgpr_mov_b32 a51, a227\n"
"v_accvgpr_mov_b32 a52, a228\n"
"v_accvgpr_mov_b32 a53, a229\n"
"v_accvgpr_mov_b32 a54, a230\n"
"v_accvgpr_mov_b32 a55, a231\n"
"v_accvgpr_mov_b32 a56, a232\n"
"v_accvgpr_mov_b32 a57, a233\n"
"v_accvgpr_mov_b32 a58, a234\n"
"v_accvgpr_mov_b32 a59, a235\n"
"v_accvgpr_mov_b32 a60, a236\n"
"v_accvgpr_mov_b32 a61, a237\n"
"v_accvgpr_mov_b32 a62, a238\n"
"v_accvgpr_mov_b32 a63, a239\n"
"ds_read_b128 a[64:67], v7 offset:0\n"
"ds_read_b128 a[68:71], v7 offset:1024\n"
"ds_read_b128 a[72:75], v7 offset:2048\n"
"ds_read_b128 a[76:79], v7 offset:3072\n"
"ds_read_b128 a[80:83], v7 offset:4096\n"
"ds_read_b128 a[84:87], v7 offset:5120\n"
"ds_read_b128 a[88:91], v7 offset:6144\n"
"ds_read_b128 a[92:95], v7 offset:7168\n"
"ds_read_b128 a[96:99], v7 offset:8192\n"
"ds_read_b128 a[100:103], v7 offset:9216\n"
"ds_read_b128 a[104:107], v7 offset:10240\n"
"ds_read_b128 a[108:111], v7 offset:11264\n"
"s_waitcnt lgkmcnt(0) ;keep\n"
"s_branch LS6%=\n"
"LS5%=:\n"
"s_mov_b32 s67, s72\n"
"v_add_u32 v16, 0, v4\n"
"v_add_u32 v17, 6144, v4\n"
"v_add_u32 v18, 12288, v4\n"
"v_add_u32 v19, 18432, v4\n"
"v_add_u32 v20, 28672, v4\n"
"v_add_u32 v21, 34816, v4\n"
"s_mov_b32 s72, 0\n"
"s_mov_b32 s72, s67\n"
"LS6%=:\n"
"s_nop 7\n"
"v_accvgpr_read_b32 v12, a128\n"
"v_accvgpr_read_b32 v13, a132\n"
"s_cmp_eq_u32 s64, 0\n"
"s_cbranch_scc1 LSF%=\n"
"v_sub_f32 v248, 0, v8\n"
"v_sub_f32 v249, 0, v9\n"
"v_mov_b64 v[250:251], v[12:13]\n"
"buffer_store_dwordx4 v[248:251], v0, s[52:55], s88 offen sc0 sc1\n"
"v_cvt_pk_bf16_f32 v112, v112, v113\n"
"v_cvt_pk_bf16_f32 v113, v114, v115\n"
"v_cvt_pk_bf16_f32 v114, v120, v121\n"
"v_cvt_pk_bf16_f32 v115, v122, v123\n"
"buffer_store_dwordx4 v[112:115], v0, s[52:55], s84 offen offset:0 sc0 sc1\n"
"v_cvt_pk_bf16_f32 v116, v116, v117\n"
"v_cvt_pk_bf16_f32 v117, v118, v119\n"
"v_cvt_pk_bf16_f32 v118, v124, v125\n"
"v_cvt_pk_bf16_f32 v119, v126, v127\n"
"buffer_store_dwordx4 v[116:119], v0, s[52:55], s84 offen offset:1024 sc0 sc1\n"
"v_cvt_pk_bf16_f32 v128, v128, v129\n"
"v_cvt_pk_bf16_f32 v129, v130, v131\n"
"v_cvt_pk_bf16_f32 v130, v136, v137\n"
"v_cvt_pk_bf16_f32 v131, v138, v139\n"
"buffer_store_dwordx4 v[128:131], v0, s[52:55], s84 offen offset:2048 sc0 sc1\n"
"v_cvt_pk_bf16_f32 v132, v132, v133\n"
"v_cvt_pk_bf16_f32 v133, v134, v135\n"
"v_cvt_pk_bf16_f32 v134, v140, v141\n"
"v_cvt_pk_bf16_f32 v135, v142, v143\n"
"buffer_store_dwordx4 v[132:135], v0, s[52:55], s84 offen offset:3072 sc0 sc1\n"
"v_cvt_pk_bf16_f32 v144, v144, v145\n"
"v_cvt_pk_bf16_f32 v145, v146, v147\n"
"v_cvt_pk_bf16_f32 v146, v152, v153\n"
"v_cvt_pk_bf16_f32 v147, v154, v155\n"
"buffer_store_dwordx4 v[144:147], v0, s[52:55], s85 offen offset:0 sc0 sc1\n"
"v_cvt_pk_bf16_f32 v148, v148, v149\n"
"v_cvt_pk_bf16_f32 v149, v150, v151\n"
"v_cvt_pk_bf16_f32 v150, v156, v157\n"
"v_cvt_pk_bf16_f32 v151, v158, v159\n"
"buffer_store_dwordx4 v[148:151], v0, s[52:55], s85 offen offset:1024 sc0 sc1\n"
"v_cvt_pk_bf16_f32 v160, v160, v161\n"
"v_cvt_pk_bf16_f32 v161, v162, v163\n"
"v_cvt_pk_bf16_f32 v162, v168, v169\n"
"v_cvt_pk_bf16_f32 v163, v170, v171\n"
"buffer_store_dwordx4 v[160:163], v0, s[52:55], s85 offen offset:2048 sc0 sc1\n"
"v_cvt_pk_bf16_f32 v164, v164, v165\n"
"v_cvt_pk_bf16_f32 v165, v166, v167\n"
"v_cvt_pk_bf16_f32 v166, v172, v173\n"
"v_cvt_pk_bf16_f32 v167, v174, v175\n"
"buffer_store_dwordx4 v[164:167], v0, s[52:55], s85 offen offset:3072 sc0 sc1\n"
"v_cvt_pk_bf16_f32 v176, v176, v177\n"
"v_cvt_pk_bf16_f32 v177, v178, v179\n"
"v_cvt_pk_bf16_f32 v178, v184, v185\n"
"v_cvt_pk_bf16_f32 v179, v186, v187\n"
"buffer_store_dwordx4 v[176:179], v0, s[52:55], s86 offen offset:0 sc0 sc1\n"
"v_cvt_pk_bf16_f32 v180, v180, v181\n"
"v_cvt_pk_bf16_f32 v181, v182, v183\n"
"v_cvt_pk_bf16_f32 v182, v188, v189\n"
"v_cvt_pk_bf16_f32 v183, v190, v191\n"
"buffer_store_dwordx4 v[180:183], v0, s[52:55], s86 offen offset:1024 sc0 sc1\n"
"v_cvt_pk_bf16_f32 v192, v192, v193\n"
"v_cvt_pk_bf16_f32 v193, v194, v195\n"
"v_cvt_pk_bf16_f32 v194, v200, v201\n"
"v_cvt_pk_bf16_f32 v195, v202, v203\n"
"buffer_store_dwordx4 v[192:195], v0, s[52:55], s86 offen offset:2048 sc0 sc1\n"
"v_cvt_pk_bf16_f32 v196, v196, v197\n"
"v_cvt_pk_bf16_f32 v197, v198, v199\n"
"v_cvt_pk_bf16_f32 v198, v204, v205\n"
"v_cvt_pk_bf16_f32 v199, v206, v207\n"
"buffer_store_dwordx4 v[196:199], v0, s[52:55], s86 offen offset:3072 sc0 sc1\n"
"v_cvt_pk_bf16_f32 v208, v208, v209\n"
"v_cvt_pk_bf16_f32 v209, v210, v211\n"
"v_cvt_pk_bf16_f32 v210, v216, v217\n"
"v_cvt_pk_bf16_f32 v211, v218, v219\n"
"buffer_store_dwordx4 v[208:211], v0, s[52:55], s87 offen offset:0 sc0 sc1\n"
"v_cvt_pk_bf16_f32 v212, v212, v213\n"
"v_cvt_pk_bf16_f32 v213, v214, v215\n"
"v_cvt_pk_bf16_f32 v214, v220, v221\n"
"v_cvt_pk_bf16_f32 v215, v222, v223\n"
"buffer_store_dwordx4 v[212:215], v0, s[52:55], s87 offen offset:1024 sc0 sc1\n"
"v_cvt_pk_bf16_f32 v224, v224, v225\n"
"v_cvt_pk_bf16_f32 v225, v226, v227\n"
"v_cvt_pk_bf16_f32 v226, v232, v233\n"
"v_cvt_pk_bf16_f32 v227, v234, v235\n"
"buffer_store_dwordx4 v[224:227], v0, s[52:55], s87 offen offset:2048 sc0 sc1\n"
"v_cvt_pk_bf16_f32 v228, v228, v229\n"
"v_cvt_pk_bf16_f32 v229, v230, v231\n"
"v_cvt_pk_bf16_f32 v230, v236, v237\n"
"v_cvt_pk_bf16_f32 v231, v238, v239\n"
"buffer_store_dwordx4 v[228:231], v0, s[52:55], s87 offen offset:3072 sc0 sc1\n"
"v_mov_b64 v[16:17], 0\n"
"v_mov_b64 v[18:19], 0\n"
"v_mov_b64 v[20:21], 0\n"
"v_mov_b64 v[22:23], 0\n"
"v_mov_b64 v[24:25], 0\n"
"v_mov_b64 v[26:27], 0\n"
"v_mov_b64 v[28:29], 0\n"
"v_mov_b64 v[30:31], 0\n"
"v_mov_b64 v[32:33], 0\n"
"v_mov_b64 v[34:35], 0\n"
"v_mov_b64 v[36:37], 0\n"
"v_mov_b64 v[38:39], 0\n"
"v_mov_b64 v[40:41], 0\n"
"v_mov_b64 v[42:43], 0\n"
"v_mov_b64 v[44:45], 0\n"
"v_mov_b64 v[46:47], 0\n"
"s_branch LSN%=\n"
"LSF%=:\n"
"s_cmp_eq_u32 s68, 1\n"
"s_cbranch_scc1 LSG%=\n"
"buffer_load_dwordx4 v[248:251], v0, s[52:55], s88 offen sc0 sc1\n"
"buffer_load_dwordx4 v[48:51], v0, s[52:55], s84 offen offset:0 sc0 sc1\n"
"buffer_load_dwordx4 v[52:55], v0, s[52:55], s84 offen offset:1024 sc0 sc1\n"
"buffer_load_dwordx4 v[56:59], v0, s[52:55], s84 offen offset:2048 sc0 sc1\n"
"buffer_load_dwordx4 v[60:63], v0, s[52:55], s84 offen offset:3072 sc0 sc1\n"
"buffer_load_dwordx4 v[64:67], v0, s[52:55], s85 offen offset:0 sc0 sc1\n"
"buffer_load_dwordx4 v[68:71], v0, s[52:55], s85 offen offset:1024 sc0 sc1\n"
"buffer_load_dwordx4 v[72:75], v0, s[52:55], s85 offen offset:2048 sc0 sc1\n"
"buffer_load_dwordx4 v[76:79], v0, s[52:55], s85 offen offset:3072 sc0 sc1\n"
"buffer_load_dwordx4 v[80:83], v0, s[52:55], s86 offen offset:0 sc0 sc1\n"
"buffer_load_dwordx4 v[84:87], v0, s[52:55], s86 offen offset:1024 sc0 sc1\n"
"buffer_load_dwordx4 v[88:91], v0, s[52:55], s86 offen offset:2048 sc0 sc1\n"
"buffer_load_dwordx4 v[92:95], v0, s[52:55], s86 offen offset:3072 sc0 sc1\n"
"buffer_load_dwordx4 v[96:99], v0, s[52:55], s87 offen offset:0 sc0 sc1\n"
"buffer_load_dwordx4 v[100:103], v0, s[52:55], s87 offen offset:1024 sc0 sc1\n"
"buffer_load_dwordx4 v[104:107], v0, s[52:55], s87 offen offset:2048 sc0 sc1\n"
"buffer_load_dwordx4 v[108:111], v0, s[52:55], s87 offen offset:3072 sc0 sc1\n"
"s_waitcnt vmcnt(0) ;keep\n"
"v_cmp_lt_f32 vcc, 0, v250\n"
"v_mov_b32 v10, 0xe0ad78ec\n"
"s_nop 0\n"
"v_cndmask_b32 v248, v10, v248, vcc\n"
"v_cmp_lt_f32 vcc, 0, v251\n"
"v_mov_b32 v10, 0xe0ad78ec\n"
"s_nop 0\n"
"v_cndmask_b32 v249, v10, v249, vcc\n"
"v_max_f32 v10, v248, -v8\n"
"v_sub_f32 v240, -v8, v10\n"
"v_sub_f32 v244, v248, v10\n"
"v_exp_f32 v240, v240\n"
"v_exp_f32 v244, v244\n"
"s_nop 0\n"
"v_mul_f32 v10, v244, v250\n"
"v_fma_f32 v10, v12, v240, v10\n"
"v_rcp_f32 v10, v10\n"
"s_nop 0\n"
"v_mul_f32 v240, v240, v10\n"
"v_mul_f32 v244, v244, v10\n"
"v_max_f32 v11, v249, -v9\n"
"v_sub_f32 v242, -v9, v11\n"
"v_sub_f32 v246, v249, v11\n"
"v_exp_f32 v242, v242\n"
"v_exp_f32 v246, v246\n"
"s_nop 0\n"
"v_mul_f32 v11, v246, v251\n"
"v_fma_f32 v11, v13, v242, v11\n"
"v_rcp_f32 v11, v11\n"
"s_nop 0\n"
"v_mul_f32 v242, v242, v11\n"
"v_mul_f32 v246, v246, v11\n"
"v_lshlrev_b32 v248, 16, v48\n"
"v_and_b32 v249, 0xffff0000, v48\n"
"v_lshlrev_b32 v250, 16, v49\n"
"v_and_b32 v251, 0xffff0000, v49\n"
"v_lshlrev_b32 v252, 16, v50\n"
"v_and_b32 v253, 0xffff0000, v50\n"
"v_lshlrev_b32 v254, 16, v51\n"
"v_and_b32 v255, 0xffff0000, v51\n"
"v_pk_mul_f32 v[248:249], v[248:249], v[244:245] op_sel_hi:[1,0]\n"
"v_pk_mul_f32 v[250:251], v[250:251], v[244:245] op_sel_hi:[1,0]\n"
"v_pk_mul_f32 v[252:253], v[252:253], v[244:245] op_sel_hi:[1,0]\n"
"v_pk_mul_f32 v[254:255], v[254:255], v[244:245] op_sel_hi:[1,0]\n"
"v_pk_fma_f32 v[248:249], v[112:113], v[240:241], v[248:249] op_sel_hi:[1,0,1]\n"
"v_pk_fma_f32 v[250:251], v[114:115], v[240:241], v[250:251] op_sel_hi:[1,0,1]\n"
"v_pk_fma_f32 v[252:253], v[120:121], v[240:241], v[252:253] op_sel_hi:[1,0,1]\n"
"v_pk_fma_f32 v[254:255], v[122:123], v[240:241], v[254:255] op_sel_hi:[1,0,1]\n"
"v_cvt_pk_bf16_f32 v112, v248, v249\n"
"v_cvt_pk_bf16_f32 v113, v250, v251\n"
"v_cvt_pk_bf16_f32 v114, v252, v253\n"
"v_cvt_pk_bf16_f32 v115, v254, v255\n"
"v_lshlrev_b32 v248, 16, v52\n"
"v_and_b32 v249, 0xffff0000, v52\n"
"v_lshlrev_b32 v250, 16, v53\n"
"v_and_b32 v251, 0xffff0000, v53\n"
"v_lshlrev_b32 v252, 16, v54\n"
"v_and_b32 v253, 0xffff0000, v54\n"
"v_lshlrev_b32 v254, 16, v55\n"
"v_and_b32 v255, 0xffff0000, v55\n"
"v_pk_mul_f32 v[248:249], v[248:249], v[246:247] op_sel_hi:[1,0]\n"
"v_pk_mul_f32 v[250:251], v[250:251], v[246:247] op_sel_hi:[1,0]\n"
"v_pk_mul_f32 v[252:253], v[252:253], v[246:247] op_sel_hi:[1,0]\n"
"v_pk_mul_f32 v[254:255], v[254:255], v[246:247] op_sel_hi:[1,0]\n"
"v_pk_fma_f32 v[248:249], v[116:117], v[242:243], v[248:249] op_sel_hi:[1,0,1]\n"
"v_pk_fma_f32 v[250:251], v[118:119], v[242:243], v[250:251] op_sel_hi:[1,0,1]\n"
"v_pk_fma_f32 v[252:253], v[124:125], v[242:243], v[252:253] op_sel_hi:[1,0,1]\n"
"v_pk_fma_f32 v[254:255], v[126:127], v[242:243], v[254:255] op_sel_hi:[1,0,1]\n"
"v_cvt_pk_bf16_f32 v116, v248, v249\n"
"v_cvt_pk_bf16_f32 v117, v250, v251\n"
"v_cvt_pk_bf16_f32 v118, v252, v253\n"
"v_cvt_pk_bf16_f32 v119, v254, v255\n"
"v_lshlrev_b32 v248, 16, v56\n"
"v_and_b32 v249, 0xffff0000, v56\n"
"v_lshlrev_b32 v250, 16, v57\n"
"v_and_b32 v251, 0xffff0000, v57\n"
"v_lshlrev_b32 v252, 16, v58\n"
"v_and_b32 v253, 0xffff0000, v58\n"
"v_lshlrev_b32 v254, 16, v59\n"
"v_and_b32 v255, 0xffff0000, v59\n"
"v_pk_mul_f32 v[248:249], v[248:249], v[244:245] op_sel_hi:[1,0]\n"
"v_pk_mul_f32 v[250:251], v[250:251], v[244:245] op_sel_hi:[1,0]\n"
"v_pk_mul_f32 v[252:253], v[252:253], v[244:245] op_sel_hi:[1,0]\n"
"v_pk_mul_f32 v[254:255], v[254:255], v[244:245] op_sel_hi:[1,0]\n"
"v_pk_fma_f32 v[248:249], v[128:129], v[240:241], v[248:249] op_sel_hi:[1,0,1]\n"
"v_pk_fma_f32 v[250:251], v[130:131], v[240:241], v[250:251] op_sel_hi:[1,0,1]\n"
"v_pk_fma_f32 v[252:253], v[136:137], v[240:241], v[252:253] op_sel_hi:[1,0,1]\n"
"v_pk_fma_f32 v[254:255], v[138:139], v[240:241], v[254:255] op_sel_hi:[1,0,1]\n"
"v_cvt_pk_bf16_f32 v128, v248, v249\n"
"v_cvt_pk_bf16_f32 v129, v250, v251\n"
"v_cvt_pk_bf16_f32 v130, v252, v253\n"
"v_cvt_pk_bf16_f32 v131, v254, v255\n"
"v_lshlrev_b32 v248, 16, v60\n"
"v_and_b32 v249, 0xffff0000, v60\n"
"v_lshlrev_b32 v250, 16, v61\n"
"v_and_b32 v251, 0xffff0000, v61\n"
"v_lshlrev_b32 v252, 16, v62\n"
"v_and_b32 v253, 0xffff0000, v62\n"
"v_lshlrev_b32 v254, 16, v63\n"
"v_and_b32 v255, 0xffff0000, v63\n"
"v_pk_mul_f32 v[248:249], v[248:249], v[246:247] op_sel_hi:[1,0]\n"
"v_pk_mul_f32 v[250:251], v[250:251], v[246:247] op_sel_hi:[1,0]\n"
"v_pk_mul_f32 v[252:253], v[252:253], v[246:247] op_sel_hi:[1,0]\n"
"v_pk_mul_f32 v[254:255], v[254:255], v[246:247] op_sel_hi:[1,0]\n"
"v_pk_fma_f32 v[248:249], v[132:133], v[242:243], v[248:249] op_sel_hi:[1,0,1]\n"
"v_pk_fma_f32 v[250:251], v[134:135], v[242:243], v[250:251] op_sel_hi:[1,0,1]\n"
"v_pk_fma_f32 v[252:253], v[140:141], v[242:243], v[252:253] op_sel_hi:[1,0,1]\n"
"v_pk_fma_f32 v[254:255], v[142:143], v[242:243], v[254:255] op_sel_hi:[1,0,1]\n"
"v_cvt_pk_bf16_f32 v132, v248, v249\n"
"v_cvt_pk_bf16_f32 v133, v250, v251\n"
"v_cvt_pk_bf16_f32 v134, v252, v253\n"
"v_cvt_pk_bf16_f32 v135, v254, v255\n"
"v_lshlrev_b32 v248, 16, v64\n"
"v_and_b32 v249, 0xffff0000, v64\n"
"v_lshlrev_b32 v250, 16, v65\n"
"v_and_b32 v251, 0xffff0000, v65\n"
"v_lshlrev_b32 v252, 16, v66\n"
"v_and_b32 v253, 0xffff0000, v66\n"
"v_lshlrev_b32 v254, 16, v67\n"
"v_and_b32 v255, 0xffff0000, v67\n"
"v_pk_mul_f32 v[248:249], v[248:249], v[244:245] op_sel_hi:[1,0]\n"
"v_pk_mul_f32 v[250:251], v[250:251], v[244:245] op_sel_hi:[1,0]\n"
"v_pk_mul_f32 v[252:253], v[252:253], v[244:245] op_sel_hi:[1,0]\n"
"v_pk_mul_f32 v[254:255], v[254:255], v[244:245] op_sel_hi:[1,0]\n"
"v_pk_fma_f32 v[248:249], v[144:145], v[240:241], v[248:249] op_sel_hi:[1,0,1]\n"
"v_pk_fma_f32 v[250:251], v[146:147], v[240:241], v[250:251] op_sel_hi:[1,0,1]\n"
"v_pk_fma_f32 v[252:253], v[152:153], v[240:241], v[252:253] op_sel_hi:[1,0,1]\n"
"v_pk_fma_f32 v[254:255], v[154:155], v[240:241], v[254:255] op_sel_hi:[1,0,1]\n"
"v_cvt_pk_bf16_f32 v144, v248, v249\n"
"v_cvt_pk_bf16_f32 v145, v250, v251\n"
"v_cvt_pk_bf16_f32 v146, v252, v253\n"
"v_cvt_pk_bf16_f32 v147, v254, v255\n"
"v_lshlrev_b32 v248, 16, v68\n"
"v_and_b32 v249, 0xffff0000, v68\n"
"v_lshlrev_b32 v250, 16, v69\n"
"v_and_b32 v251, 0xffff0000, v69\n"
"v_lshlrev_b32 v252, 16, v70\n"
"v_and_b32 v253, 0xffff0000, v70\n"
"v_lshlrev_b32 v254, 16, v71\n"
"v_and_b32 v255, 0xffff0000, v71\n"
"v_pk_mul_f32 v[248:249], v[248:249], v[246:247] op_sel_hi:[1,0]\n"
"v_pk_mul_f32 v[250:251], v[250:251], v[246:247] op_sel_hi:[1,0]\n"
"v_pk_mul_f32 v[252:253], v[252:253], v[246:247] op_sel_hi:[1,0]\n"
"v_pk_mul_f32 v[254:255], v[254:255], v[246:247] op_sel_hi:[1,0]\n"
"v_pk_fma_f32 v[248:249], v[148:149], v[242:243], v[248:249] op_sel_hi:[1,0,1]\n"
"v_pk_fma_f32 v[250:251], v[150:151], v[242:243], v[250:251] op_sel_hi:[1,0,1]\n"
"v_pk_fma_f32 v[252:253], v[156:157], v[242:243], v[252:253] op_sel_hi:[1,0,1]\n"
"v_pk_fma_f32 v[254:255], v[158:159], v[242:243], v[254:255] op_sel_hi:[1,0,1]\n"
"v_cvt_pk_bf16_f32 v148, v248, v249\n"
"v_cvt_pk_bf16_f32 v149, v250, v251\n"
"v_cvt_pk_bf16_f32 v150, v252, v253\n"
"v_cvt_pk_bf16_f32 v151, v254, v255\n"
"v_lshlrev_b32 v248, 16, v72\n"
"v_and_b32 v249, 0xffff0000, v72\n"
"v_lshlrev_b32 v250, 16, v73\n"
"v_and_b32 v251, 0xffff0000, v73\n"
"v_lshlrev_b32 v252, 16, v74\n"
"v_and_b32 v253, 0xffff0000, v74\n"
"v_lshlrev_b32 v254, 16, v75\n"
"v_and_b32 v255, 0xffff0000, v75\n"
"v_pk_mul_f32 v[248:249], v[248:249], v[244:245] op_sel_hi:[1,0]\n"
"v_pk_mul_f32 v[250:251], v[250:251], v[244:245] op_sel_hi:[1,0]\n"
"v_pk_mul_f32 v[252:253], v[252:253], v[244:245] op_sel_hi:[1,0]\n"
"v_pk_mul_f32 v[254:255], v[254:255], v[244:245] op_sel_hi:[1,0]\n"
"v_pk_fma_f32 v[248:249], v[160:161], v[240:241], v[248:249] op_sel_hi:[1,0,1]\n"
"v_pk_fma_f32 v[250:251], v[162:163], v[240:241], v[250:251] op_sel_hi:[1,0,1]\n"
"v_pk_fma_f32 v[252:253], v[168:169], v[240:241], v[252:253] op_sel_hi:[1,0,1]\n"
"v_pk_fma_f32 v[254:255], v[170:171], v[240:241], v[254:255] op_sel_hi:[1,0,1]\n"
"v_cvt_pk_bf16_f32 v160, v248, v249\n"
"v_cvt_pk_bf16_f32 v161, v250, v251\n"
"v_cvt_pk_bf16_f32 v162, v252, v253\n"
"v_cvt_pk_bf16_f32 v163, v254, v255\n"
"v_lshlrev_b32 v248, 16, v76\n"
"v_and_b32 v249, 0xffff0000, v76\n"
"v_lshlrev_b32 v250, 16, v77\n"
"v_and_b32 v251, 0xffff0000, v77\n"
"v_lshlrev_b32 v252, 16, v78\n"
"v_and_b32 v253, 0xffff0000, v78\n"
"v_lshlrev_b32 v254, 16, v79\n"
"v_and_b32 v255, 0xffff0000, v79\n"
"v_pk_mul_f32 v[248:249], v[248:249], v[246:247] op_sel_hi:[1,0]\n"
"v_pk_mul_f32 v[250:251], v[250:251], v[246:247] op_sel_hi:[1,0]\n"
"v_pk_mul_f32 v[252:253], v[252:253], v[246:247] op_sel_hi:[1,0]\n"
"v_pk_mul_f32 v[254:255], v[254:255], v[246:247] op_sel_hi:[1,0]\n"
"v_pk_fma_f32 v[248:249], v[164:165], v[242:243], v[248:249] op_sel_hi:[1,0,1]\n"
"v_pk_fma_f32 v[250:251], v[166:167], v[242:243], v[250:251] op_sel_hi:[1,0,1]\n"
"v_pk_fma_f32 v[252:253], v[172:173], v[242:243], v[252:253] op_sel_hi:[1,0,1]\n"
"v_pk_fma_f32 v[254:255], v[174:175], v[242:243], v[254:255] op_sel_hi:[1,0,1]\n"
"v_cvt_pk_bf16_f32 v164, v248, v249\n"
"v_cvt_pk_bf16_f32 v165, v250, v251\n"
"v_cvt_pk_bf16_f32 v166, v252, v253\n"
"v_cvt_pk_bf16_f32 v167, v254, v255\n"
"v_lshlrev_b32 v248, 16, v80\n"
"v_and_b32 v249, 0xffff0000, v80\n"
"v_lshlrev_b32 v250, 16, v81\n"
"v_and_b32 v251, 0xffff0000, v81\n"
"v_lshlrev_b32 v252, 16, v82\n"
"v_and_b32 v253, 0xffff0000, v82\n"
"v_lshlrev_b32 v254, 16, v83\n"
"v_and_b32 v255, 0xffff0000, v83\n"
"v_pk_mul_f32 v[248:249], v[248:249], v[244:245] op_sel_hi:[1,0]\n"
"v_pk_mul_f32 v[250:251], v[250:251], v[244:245] op_sel_hi:[1,0]\n"
"v_pk_mul_f32 v[252:253], v[252:253], v[244:245] op_sel_hi:[1,0]\n"
"v_pk_mul_f32 v[254:255], v[254:255], v[244:245] op_sel_hi:[1,0]\n"
"v_pk_fma_f32 v[248:249], v[176:177], v[240:241], v[248:249] op_sel_hi:[1,0,1]\n"
"v_pk_fma_f32 v[250:251], v[178:179], v[240:241], v[250:251] op_sel_hi:[1,0,1]\n"
"v_pk_fma_f32 v[252:253], v[184:185], v[240:241], v[252:253] op_sel_hi:[1,0,1]\n"
"v_pk_fma_f32 v[254:255], v[186:187], v[240:241], v[254:255] op_sel_hi:[1,0,1]\n"
"v_cvt_pk_bf16_f32 v176, v248, v249\n"
"v_cvt_pk_bf16_f32 v177, v250, v251\n"
"v_cvt_pk_bf16_f32 v178, v252, v253\n"
"v_cvt_pk_bf16_f32 v179, v254, v255\n"
"v_lshlrev_b32 v248, 16, v84\n"
"v_and_b32 v249, 0xffff0000, v84\n"
"v_lshlrev_b32 v250, 16, v85\n"
"v_and_b32 v251, 0xffff0000, v85\n"
"v_lshlrev_b32 v252, 16, v86\n"
"v_and_b32 v253, 0xffff0000, v86\n"
"v_lshlrev_b32 v254, 16, v87\n"
"v_and_b32 v255, 0xffff0000, v87\n"
"v_pk_mul_f32 v[248:249], v[248:249], v[246:247] op_sel_hi:[1,0]\n"
"v_pk_mul_f32 v[250:251], v[250:251], v[246:247] op_sel_hi:[1,0]\n"
"v_pk_mul_f32 v[252:253], v[252:253], v[246:247] op_sel_hi:[1,0]\n"
"v_pk_mul_f32 v[254:255], v[254:255], v[246:247] op_sel_hi:[1,0]\n"
"v_pk_fma_f32 v[248:249], v[180:181], v[242:243], v[248:249] op_sel_hi:[1,0,1]\n"
"v_pk_fma_f32 v[250:251], v[182:183], v[242:243], v[250:251] op_sel_hi:[1,0,1]\n"
"v_pk_fma_f32 v[252:253], v[188:189], v[242:243], v[252:253] op_sel_hi:[1,0,1]\n"
"v_pk_fma_f32 v[254:255], v[190:191], v[242:243], v[254:255] op_sel_hi:[1,0,1]\n"
"v_cvt_pk_bf16_f32 v180, v248, v249\n"
"v_cvt_pk_bf16_f32 v181, v250, v251\n"
"v_cvt_pk_bf16_f32 v182, v252, v253\n"
"v_cvt_pk_bf16_f32 v183, v254, v255\n"
"v_lshlrev_b32 v248, 16, v88\n"
"v_and_b32 v249, 0xffff0000, v88\n"
"v_lshlrev_b32 v250, 16, v89\n"
"v_and_b32 v251, 0xffff0000, v89\n"
"v_lshlrev_b32 v252, 16, v90\n"
"v_and_b32 v253, 0xffff0000, v90\n"
"v_lshlrev_b32 v254, 16, v91\n"
"v_and_b32 v255, 0xffff0000, v91\n"
"v_pk_mul_f32 v[248:249], v[248:249], v[244:245] op_sel_hi:[1,0]\n"
"v_pk_mul_f32 v[250:251], v[250:251], v[244:245] op_sel_hi:[1,0]\n"
"v_pk_mul_f32 v[252:253], v[252:253], v[244:245] op_sel_hi:[1,0]\n"
"v_pk_mul_f32 v[254:255], v[254:255], v[244:245] op_sel_hi:[1,0]\n"
"v_pk_fma_f32 v[248:249], v[192:193], v[240:241], v[248:249] op_sel_hi:[1,0,1]\n"
"v_pk_fma_f32 v[250:251], v[194:195], v[240:241], v[250:251] op_sel_hi:[1,0,1]\n"
"v_pk_fma_f32 v[252:253], v[200:201], v[240:241], v[252:253] op_sel_hi:[1,0,1]\n"
"v_pk_fma_f32 v[254:255], v[202:203], v[240:241], v[254:255] op_sel_hi:[1,0,1]\n"
"v_cvt_pk_bf16_f32 v192, v248, v249\n"
"v_cvt_pk_bf16_f32 v193, v250, v251\n"
"v_cvt_pk_bf16_f32 v194, v252, v253\n"
"v_cvt_pk_bf16_f32 v195, v254, v255\n"
"v_lshlrev_b32 v248, 16, v92\n"
"v_and_b32 v249, 0xffff0000, v92\n"
"v_lshlrev_b32 v250, 16, v93\n"
"v_and_b32 v251, 0xffff0000, v93\n"
"v_lshlrev_b32 v252, 16, v94\n"
"v_and_b32 v253, 0xffff0000, v94\n"
"v_lshlrev_b32 v254, 16, v95\n"
"v_and_b32 v255, 0xffff0000, v95\n"
"v_pk_mul_f32 v[248:249], v[248:249], v[246:247] op_sel_hi:[1,0]\n"
"v_pk_mul_f32 v[250:251], v[250:251], v[246:247] op_sel_hi:[1,0]\n"
"v_pk_mul_f32 v[252:253], v[252:253], v[246:247] op_sel_hi:[1,0]\n"
"v_pk_mul_f32 v[254:255], v[254:255], v[246:247] op_sel_hi:[1,0]\n"
"v_pk_fma_f32 v[248:249], v[196:197], v[242:243], v[248:249] op_sel_hi:[1,0,1]\n"
"v_pk_fma_f32 v[250:251], v[198:199], v[242:243], v[250:251] op_sel_hi:[1,0,1]\n"
"v_pk_fma_f32 v[252:253], v[204:205], v[242:243], v[252:253] op_sel_hi:[1,0,1]\n"
"v_pk_fma_f32 v[254:255], v[206:207], v[242:243], v[254:255] op_sel_hi:[1,0,1]\n"
"v_cvt_pk_bf16_f32 v196, v248, v249\n"
"v_cvt_pk_bf16_f32 v197, v250, v251\n"
"v_cvt_pk_bf16_f32 v198, v252, v253\n"
"v_cvt_pk_bf16_f32 v199, v254, v255\n"
"v_lshlrev_b32 v248, 16, v96\n"
"v_and_b32 v249, 0xffff0000, v96\n"
"v_lshlrev_b32 v250, 16, v97\n"
"v_and_b32 v251, 0xffff0000, v97\n"
"v_lshlrev_b32 v252, 16, v98\n"
"v_and_b32 v253, 0xffff0000, v98\n"
"v_lshlrev_b32 v254, 16, v99\n"
"v_and_b32 v255, 0xffff0000, v99\n"
"v_pk_mul_f32 v[248:249], v[248:249], v[244:245] op_sel_hi:[1,0]\n"
"v_pk_mul_f32 v[250:251], v[250:251], v[244:245] op_sel_hi:[1,0]\n"
"v_pk_mul_f32 v[252:253], v[252:253], v[244:245] op_sel_hi:[1,0]\n"
"v_pk_mul_f32 v[254:255], v[254:255], v[244:245] op_sel_hi:[1,0]\n"
"v_pk_fma_f32 v[248:249], v[208:209], v[240:241], v[248:249] op_sel_hi:[1,0,1]\n"
"v_pk_fma_f32 v[250:251], v[210:211], v[240:241], v[250:251] op_sel_hi:[1,0,1]\n"
"v_pk_fma_f32 v[252:253], v[216:217], v[240:241], v[252:253] op_sel_hi:[1,0,1]\n"
"v_pk_fma_f32 v[254:255], v[218:219], v[240:241], v[254:255] op_sel_hi:[1,0,1]\n"
"v_cvt_pk_bf16_f32 v208, v248, v249\n"
"v_cvt_pk_bf16_f32 v209, v250, v251\n"
"v_cvt_pk_bf16_f32 v210, v252, v253\n"
"v_cvt_pk_bf16_f32 v211, v254, v255\n"
"v_lshlrev_b32 v248, 16, v100\n"
"v_and_b32 v249, 0xffff0000, v100\n"
"v_lshlrev_b32 v250, 16, v101\n"
"v_and_b32 v251, 0xffff0000, v101\n"
"v_lshlrev_b32 v252, 16, v102\n"
"v_and_b32 v253, 0xffff0000, v102\n"
"v_lshlrev_b32 v254, 16, v103\n"
"v_and_b32 v255, 0xffff0000, v103\n"
"v_pk_mul_f32 v[248:249], v[248:249], v[246:247] op_sel_hi:[1,0]\n"
"v_pk_mul_f32 v[250:251], v[250:251], v[246:247] op_sel_hi:[1,0]\n"
"v_pk_mul_f32 v[252:253], v[252:253], v[246:247] op_sel_hi:[1,0]\n"
"v_pk_mul_f32 v[254:255], v[254:255], v[246:247] op_sel_hi:[1,0]\n"
"v_pk_fma_f32 v[248:249], v[212:213], v[242:243], v[248:249] op_sel_hi:[1,0,1]\n"
"v_pk_fma_f32 v[250:251], v[214:215], v[242:243], v[250:251] op_sel_hi:[1,0,1]\n"
"v_pk_fma_f32 v[252:253], v[220:221], v[242:243], v[252:253] op_sel_hi:[1,0,1]\n"
"v_pk_fma_f32 v[254:255], v[222:223], v[242:243], v[254:255] op_sel_hi:[1,0,1]\n"
"v_cvt_pk_bf16_f32 v212, v248, v249\n"
"v_cvt_pk_bf16_f32 v213, v250, v251\n"
"v_cvt_pk_bf16_f32 v214, v252, v253\n"
"v_cvt_pk_bf16_f32 v215, v254, v255\n"
"v_lshlrev_b32 v248, 16, v104\n"
"v_and_b32 v249, 0xffff0000, v104\n"
"v_lshlrev_b32 v250, 16, v105\n"
"v_and_b32 v251, 0xffff0000, v105\n"
"v_lshlrev_b32 v252, 16, v106\n"
"v_and_b32 v253, 0xffff0000, v106\n"
"v_lshlrev_b32 v254, 16, v107\n"
"v_and_b32 v255, 0xffff0000, v107\n"
"v_pk_mul_f32 v[248:249], v[248:249], v[244:245] op_sel_hi:[1,0]\n"
"v_pk_mul_f32 v[250:251], v[250:251], v[244:245] op_sel_hi:[1,0]\n"
"v_pk_mul_f32 v[252:253], v[252:253], v[244:245] op_sel_hi:[1,0]\n"
"v_pk_mul_f32 v[254:255], v[254:255], v[244:245] op_sel_hi:[1,0]\n"
"v_pk_fma_f32 v[248:249], v[224:225], v[240:241], v[248:249] op_sel_hi:[1,0,1]\n"
"v_pk_fma_f32 v[250:251], v[226:227], v[240:241], v[250:251] op_sel_hi:[1,0,1]\n"
"v_pk_fma_f32 v[252:253], v[232:233], v[240:241], v[252:253] op_sel_hi:[1,0,1]\n"
"v_pk_fma_f32 v[254:255], v[234:235], v[240:241], v[254:255] op_sel_hi:[1,0,1]\n"
"v_cvt_pk_bf16_f32 v224, v248, v249\n"
"v_cvt_pk_bf16_f32 v225, v250, v251\n"
"v_cvt_pk_bf16_f32 v226, v252, v253\n"
"v_cvt_pk_bf16_f32 v227, v254, v255\n"
"v_lshlrev_b32 v248, 16, v108\n"
"v_and_b32 v249, 0xffff0000, v108\n"
"v_lshlrev_b32 v250, 16, v109\n"
"v_and_b32 v251, 0xffff0000, v109\n"
"v_lshlrev_b32 v252, 16, v110\n"
"v_and_b32 v253, 0xffff0000, v110\n"
"v_lshlrev_b32 v254, 16, v111\n"
"v_and_b32 v255, 0xffff0000, v111\n"
"v_pk_mul_f32 v[248:249], v[248:249], v[246:247] op_sel_hi:[1,0]\n"
"v_pk_mul_f32 v[250:251], v[250:251], v[246:247] op_sel_hi:[1,0]\n"
"v_pk_mul_f32 v[252:253], v[252:253], v[246:247] op_sel_hi:[1,0]\n"
"v_pk_mul_f32 v[254:255], v[254:255], v[246:247] op_sel_hi:[1,0]\n"
"v_pk_fma_f32 v[248:249], v[228:229], v[242:243], v[248:249] op_sel_hi:[1,0,1]\n"
"v_pk_fma_f32 v[250:251], v[230:231], v[242:243], v[250:251] op_sel_hi:[1,0,1]\n"
"v_pk_fma_f32 v[252:253], v[236:237], v[242:243], v[252:253] op_sel_hi:[1,0,1]\n"
"v_pk_fma_f32 v[254:255], v[238:239], v[242:243], v[254:255] op_sel_hi:[1,0,1]\n"
"v_cvt_pk_bf16_f32 v228, v248, v249\n"
"v_cvt_pk_bf16_f32 v229, v250, v251\n"
"v_cvt_pk_bf16_f32 v230, v252, v253\n"
"v_cvt_pk_bf16_f32 v231, v254, v255\n"
"s_branch LEP%=\n"
"LSG%=:\n"
"v_max_f32 v242, 0x0da24260, v12\n"
"v_max_f32 v244, 0x0da24260, v13\n"
"v_rcp_f32 v242, v242\n"
"v_rcp_f32 v244, v244\n"
"s_nop 0\n"
"v_pk_mul_f32 v[112:113], v[112:113], v[242:243] op_sel_hi:[1,0]\n"
"v_pk_mul_f32 v[114:115], v[114:115], v[242:243] op_sel_hi:[1,0]\n"
"v_pk_mul_f32 v[120:121], v[120:121], v[242:243] op_sel_hi:[1,0]\n"
"v_pk_mul_f32 v[122:123], v[122:123], v[242:243] op_sel_hi:[1,0]\n"
"v_cvt_pk_bf16_f32 v112, v112, v113\n"
"v_cvt_pk_bf16_f32 v113, v114, v115\n"
"v_cvt_pk_bf16_f32 v114, v120, v121\n"
"v_cvt_pk_bf16_f32 v115, v122, v123\n"
"v_pk_mul_f32 v[116:117], v[116:117], v[244:245] op_sel_hi:[1,0]\n"
"v_pk_mul_f32 v[118:119], v[118:119], v[244:245] op_sel_hi:[1,0]\n"
"v_pk_mul_f32 v[124:125], v[124:125], v[244:245] op_sel_hi:[1,0]\n"
"v_pk_mul_f32 v[126:127], v[126:127], v[244:245] op_sel_hi:[1,0]\n"
"v_cvt_pk_bf16_f32 v116, v116, v117\n"
"v_cvt_pk_bf16_f32 v117, v118, v119\n"
"v_cvt_pk_bf16_f32 v118, v124, v125\n"
"v_cvt_pk_bf16_f32 v119, v126, v127\n"
"v_pk_mul_f32 v[128:129], v[128:129], v[242:243] op_sel_hi:[1,0]\n"
"v_pk_mul_f32 v[130:131], v[130:131], v[242:243] op_sel_hi:[1,0]\n"
"v_pk_mul_f32 v[136:137], v[136:137], v[242:243] op_sel_hi:[1,0]\n"
"v_pk_mul_f32 v[138:139], v[138:139], v[242:243] op_sel_hi:[1,0]\n"
"v_cvt_pk_bf16_f32 v128, v128, v129\n"
"v_cvt_pk_bf16_f32 v129, v130, v131\n"
"v_cvt_pk_bf16_f32 v130, v136, v137\n"
"v_cvt_pk_bf16_f32 v131, v138, v139\n"
"v_pk_mul_f32 v[132:133], v[132:133], v[244:245] op_sel_hi:[1,0]\n"
"v_pk_mul_f32 v[134:135], v[134:135], v[244:245] op_sel_hi:[1,0]\n"
"v_pk_mul_f32 v[140:141], v[140:141], v[244:245] op_sel_hi:[1,0]\n"
"v_pk_mul_f32 v[142:143], v[142:143], v[244:245] op_sel_hi:[1,0]\n"
"v_cvt_pk_bf16_f32 v132, v132, v133\n"
"v_cvt_pk_bf16_f32 v133, v134, v135\n"
"v_cvt_pk_bf16_f32 v134, v140, v141\n"
"v_cvt_pk_bf16_f32 v135, v142, v143\n"
"v_pk_mul_f32 v[144:145], v[144:145], v[242:243] op_sel_hi:[1,0]\n"
"v_pk_mul_f32 v[146:147], v[146:147], v[242:243] op_sel_hi:[1,0]\n"
"v_pk_mul_f32 v[152:153], v[152:153], v[242:243] op_sel_hi:[1,0]\n"
"v_pk_mul_f32 v[154:155], v[154:155], v[242:243] op_sel_hi:[1,0]\n"
"v_cvt_pk_bf16_f32 v144, v144, v145\n"
"v_cvt_pk_bf16_f32 v145, v146, v147\n"
"v_cvt_pk_bf16_f32 v146, v152, v153\n"
"v_cvt_pk_bf16_f32 v147, v154, v155\n"
"v_pk_mul_f32 v[148:149], v[148:149], v[244:245] op_sel_hi:[1,0]\n"
"v_pk_mul_f32 v[150:151], v[150:151], v[244:245] op_sel_hi:[1,0]\n"
"v_pk_mul_f32 v[156:157], v[156:157], v[244:245] op_sel_hi:[1,0]\n"
"v_pk_mul_f32 v[158:159], v[158:159], v[244:245] op_sel_hi:[1,0]\n"
"v_cvt_pk_bf16_f32 v148, v148, v149\n"
"v_cvt_pk_bf16_f32 v149, v150, v151\n"
"v_cvt_pk_bf16_f32 v150, v156, v157\n"
"v_cvt_pk_bf16_f32 v151, v158, v159\n"
"v_pk_mul_f32 v[160:161], v[160:161], v[242:243] op_sel_hi:[1,0]\n"
"v_pk_mul_f32 v[162:163], v[162:163], v[242:243] op_sel_hi:[1,0]\n"
"v_pk_mul_f32 v[168:169], v[168:169], v[242:243] op_sel_hi:[1,0]\n"
"v_pk_mul_f32 v[170:171], v[170:171], v[242:243] op_sel_hi:[1,0]\n"
"v_cvt_pk_bf16_f32 v160, v160, v161\n"
"v_cvt_pk_bf16_f32 v161, v162, v163\n"
"v_cvt_pk_bf16_f32 v162, v168, v169\n"
"v_cvt_pk_bf16_f32 v163, v170, v171\n"
"v_pk_mul_f32 v[164:165], v[164:165], v[244:245] op_sel_hi:[1,0]\n"
"v_pk_mul_f32 v[166:167], v[166:167], v[244:245] op_sel_hi:[1,0]\n"
"v_pk_mul_f32 v[172:173], v[172:173], v[244:245] op_sel_hi:[1,0]\n"
"v_pk_mul_f32 v[174:175], v[174:175], v[244:245] op_sel_hi:[1,0]\n"
"v_cvt_pk_bf16_f32 v164, v164, v165\n"
"v_cvt_pk_bf16_f32 v165, v166, v167\n"
"v_cvt_pk_bf16_f32 v166, v172, v173\n"
"v_cvt_pk_bf16_f32 v167, v174, v175\n"
"v_pk_mul_f32 v[176:177], v[176:177], v[242:243] op_sel_hi:[1,0]\n"
"v_pk_mul_f32 v[178:179], v[178:179], v[242:243] op_sel_hi:[1,0]\n"
"v_pk_mul_f32 v[184:185], v[184:185], v[242:243] op_sel_hi:[1,0]\n"
"v_pk_mul_f32 v[186:187], v[186:187], v[242:243] op_sel_hi:[1,0]\n"
"v_cvt_pk_bf16_f32 v176, v176, v177\n"
"v_cvt_pk_bf16_f32 v177, v178, v179\n"
"v_cvt_pk_bf16_f32 v178, v184, v185\n"
"v_cvt_pk_bf16_f32 v179, v186, v187\n"
"v_pk_mul_f32 v[180:181], v[180:181], v[244:245] op_sel_hi:[1,0]\n"
"v_pk_mul_f32 v[182:183], v[182:183], v[244:245] op_sel_hi:[1,0]\n"
"v_pk_mul_f32 v[188:189], v[188:189], v[244:245] op_sel_hi:[1,0]\n"
"v_pk_mul_f32 v[190:191], v[190:191], v[244:245] op_sel_hi:[1,0]\n"
"v_cvt_pk_bf16_f32 v180, v180, v181\n"
"v_cvt_pk_bf16_f32 v181, v182, v183\n"
"v_cvt_pk_bf16_f32 v182, v188, v189\n"
"v_cvt_pk_bf16_f32 v183, v190, v191\n"
"v_pk_mul_f32 v[192:193], v[192:193], v[242:243] op_sel_hi:[1,0]\n"
"v_pk_mul_f32 v[194:195], v[194:195], v[242:243] op_sel_hi:[1,0]\n"
"v_pk_mul_f32 v[200:201], v[200:201], v[242:243] op_sel_hi:[1,0]\n"
"v_pk_mul_f32 v[202:203], v[202:203], v[242:243] op_sel_hi:[1,0]\n"
"v_cvt_pk_bf16_f32 v192, v192, v193\n"
"v_cvt_pk_bf16_f32 v193, v194, v195\n"
"v_cvt_pk_bf16_f32 v194, v200, v201\n"
"v_cvt_pk_bf16_f32 v195, v202, v203\n"
"v_pk_mul_f32 v[196:197], v[196:197], v[244:245] op_sel_hi:[1,0]\n"
"v_pk_mul_f32 v[198:199], v[198:199], v[244:245] op_sel_hi:[1,0]\n"
"v_pk_mul_f32 v[204:205], v[204:205], v[244:245] op_sel_hi:[1,0]\n"
"v_pk_mul_f32 v[206:207], v[206:207], v[244:245] op_sel_hi:[1,0]\n"
"v_cvt_pk_bf16_f32 v196, v196, v197\n"
"v_cvt_pk_bf16_f32 v197, v198, v199\n"
"v_cvt_pk_bf16_f32 v198, v204, v205\n"
"v_cvt_pk_bf16_f32 v199, v206, v207\n"
"v_pk_mul_f32 v[208:209], v[208:209], v[242:243] op_sel_hi:[1,0]\n"
"v_pk_mul_f32 v[210:211], v[210:211], v[242:243] op_sel_hi:[1,0]\n"
"v_pk_mul_f32 v[216:217], v[216:217], v[242:243] op_sel_hi:[1,0]\n"
"v_pk_mul_f32 v[218:219], v[218:219], v[242:243] op_sel_hi:[1,0]\n"
"v_cvt_pk_bf16_f32 v208, v208, v209\n"
"v_cvt_pk_bf16_f32 v209, v210, v211\n"
"v_cvt_pk_bf16_f32 v210, v216, v217\n"
"v_cvt_pk_bf16_f32 v211, v218, v219\n"
"v_pk_mul_f32 v[212:213], v[212:213], v[244:245] op_sel_hi:[1,0]\n"
"v_pk_mul_f32 v[214:215], v[214:215], v[244:245] op_sel_hi:[1,0]\n"
"v_pk_mul_f32 v[220:221], v[220:221], v[244:245] op_sel_hi:[1,0]\n"
"v_pk_mul_f32 v[222:223], v[222:223], v[244:245] op_sel_hi:[1,0]\n"
"v_cvt_pk_bf16_f32 v212, v212, v213\n"
"v_cvt_pk_bf16_f32 v213, v214, v215\n"
"v_cvt_pk_bf16_f32 v214, v220, v221\n"
"v_cvt_pk_bf16_f32 v215, v222, v223\n"
"v_pk_mul_f32 v[224:225], v[224:225], v[242:243] op_sel_hi:[1,0]\n"
"v_pk_mul_f32 v[226:227], v[226:227], v[242:243] op_sel_hi:[1,0]\n"
"v_pk_mul_f32 v[232:233], v[232:233], v[242:243] op_sel_hi:[1,0]\n"
"v_pk_mul_f32 v[234:235], v[234:235], v[242:243] op_sel_hi:[1,0]\n"
"v_cvt_pk_bf16_f32 v224, v224, v225\n"
"v_cvt_pk_bf16_f32 v225, v226, v227\n"
"v_cvt_pk_bf16_f32 v226, v232, v233\n"
"v_cvt_pk_bf16_f32 v227, v234, v235\n"
"v_pk_mul_f32 v[228:229], v[228:229], v[244:245] op_sel_hi:[1,0]\n"
"v_pk_mul_f32 v[230:231], v[230:231], v[244:245] op_sel_hi:[1,0]\n"
"v_pk_mul_f32 v[236:237], v[236:237], v[244:245] op_sel_hi:[1,0]\n"
"v_pk_mul_f32 v[238:239], v[238:239], v[244:245] op_sel_hi:[1,0]\n"
"v_cvt_pk_bf16_f32 v228, v228, v229\n"
"v_cvt_pk_bf16_f32 v229, v230, v231\n"
"v_cvt_pk_bf16_f32 v230, v236, v237\n"
"v_cvt_pk_bf16_f32 v231, v238, v239\n"
"s_branch LEP%=\n"
"LF1%=:\n"
"s_waitcnt vmcnt(6)\n"
"v_cmp_ne_u32 vcc, 1, v247\n"
"s_nop 4\n"
"s_cbranch_vccz LBF1%=\n"
"s_mov_b32 s73, 0x40000\n"
"LFSA%=:\n"
"buffer_load_dword v247, v246, s[52:55], 0 offen sc0 sc1\n"
"s_waitcnt vmcnt(0) ;keep\n"
"v_cmp_ne_u32 vcc, 1, v247\n"
"s_nop 4\n"
"s_cbranch_vccz LBF1%=\n"
"s_sleep 2\n"
"s_sub_u32 s73, s73, 1\n"
"s_cbranch_scc0 LFSA%=\n"
"LBF1%=:\n"
"v_mfma_f32_16x16x32_bf16 a[128:131], a[136:139], v[80:83], a[128:131]\n"
"ds_read_b128 a[112:115], v7 offset:12288\n"
"v_accvgpr_mov_b32 a140, a176\n"
"v_accvgpr_mov_b32 a141, a177\n"
"v_accvgpr_mov_b32 a142, a178\n"
"v_accvgpr_mov_b32 a143, a179\n"
"v_mfma_f32_16x16x32_bf16 a[128:131], a[136:139], v[84:87], a[128:131]\n"
"buffer_load_dwordx4 v[248:251], v0, s[52:55], s88 offen sc0 sc1\n"
"s_add_u32 s59, s59, 32768\n"
"s_cmp_eq_u32 s59, 163840\n"
"v_mfma_f32_16x16x32_bf16 a[132:135], a[136:139], v[88:91], a[132:135]\n"
"ds_read_b128 a[116:119], v7 offset:13312\n"
"v_accvgpr_mov_b32 a144, a180\n"
"v_accvgpr_mov_b32 a145, a181\n"
"v_accvgpr_mov_b32 a146, a182\n"
"v_accvgpr_mov_b32 a147, a183\n"
"v_mfma_f32_16x16x32_bf16 a[132:135], a[136:139], v[92:95], a[132:135]\n"
"buffer_load_dwordx4 a[176:179], v0, s[52:55], s84 offen offset:0 sc0 sc1\n"
"s_cselect_b32 s59, 0, s59\n"
"s_mov_b32 s60, s59\n"
"v_mfma_f32_16x16x32_bf16 v[112:115], a[64:67], v[80:83], v[112:115]\n"
"ds_read_b128 a[120:123], v7 offset:14336\n"
"v_accvgpr_mov_b32 a148, a184\n"
"v_accvgpr_mov_b32 a149, a185\n"
"v_accvgpr_mov_b32 a150, a186\n"
"v_accvgpr_mov_b32 a151, a187\n"
"v_mfma_f32_16x16x32_bf16 v[116:119], a[64:67], v[88:91], v[116:119]\n"
"buffer_load_dwordx4 a[180:183], v0, s[52:55], s84 offen offset:1024 sc0 sc1\n"
"v_add_u32 v16, 0, v4\n"
"v_add_u32 v17, 6144, v4\n"
"v_mfma_f32_16x16x32_bf16 v[112:115], a[68:71], v[84:87], v[112:115]\n"
"ds_read_b128 a[124:127], v7 offset:15360\n"
"s_waitcnt lgkmcnt(8)\n"
"v_accvgpr_mov_b32 a152, a188\n"
"v_accvgpr_mov_b32 a153, a189\n"
"v_accvgpr_mov_b32 a154, a190\n"
"v_accvgpr_mov_b32 a155, a191\n"
"v_mfma_f32_16x16x32_bf16 v[116:119], a[68:71], v[92:95], v[116:119]\n"
"buffer_load_dwordx4 a[184:187], v0, s[52:55], s84 offen offset:2048 sc0 sc1\n"
"v_add_u32 v18, 12288, v4\n"
"v_add_u32 v19, 18432, v4\n"
"v_mfma_f32_16x16x32_bf16 v[120:123], a[72:75], v[80:83], v[120:123]\n"
"ds_read_b128 a[64:67], v7 offset:16384\n"
"v_accvgpr_mov_b32 a156, a192\n"
"v_accvgpr_mov_b32 a157, a193\n"
"v_accvgpr_mov_b32 a158, a194\n"
"v_accvgpr_mov_b32 a159, a195\n"
"v_mfma_f32_16x16x32_bf16 v[124:127], a[72:75], v[88:91], v[124:127]\n"
"buffer_load_dwordx4 a[188:191], v0, s[52:55], s84 offen offset:3072 sc0 sc1\n"
"v_add_u32 v20, 28672, v4\n"
"v_add_u32 v21, 34816, v4\n"
"v_mfma_f32_16x16x32_bf16 v[120:123], a[76:79], v[84:87], v[120:123]\n"
"ds_read_b128 a[68:71], v7 offset:17408\n"
"v_accvgpr_mov_b32 a160, a196\n"
"v_accvgpr_mov_b32 a161, a197\n"
"v_accvgpr_mov_b32 a162, a198\n"
"v_accvgpr_mov_b32 a163, a199\n"
"v_mfma_f32_16x16x32_bf16 v[124:127], a[76:79], v[92:95], v[124:127]\n"
"buffer_load_dwordx4 a[192:195], v0, s[52:55], s85 offen offset:0 sc0 sc1\n"
"s_mov_b32 s72, 0\n"
"v_accvgpr_read_b32 v12, a128\n"
"v_mfma_f32_16x16x32_bf16 v[128:131], a[80:83], v[80:83], v[128:131]\n"
"ds_read_b128 a[72:75], v7 offset:18432\n"
"v_accvgpr_mov_b32 a164, a200\n"
"v_accvgpr_mov_b32 a165, a201\n"
"v_accvgpr_mov_b32 a166, a202\n"
"v_accvgpr_mov_b32 a167, a203\n"
"v_mfma_f32_16x16x32_bf16 v[132:135], a[80:83], v[88:91], v[132:135]\n"
"buffer_load_dwordx4 a[196:199], v0, s[52:55], s85 offen offset:1024 sc0 sc1\n"
"v_accvgpr_read_b32 v13, a132\n"
"v_mfma_f32_16x16x32_bf16 v[128:131], a[84:87], v[84:87], v[128:131]\n"
"ds_read_b128 a[76:79], v7 offset:19456\n"
"s_waitcnt lgkmcnt(8)\n"
"v_accvgpr_mov_b32 a168, a204\n"
"v_accvgpr_mov_b32 a169, a205\n"
"v_accvgpr_mov_b32 a170, a206\n"
"v_accvgpr_mov_b32 a171, a207\n"
"v_mfma_f32_16x16x32_bf16 v[132:135], a[84:87], v[92:95], v[132:135]\n"
"buffer_load_dwordx4 a[200:203], v0, s[52:55], s85 offen offset:2048 sc0 sc1\n"
"v_add_f32 v14, s79, v12\n"
"v_mfma_f32_16x16x32_bf16 v[136:139], a[88:91], v[80:83], v[136:139]\n"
"ds_read_b128 a[80:83], v7 offset:20480\n"
"v_add_f32 v15, v12, v13\n"
"v_mfma_f32_16x16x32_bf16 v[140:143], a[88:91], v[88:91], v[140:143]\n"
"buffer_load_dwordx4 a[204:207], v0, s[52:55], s85 offen offset:3072 sc0 sc1\n"
"v_min_f32 v14, v14, v13\n"
"v_mfma_f32_16x16x32_bf16 v[136:139], a[92:95], v[84:87], v[136:139]\n"
"ds_read_b128 a[84:87], v7 offset:21504\n"
"v_cmp_nle_f32 s[80:81], v15, v240\n"
"v_mfma_f32_16x16x32_bf16 v[140:143], a[92:95], v[92:95], v[140:143]\n"
"buffer_load_dwordx4 a[208:211], v0, s[52:55], s86 offen offset:0 sc0 sc1\n"
"v_cmp_lt_f32 vcc, v14, v241\n"
"v_mfma_f32_16x16x32_bf16 v[144:147], a[96:99], v[80:83], v[144:147]\n"
"ds_read_b128 a[88:91], v7 offset:22528\n"
"s_or_b64 s[80:81], s[80:81], vcc\n"
"v_mfma_f32_16x16x32_bf16 v[148:151], a[96:99], v[88:91], v[148:151]\n"
"buffer_load_dwordx4 a[212:215], v0, s[52:55], s86 offen offset:1024 sc0 sc1\n"
"v_mfma_f32_16x16x32_bf16 v[144:147], a[100:103], v[84:87], v[144:147]\n"
"ds_read_b128 a[92:95], v7 offset:23552\n"
"s_waitcnt lgkmcnt(8)\n"
"v_mfma_f32_16x16x32_bf16 v[148:151], a[100:103], v[92:95], v[148:151]\n"
"buffer_load_dwordx4 a[216:219], v0, s[52:55], s86 offen offset:2048 sc0 sc1\n"
"v_mfma_f32_16x16x32_bf16 v[152:155], a[104:107], v[80:83], v[152:155]\n"
"ds_read_b128 a[96:99], v7 offset:24576\n"
"v_mfma_f32_16x16x32_bf16 v[156:159], a[104:107], v[88:91], v[156:159]\n"
"buffer_load_dwordx4 a[220:223], v0, s[52:55], s86 offen offset:3072 sc0 sc1\n"
"v_mfma_f32_16x16x32_bf16 v[152:155], a[108:111], v[84:87], v[152:155]\n"
"ds_read_b128 a[100:103], v7 offset:25600\n"
"v_mfma_f32_16x16x32_bf16 v[156:159], a[108:111], v[92:95], v[156:159]\n"
"buffer_load_dwordx4 a[224:227], v0, s[52:55], s87 offen offset:0 sc0 sc1\n"
"v_mfma_f32_16x16x32_bf16 v[160:163], a[112:115], v[80:83], v[160:163]\n"
"ds_read_b128 a[104:107], v7 offset:26624\n"
"v_mfma_f32_16x16x32_bf16 v[164:167], a[112:115], v[88:91], v[164:167]\n"
"buffer_load_dwordx4 a[228:231], v0, s[52:55], s87 offen offset:1024 sc0 sc1\n"
"v_mfma_f32_16x16x32_bf16 v[160:163], a[116:119], v[84:87], v[160:163]\n"
"ds_read_b128 a[108:111], v7 offset:27648\n"
"s_waitcnt lgkmcnt(8)\n"
"v_mfma_f32_16x16x32_bf16 v[164:167], a[116:119], v[92:95], v[164:167]\n"
"buffer_load_dwordx4 a[232:235], v0, s[52:55], s87 offen offset:2048 sc0 sc1\n"
"v_mfma_f32_16x16x32_bf16 v[168:171], a[120:123], v[80:83], v[168:171]\n"
"ds_read_b128 a[112:115], v7 offset:28672\n"
"v_mfma_f32_16x16x32_bf16 v[172:175], a[120:123], v[88:91], v[172:175]\n"
"buffer_load_dwordx4 a[236:239], v0, s[52:55], s87 offen offset:3072 sc0 sc1\n"
"v_mfma_f32_16x16x32_bf16 v[168:171], a[124:127], v[84:87], v[168:171]\n"
"ds_read_b128 a[116:119], v7 offset:29696\n"
"v_mfma_f32_16x16x32_bf16 v[172:175], a[124:127], v[92:95], v[172:175]\n"
"v_mfma_f32_16x16x32_bf16 v[176:179], a[64:67], v[80:83], v[176:179]\n"
"ds_read_b128 a[120:123], v7 offset:30720\n"
"s_add_u32 m0, s60, s62\n"
"s_nop 0\n"
"buffer_load_dwordx4 v16, s[44:47], s72 offen offset:0 lds\n"
"v_mfma_f32_16x16x32_bf16 v[180:183], a[64:67], v[88:91], v[180:183]\n"
"v_mfma_f32_16x16x32_bf16 v[176:179], a[68:71], v[84:87], v[176:179]\n"
"ds_read_b128 a[124:127], v7 offset:31744\n"
"s_waitcnt lgkmcnt(8)\n"
"v_mfma_f32_16x16x32_bf16 v[180:183], a[68:71], v[92:95], v[180:183]\n"
"buffer_load_dwordx4 v17, s[44:47], s72 offen offset:1024 lds\n"
"v_mfma_f32_16x16x32_bf16 v[184:187], a[72:75], v[80:83], v[184:187]\n"
"v_mfma_f32_16x16x32_bf16 v[188:191], a[72:75], v[88:91], v[188:191]\n"
"v_mfma_f32_16x16x32_bf16 v[184:187], a[76:79], v[84:87], v[184:187]\n"
"buffer_load_dwordx4 v18, s[44:47], s72 offen offset:2048 lds\n"
"v_mfma_f32_16x16x32_bf16 v[188:191], a[76:79], v[92:95], v[188:191]\n"
"v_mfma_f32_16x16x32_bf16 v[192:195], a[80:83], v[80:83], v[192:195]\n"
"v_mfma_f32_16x16x32_bf16 v[196:199], a[80:83], v[88:91], v[196:199]\n"
"buffer_load_dwordx4 v19, s[44:47], s72 offen offset:3072 lds\n"
"v_mfma_f32_16x16x32_bf16 v[192:195], a[84:87], v[84:87], v[192:195]\n"
"v_mfma_f32_16x16x32_bf16 v[196:199], a[84:87], v[92:95], v[196:199]\n"
"v_mfma_f32_16x16x32_bf16 v[200:203], a[88:91], v[80:83], v[200:203]\n"
"s_add_u32 m0, m0, 0x1000\n"
"s_nop 0\n"
"buffer_load_dwordx4 v20, s[44:47], s72 offen offset:0 lds\n"
"v_mfma_f32_16x16x32_bf16 v[204:207], a[88:91], v[88:91], v[204:207]\n"
"v_mfma_f32_16x16x32_bf16 v[200:203], a[92:95], v[84:87], v[200:203]\n"
"v_mfma_f32_16x16x32_bf16 v[204:207], a[92:95], v[92:95], v[204:207]\n"
"buffer_load_dwordx4 v21, s[44:47], s72 offen offset:1024 lds\n"
"s_add_u32 s60, s60, 32768\n"
"s_cmp_eq_u32 s60, 163840\n"
"s_cselect_b32 s60, 0, s60\n"
"s_waitcnt lgkmcnt(7)\n"
"v_mfma_f32_16x16x32_bf16 v[208:211], a[96:99], v[80:83], v[208:211]\n"
"v_mfma_f32_16x16x32_bf16 v[212:215], a[96:99], v[88:91], v[212:215]\n"
"s_waitcnt lgkmcnt(6)\n"
"v_mfma_f32_16x16x32_bf16 v[208:211], a[100:103], v[84:87], v[208:211]\n"
"s_add_u32 m0, s60, s62\n"
"s_nop 0\n"
"buffer_load_dwordx4 v16, s[48:51], s72 offen offset:0 lds\n"
"v_mfma_f32_16x16x32_bf16 v[212:215], a[100:103], v[92:95], v[212:215]\n"
"s_waitcnt lgkmcnt(5)\n"
"v_mfma_f32_16x16x32_bf16 v[216:219], a[104:107], v[80:83], v[216:219]\n"
"v_mfma_f32_16x16x32_bf16 v[220:223], a[104:107], v[88:91], v[220:223]\n"
"buffer_load_dwordx4 v17, s[48:51], s72 offen offset:1024 lds\n"
"s_waitcnt lgkmcnt(4)\n"
"v_mfma_f32_16x16x32_bf16 v[216:219], a[108:111], v[84:87], v[216:219]\n"
"v_mfma_f32_16x16x32_bf16 v[220:223], a[108:111], v[92:95], v[220:223]\n"
"s_waitcnt lgkmcnt(3)\n"
"v_mfma_f32_16x16x32_bf16 v[224:227], a[112:115], v[80:83], v[224:227]\n"
"buffer_load_dwordx4 v18, s[96:99], s72 offen offset:2048 lds\n"
"s_waitcnt vmcnt(25)\n"
"v_or_b32 v14, v248, v249\n"
"v_mfma_f32_16x16x32_bf16 v[228:231], a[112:115], v[88:91], v[228:231]\n"
"v_add_f32 v12, v12, v250\n"
"s_waitcnt lgkmcnt(2)\n"
"v_mfma_f32_16x16x32_bf16 v[224:227], a[116:119], v[84:87], v[224:227]\n"
"v_add_f32 v13, v13, v251\n"
"v_mfma_f32_16x16x32_bf16 v[228:231], a[116:119], v[92:95], v[228:231]\n"
"buffer_load_dwordx4 v19, s[96:99], s72 offen offset:3072 lds\n"
"v_cmp_ne_u32 vcc, 0, v14\n"
"s_waitcnt lgkmcnt(1)\n"
"v_mfma_f32_16x16x32_bf16 v[232:235], a[120:123], v[80:83], v[232:235]\n"
"v_max_f32 v242, 0x0da24260, v12\n"
"v_mfma_f32_16x16x32_bf16 v[236:239], a[120:123], v[88:91], v[236:239]\n"
"v_max_f32 v244, 0x0da24260, v13\n"
"s_waitcnt lgkmcnt(0)\n"
"v_mfma_f32_16x16x32_bf16 v[232:235], a[124:127], v[84:87], v[232:235]\n"
"s_add_u32 m0, m0, 0x1000\n"
"s_nop 0\n"
"buffer_load_dwordx4 v20, s[96:99], s72 offen offset:0 lds\n"
"v_mfma_f32_16x16x32_bf16 v[236:239], a[124:127], v[92:95], v[236:239]\n"
"v_rcp_f32 v242, v242\n"
"s_waitcnt vmcnt(26)\n"
"v_mfma_f32_16x16x32_bf16 v[112:115], a[240:243], a[176:179], v[112:115]\n"
"v_rcp_f32 v244, v244\n"
"v_mfma_f32_16x16x32_bf16 v[120:123], a[244:247], a[176:179], v[120:123]\n"
"buffer_load_dwordx4 v21, s[96:99], s72 offen offset:1024 lds\n"
"s_add_u32 s60, s60, 32768\n"
"s_cmp_eq_u32 s60, 163840\n"
"s_cselect_b32 s60, 0, s60\n"
"s_add_u32 s72, s72, 128\n"
"s_waitcnt vmcnt(26)\n"
"v_mfma_f32_16x16x32_bf16 v[116:119], a[240:243], a[180:183], v[116:119]\n"
"s_nop 0\n"
"v_mfma_f32_16x16x32_bf16 v[124:127], a[244:247], a[180:183], v[124:127]\n"
"s_or_b64 s[80:81], s[80:81], vcc\n"
"s_waitcnt vmcnt(25)\n"
"v_mfma_f32_16x16x32_bf16 v[128:131], a[240:243], a[184:187], v[128:131]\n"
"s_add_u32 m0, s60, s62\n"
"s_nop 0\n"
"buffer_load_dwordx4 v16, s[44:47], s72 offen offset:0 lds\n"
"v_mfma_f32_16x16x32_bf16 v[136:139], a[244:247], a[184:187], v[136:139]\n"
"v_pk_mul_f32 v[112:113], v[112:113], v[242:243] op_sel_hi:[1,0]\n"
"v_pk_mul_f32 v[114:115], v[114:115], v[242:243] op_sel_hi:[1,0]\n"
"v_pk_mul_f32 v[120:121], v[120:121], v[242:243] op_sel_hi:[1,0]\n"
"s_waitcnt vmcnt(25)\n"
"v_mfma_f32_16x16x32_bf16 v[132:135], a[240:243], a[188:191], v[132:135]\n"
"v_pk_mul_f32 v[122:123], v[122:123], v[242:243] op_sel_hi:[1,0]\n"
"v_cvt_pk_bf16_f32 v112, v112, v113\n"
"v_cvt_pk_bf16_f32 v113, v114, v115\n"
"v_mfma_f32_16x16x32_bf16 v[140:143], a[244:247], a[188:191], v[140:143]\n"
"buffer_load_dwordx4 v17, s[44:47], s72 offen offset:1024 lds\n"
"v_cvt_pk_bf16_f32 v114, v120, v121\n"
"v_cvt_pk_bf16_f32 v115, v122, v123\n"
"s_waitcnt vmcnt(25)\n"
"v_mfma_f32_16x16x32_bf16 v[144:147], a[240:243], a[192:195], v[144:147]\n"
"v_pk_mul_f32 v[116:117], v[116:117], v[244:245] op_sel_hi:[1,0]\n"
"v_pk_mul_f32 v[118:119], v[118:119], v[244:245] op_sel_hi:[1,0]\n"
"v_pk_mul_f32 v[124:125], v[124:125], v[244:245] op_sel_hi:[1,0]\n"
"v_mfma_f32_16x16x32_bf16 v[152:155], a[244:247], a[192:195], v[152:155]\n"
"v_pk_mul_f32 v[126:127], v[126:127], v[244:245] op_sel_hi:[1,0]\n"
"v_cvt_pk_bf16_f32 v116, v116, v117\n"
"v_cvt_pk_bf16_f32 v117, v118, v119\n"
"s_waitcnt vmcnt(24)\n"
"v_mfma_f32_16x16x32_bf16 v[148:151], a[240:243], a[196:199], v[148:151]\n"
"buffer_load_dwordx4 v18, s[44:47], s72 offen offset:2048 lds\n"
"v_cvt_pk_bf16_f32 v118, v124, v125\n"
"v_cvt_pk_bf16_f32 v119, v126, v127\n"
"v_mfma_f32_16x16x32_bf16 v[156:159], a[244:247], a[196:199], v[156:159]\n"
"v_pk_mul_f32 v[128:129], v[128:129], v[242:243] op_sel_hi:[1,0]\n"
"v_pk_mul_f32 v[130:131], v[130:131], v[242:243] op_sel_hi:[1,0]\n"
"v_pk_mul_f32 v[136:137], v[136:137], v[242:243] op_sel_hi:[1,0]\n"
"s_waitcnt vmcnt(24)\n"
"v_mfma_f32_16x16x32_bf16 v[160:163], a[240:243], a[200:203], v[160:163]\n"
"v_pk_mul_f32 v[138:139], v[138:139], v[242:243] op_sel_hi:[1,0]\n"
"v_cvt_pk_bf16_f32 v128, v128, v129\n"
"v_cvt_pk_bf16_f32 v129, v130, v131\n"
"v_mfma_f32_16x16x32_bf16 v[168:171], a[244:247], a[200:203], v[168:171]\n"
"buffer_load_dwordx4 v19, s[44:47], s72 offen offset:3072 lds\n"
"v_cvt_pk_bf16_f32 v130, v136, v137\n"
"v_cvt_pk_bf16_f32 v131, v138, v139\n"
"s_waitcnt vmcnt(24)\n"
"v_mfma_f32_16x16x32_bf16 v[164:167], a[240:243], a[204:207], v[164:167]\n"
"v_pk_mul_f32 v[132:133], v[132:133], v[244:245] op_sel_hi:[1,0]\n"
"v_pk_mul_f32 v[134:135], v[134:135], v[244:245] op_sel_hi:[1,0]\n"
"v_pk_mul_f32 v[140:141], v[140:141], v[244:245] op_sel_hi:[1,0]\n"
"v_mfma_f32_16x16x32_bf16 v[172:175], a[244:247], a[204:207], v[172:175]\n"
"v_pk_mul_f32 v[142:143], v[142:143], v[244:245] op_sel_hi:[1,0]\n"
"v_cvt_pk_bf16_f32 v132, v132, v133\n"
"v_cvt_pk_bf16_f32 v133, v134, v135\n"
"s_waitcnt vmcnt(23)\n"
"v_mfma_f32_16x16x32_bf16 v[176:179], a[240:243], a[208:211], v[176:179]\n"
"s_add_u32 m0, m0, 0x1000\n"
"s_nop 0\n"
"buffer_load_dwordx4 v20, s[44:47], s72 offen offset:0 lds\n"
"v_mfma_f32_16x16x32_bf16 v[184:187], a[244:247], a[208:211], v[184:187]\n"
"v_cvt_pk_bf16_f32 v134, v140, v141\n"
"v_cvt_pk_bf16_f32 v135, v142, v143\n"
"v_pk_mul_f32 v[144:145], v[144:145], v[242:243] op_sel_hi:[1,0]\n"
"s_waitcnt vmcnt(23)\n"
"v_mfma_f32_16x16x32_bf16 v[180:183], a[240:243], a[212:215], v[180:183]\n"
"v_pk_mul_f32 v[146:147], v[146:147], v[242:243] op_sel_hi:[1,0]\n"
"v_pk_mul_f32 v[152:153], v[152:153], v[242:243] op_sel_hi:[1,0]\n"
"v_pk_mul_f32 v[154:155], v[154:155], v[242:243] op_sel_hi:[1,0]\n"
"v_mfma_f32_16x16x32_bf16 v[188:191], a[244:247], a[212:215], v[188:191]\n"
"buffer_load_dwordx4 v21, s[44:47], s72 offen offset:1024 lds\n"
"s_add_u32 s60, s60, 32768\n"
"s_cmp_eq_u32 s60, 163840\n"
"s_cselect_b32 s60, 0, s60\n"
"s_waitcnt vmcnt(23)\n"
"v_mfma_f32_16x16x32_bf16 v[192:195], a[240:243], a[216:219], v[192:195]\n"
"v_cvt_pk_bf16_f32 v144, v144, v145\n"
"v_cvt_pk_bf16_f32 v145, v146, v147\n"
"v_cvt_pk_bf16_f32 v146, v152, v153\n"
"v_mfma_f32_16x16x32_bf16 v[200:203], a[244:247], a[216:219], v[200:203]\n"
"v_cvt_pk_bf16_f32 v147, v154, v155\n"
"v_pk_mul_f32 v[148:149], v[148:149], v[244:245] op_sel_hi:[1,0]\n"
"v_pk_mul_f32 v[150:151], v[150:151], v[244:245] op_sel_hi:[1,0]\n"
"s_waitcnt vmcnt(22)\n"
"v_mfma_f32_16x16x32_bf16 v[196:199], a[240:243], a[220:223], v[196:199]\n"
"s_add_u32 m0, s60, s62\n"
"s_nop 0\n"
"buffer_load_dwordx4 v16, s[48:51], s72 offen offset:0 lds\n"
"v_mfma_f32_16x16x32_bf16 v[204:207], a[244:247], a[220:223], v[204:207]\n"
"v_pk_mul_f32 v[156:157], v[156:157], v[244:245] op_sel_hi:[1,0]\n"
"v_pk_mul_f32 v[158:159], v[158:159], v[244:245] op_sel_hi:[1,0]\n"
"v_cvt_pk_bf16_f32 v148, v148, v149\n"
"s_waitcnt vmcnt(22)\n"
"v_mfma_f32_16x16x32_bf16 v[208:211], a[240:243], a[224:227], v[208:211]\n"
"v_cvt_pk_bf16_f32 v149, v150, v151\n"
"v_cvt_pk_bf16_f32 v150, v156, v157\n"
"v_cvt_pk_bf16_f32 v151, v158, v159\n"
"v_mfma_f32_16x16x32_bf16 v[216:219], a[244:247], a[224:227], v[216:219]\n"
"buffer_load_dwordx4 v17, s[48:51], s72 offen offset:1024 lds\n"
"v_pk_mul_f32 v[160:161], v[160:161], v[242:243] op_sel_hi:[1,0]\n"
"v_pk_mul_f32 v[162:163], v[162:163], v[242:243] op_sel_hi:[1,0]\n"
"s_waitcnt vmcnt(22)\n"
"v_mfma_f32_16x16x32_bf16 v[212:215], a[240:243], a[228:231], v[212:215]\n"
"v_pk_mul_f32 v[168:169], v[168:169], v[242:243] op_sel_hi:[1,0]\n"
"v_pk_mul_f32 v[170:171], v[170:171], v[242:243] op_sel_hi:[1,0]\n"
"v_cvt_pk_bf16_f32 v160, v160, v161\n"
"v_mfma_f32_16x16x32_bf16 v[220:223], a[244:247], a[228:231], v[220:223]\n"
"v_cvt_pk_bf16_f32 v161, v162, v163\n"
"v_cvt_pk_bf16_f32 v162, v168, v169\n"
"v_cvt_pk_bf16_f32 v163, v170, v171\n"
"s_waitcnt vmcnt(21)\n"
"v_mfma_f32_16x16x32_bf16 v[224:227], a[240:243], a[232:235], v[224:227]\n"
"buffer_load_dwordx4 v18, s[96:99], s72 offen offset:2048 lds\n"
"v_pk_mul_f32 v[164:165], v[164:165], v[244:245] op_sel_hi:[1,0]\n"
"v_pk_mul_f32 v[166:167], v[166:167], v[244:245] op_sel_hi:[1,0]\n"
"v_mfma_f32_16x16x32_bf16 v[232:235], a[244:247], a[232:235], v[232:235]\n"
"v_pk_mul_f32 v[172:173], v[172:173], v[244:245] op_sel_hi:[1,0]\n"
"v_pk_mul_f32 v[174:175], v[174:175], v[244:245] op_sel_hi:[1,0]\n"
"v_cvt_pk_bf16_f32 v164, v164, v165\n"
"s_waitcnt vmcnt(21)\n"
"v_mfma_f32_16x16x32_bf16 v[228:231], a[240:243], a[236:239], v[228:231]\n"
"v_cvt_pk_bf16_f32 v165, v166, v167\n"
"v_cvt_pk_bf16_f32 v166, v172, v173\n"
"v_cvt_pk_bf16_f32 v167, v174, v175\n"
"v_mfma_f32_16x16x32_bf16 v[236:239], a[244:247], a[236:239], v[236:239]\n"
"v_pk_mul_f32 v[176:177], v[176:177], v[242:243] op_sel_hi:[1,0]\n"
"v_pk_mul_f32 v[178:179], v[178:179], v[242:243] op_sel_hi:[1,0]\n"
"v_pk_mul_f32 v[184:185], v[184:185], v[242:243] op_sel_hi:[1,0]\n"
"s_nop 7\n"
"v_pk_mul_f32 v[186:187], v[186:187], v[242:243] op_sel_hi:[1,0]\n"
"v_cvt_pk_bf16_f32 v176, v176, v177\n"
"v_cvt_pk_bf16_f32 v177, v178, v179\n"
"v_cvt_pk_bf16_f32 v178, v184, v185\n"
"v_cvt_pk_bf16_f32 v179, v186, v187\n"
"v_pk_mul_f32 v[180:181], v[180:181], v[244:245] op_sel_hi:[1,0]\n"
"v_pk_mul_f32 v[182:183], v[182:183], v[244:245] op_sel_hi:[1,0]\n"
"v_pk_mul_f32 v[188:189], v[188:189], v[244:245] op_sel_hi:[1,0]\n"
"v_pk_mul_f32 v[190:191], v[190:191], v[244:245] op_sel_hi:[1,0]\n"
"v_cvt_pk_bf16_f32 v180, v180, v181\n"
"v_cvt_pk_bf16_f32 v181, v182, v183\n"
"v_cvt_pk_bf16_f32 v182, v188, v189\n"
"v_cvt_pk_bf16_f32 v183, v190, v191\n"
"v_pk_mul_f32 v[192:193], v[192:193], v[242:243] op_sel_hi:[1,0]\n"
"v_pk_mul_f32 v[194:195], v[194:195], v[242:243] op_sel_hi:[1,0]\n"
"v_pk_mul_f32 v[200:201], v[200:201], v[242:243] op_sel_hi:[1,0]\n"
"v_pk_mul_f32 v[202:203], v[202:203], v[242:243] op_sel_hi:[1,0]\n"
"v_cvt_pk_bf16_f32 v192, v192, v193\n"
"v_cvt_pk_bf16_f32 v193, v194, v195\n"
"v_cvt_pk_bf16_f32 v194, v200, v201\n"
"v_cvt_pk_bf16_f32 v195, v202, v203\n"
"v_pk_mul_f32 v[196:197], v[196:197], v[244:245] op_sel_hi:[1,0]\n"
"v_pk_mul_f32 v[198:199], v[198:199], v[244:245] op_sel_hi:[1,0]\n"
"v_pk_mul_f32 v[204:205], v[204:205], v[244:245] op_sel_hi:[1,0]\n"
"v_pk_mul_f32 v[206:207], v[206:207], v[244:245] op_sel_hi:[1,0]\n"
"v_cvt_pk_bf16_f32 v196, v196, v197\n"
"v_cvt_pk_bf16_f32 v197, v198, v199\n"
"v_cvt_pk_bf16_f32 v198, v204, v205\n"
"v_cvt_pk_bf16_f32 v199, v206, v207\n"
"v_pk_mul_f32 v[208:209], v[208:209], v[242:243] op_sel_hi:[1,0]\n"
"v_pk_mul_f32 v[210:211], v[210:211], v[242:243] op_sel_hi:[1,0]\n"
"v_pk_mul_f32 v[216:217], v[216:217], v[242:243] op_sel_hi:[1,0]\n"
"v_pk_mul_f32 v[218:219], v[218:219], v[242:243] op_sel_hi:[1,0]\n"
"v_cvt_pk_bf16_f32 v208, v208, v209\n"
"v_cvt_pk_bf16_f32 v209, v210, v211\n"
"v_cvt_pk_bf16_f32 v210, v216, v217\n"
"v_cvt_pk_bf16_f32 v211, v218, v219\n"
"v_pk_mul_f32 v[212:213], v[212:213], v[244:245] op_sel_hi:[1,0]\n"
"v_pk_mul_f32 v[214:215], v[214:215], v[244:245] op_sel_hi:[1,0]\n"
"v_pk_mul_f32 v[220:221], v[220:221], v[244:245] op_sel_hi:[1,0]\n"
"v_pk_mul_f32 v[222:223], v[222:223], v[244:245] op_sel_hi:[1,0]\n"
"v_cvt_pk_bf16_f32 v212, v212, v213\n"
"v_cvt_pk_bf16_f32 v213, v214, v215\n"
"v_cvt_pk_bf16_f32 v214, v220, v221\n"
"v_cvt_pk_bf16_f32 v215, v222, v223\n"
"v_pk_mul_f32 v[224:225], v[224:225], v[242:243] op_sel_hi:[1,0]\n"
"v_pk_mul_f32 v[226:227], v[226:227], v[242:243] op_sel_hi:[1,0]\n"
"v_pk_mul_f32 v[232:233], v[232:233], v[242:243] op_sel_hi:[1,0]\n"
"v_pk_mul_f32 v[234:235], v[234:235], v[242:243] op_sel_hi:[1,0]\n"
"v_cvt_pk_bf16_f32 v224, v224, v225\n"
"v_cvt_pk_bf16_f32 v225, v226, v227\n"
"v_cvt_pk_bf16_f32 v226, v232, v233\n"
"v_cvt_pk_bf16_f32 v227, v234, v235\n"
"v_pk_mul_f32 v[228:229], v[228:229], v[244:245] op_sel_hi:[1,0]\n"
"v_pk_mul_f32 v[230:231], v[230:231], v[244:245] op_sel_hi:[1,0]\n"
"v_pk_mul_f32 v[236:237], v[236:237], v[244:245] op_sel_hi:[1,0]\n"
"v_pk_mul_f32 v[238:239], v[238:239], v[244:245] op_sel_hi:[1,0]\n"
"v_cvt_pk_bf16_f32 v228, v228, v229\n"
"v_cvt_pk_bf16_f32 v229, v230, v231\n"
"v_cvt_pk_bf16_f32 v230, v236, v237\n"
"v_cvt_pk_bf16_f32 v231, v238, v239\n"
"s_branch LCK%=\n"
"LBF0%=:\n"
"v_mfma_f32_16x16x32_bf16 a[128:131], a[136:139], v[96:99], a[128:131]\n"
"ds_read_b128 a[112:115], v7 offset:12288\n"
"s_add_u32 s59, s59, 32768\n"
"s_cmp_eq_u32 s59, 163840\n"
"v_mfma_f32_16x16x32_bf16 a[128:131], a[136:139], v[100:103], a[128:131]\n"
"s_cselect_b32 s59, 0, s59\n"
"s_mov_b32 s60, s59\n"
"v_add_u32 v16, 0, v4\n"
"v_mfma_f32_16x16x32_bf16 a[132:135], a[136:139], v[104:107], a[132:135]\n"
"ds_read_b128 a[116:119], v7 offset:13312\n"
"v_add_u32 v17, 6144, v4\n"
"v_add_u32 v18, 12288, v4\n"
"v_mfma_f32_16x16x32_bf16 a[132:135], a[136:139], v[108:111], a[132:135]\n"
"v_add_u32 v19, 18432, v4\n"
"v_add_u32 v20, 28672, v4\n"
"v_add_u32 v21, 34816, v4\n"
"v_mfma_f32_16x16x32_bf16 v[112:115], a[64:67], v[96:99], v[112:115]\n"
"ds_read_b128 a[120:123], v7 offset:14336\n"
"s_mov_b32 s72, 0\n"
"v_mfma_f32_16x16x32_bf16 v[116:119], a[64:67], v[104:107], v[116:119]\n"
"s_add_u32 m0, s60, s62\n"
"s_nop 0\n"
"buffer_load_dwordx4 v16, s[44:47], s72 offen offset:0 lds\n"
"v_mfma_f32_16x16x32_bf16 v[112:115], a[68:71], v[100:103], v[112:115]\n"
"ds_read_b128 a[124:127], v7 offset:15360\n"
"s_waitcnt lgkmcnt(8)\n"
"v_mfma_f32_16x16x32_bf16 v[116:119], a[68:71], v[108:111], v[116:119]\n"
"v_mfma_f32_16x16x32_bf16 v[120:123], a[72:75], v[96:99], v[120:123]\n"
"ds_read_b128 a[64:67], v7 offset:16384\n"
"buffer_load_dwordx4 v17, s[44:47], s72 offen offset:1024 lds\n"
"v_accvgpr_read_b32 v12, a128\n"
"v_mfma_f32_16x16x32_bf16 v[124:127], a[72:75], v[104:107], v[124:127]\n"
"v_accvgpr_read_b32 v13, a132\n"
"v_mfma_f32_16x16x32_bf16 v[120:123], a[76:79], v[100:103], v[120:123]\n"
"ds_read_b128 a[68:71], v7 offset:17408\n"
"v_add_f32 v14, s79, v12\n"
"v_mfma_f32_16x16x32_bf16 v[124:127], a[76:79], v[108:111], v[124:127]\n"
"buffer_load_dwordx4 v18, s[44:47], s72 offen offset:2048 lds\n"
"v_add_f32 v15, v12, v13\n"
"v_mfma_f32_16x16x32_bf16 v[128:131], a[80:83], v[96:99], v[128:131]\n"
"ds_read_b128 a[72:75], v7 offset:18432\n"
"v_min_f32 v14, v14, v13\n"
"v_mfma_f32_16x16x32_bf16 v[132:135], a[80:83], v[104:107], v[132:135]\n"
"v_cmp_nle_f32 s[80:81], v15, v240\n"
"v_mfma_f32_16x16x32_bf16 v[128:131], a[84:87], v[100:103], v[128:131]\n"
"ds_read_b128 a[76:79], v7 offset:19456\n"
"s_waitcnt lgkmcnt(8)\n"
"buffer_load_dwordx4 v19, s[44:47], s72 offen offset:3072 lds\n"
"v_mfma_f32_16x16x32_bf16 v[132:135], a[84:87], v[108:111], v[132:135]\n"
"v_cmp_lt_f32 vcc, v14, v241\n"
"v_mfma_f32_16x16x32_bf16 v[136:139], a[88:91], v[96:99], v[136:139]\n"
"ds_read_b128 a[80:83], v7 offset:20480\n"
"s_or_b64 s[80:81], s[80:81], vcc\n"
"v_mfma_f32_16x16x32_bf16 v[140:143], a[88:91], v[104:107], v[140:143]\n"
"s_add_u32 m0, m0, 0x1000\n"
"s_nop 0\n"
"buffer_load_dwordx4 v20, s[44:47], s72 offen offset:0 lds\n"
"v_mfma_f32_16x16x32_bf16 v[136:139], a[92:95], v[100:103], v[136:139]\n"
"ds_read_b128 a[84:87], v7 offset:21504\n"
"v_max_f32 v242, 0x0da24260, v12\n"
"v_mfma_f32_16x16x32_bf16 v[140:143], a[92:95], v[108:111], v[140:143]\n"
"v_max_f32 v244, 0x0da24260, v13\n"
"v_mfma_f32_16x16x32_bf16 v[144:147], a[96:99], v[96:99], v[144:147]\n"
"ds_read_b128 a[88:91], v7 offset:22528\n"
"buffer_load_dwordx4 v21, s[44:47], s72 offen offset:1024 lds\n"
"s_add_u32 s60, s60, 32768\n"
"s_cmp_eq_u32 s60, 163840\n"
"s_cselect_b32 s60, 0, s60\n"
"v_mfma_f32_16x16x32_bf16 v[148:151], a[96:99], v[104:107], v[148:151]\n"
"v_rcp_f32 v242, v242\n"
"v_mfma_f32_16x16x32_bf16 v[144:147], a[100:103], v[100:103], v[144:147]\n"
"ds_read_b128 a[92:95], v7 offset:23552\n"
"s_waitcnt lgkmcnt(8)\n"
"v_mfma_f32_16x16x32_bf16 v[148:151], a[100:103], v[108:111], v[148:151]\n"
"s_add_u32 m0, s60, s62\n"
"s_nop 0\n"
"buffer_load_dwordx4 v16, s[48:51], s72 offen offset:0 lds\n"
"v_mfma_f32_16x16x32_bf16 v[152:155], a[104:107], v[96:99], v[152:155]\n"
"ds_read_b128 a[96:99], v7 offset:24576\n"
"v_rcp_f32 v244, v244\n"
"v_mfma_f32_16x16x32_bf16 v[156:159], a[104:107], v[104:107], v[156:159]\n"
"s_nop 0\n"
"v_mfma_f32_16x16x32_bf16 v[152:155], a[108:111], v[100:103], v[152:155]\n"
"ds_read_b128 a[100:103], v7 offset:25600\n"
"buffer_load_dwordx4 v17, s[48:51], s72 offen offset:1024 lds\n"
"v_pk_mul_f32 v[112:113], v[112:113], v[242:243] op_sel_hi:[1,0]\n"
"v_mfma_f32_16x16x32_bf16 v[156:159], a[108:111], v[108:111], v[156:159]\n"
"v_pk_mul_f32 v[114:115], v[114:115], v[242:243] op_sel_hi:[1,0]\n"
"v_pk_mul_f32 v[120:121], v[120:121], v[242:243] op_sel_hi:[1,0]\n"
"v_pk_mul_f32 v[122:123], v[122:123], v[242:243] op_sel_hi:[1,0]\n"
"v_mfma_f32_16x16x32_bf16 v[160:163], a[112:115], v[96:99], v[160:163]\n"
"ds_read_b128 a[104:107], v7 offset:26624\n"
"v_cvt_pk_bf16_f32 v112, v112, v113\n"
"v_cvt_pk_bf16_f32 v113, v114, v115\n"
"v_mfma_f32_16x16x32_bf16 v[164:167], a[112:115], v[104:107], v[164:167]\n"
"buffer_load_dwordx4 v18, s[96:99], s72 offen offset:2048 lds\n"
"v_cvt_pk_bf16_f32 v114, v120, v121\n"
"v_cvt_pk_bf16_f32 v115, v122, v123\n"
"v_mfma_f32_16x16x32_bf16 v[160:163], a[116:119], v[100:103], v[160:163]\n"
"ds_read_b128 a[108:111], v7 offset:27648\n"
"s_waitcnt lgkmcnt(8)\n"
"v_pk_mul_f32 v[116:117], v[116:117], v[244:245] op_sel_hi:[1,0]\n"
"v_mfma_f32_16x16x32_bf16 v[164:167], a[116:119], v[108:111], v[164:167]\n"
"v_pk_mul_f32 v[118:119], v[118:119], v[244:245] op_sel_hi:[1,0]\n"
"v_pk_mul_f32 v[124:125], v[124:125], v[244:245] op_sel_hi:[1,0]\n"
"v_pk_mul_f32 v[126:127], v[126:127], v[244:245] op_sel_hi:[1,0]\n"
"v_mfma_f32_16x16x32_bf16 v[168:171], a[120:123], v[96:99], v[168:171]\n"
"ds_read_b128 a[112:115], v7 offset:28672\n"
"buffer_load_dwordx4 v19, s[96:99], s72 offen offset:3072 lds\n"
"v_cvt_pk_bf16_f32 v116, v116, v117\n"
"v_mfma_f32_16x16x32_bf16 v[172:175], a[120:123], v[104:107], v[172:175]\n"
"v_cvt_pk_bf16_f32 v117, v118, v119\n"
"v_cvt_pk_bf16_f32 v118, v124, v125\n"
"v_cvt_pk_bf16_f32 v119, v126, v127\n"
"v_mfma_f32_16x16x32_bf16 v[168:171], a[124:127], v[100:103], v[168:171]\n"
"ds_read_b128 a[116:119], v7 offset:29696\n"
"v_pk_mul_f32 v[128:129], v[128:129], v[242:243] op_sel_hi:[1,0]\n"
"v_pk_mul_f32 v[130:131], v[130:131], v[242:243] op_sel_hi:[1,0]\n"
"v_mfma_f32_16x16x32_bf16 v[172:175], a[124:127], v[108:111], v[172:175]\n"
"s_add_u32 m0, m0, 0x1000\n"
"s_nop 0\n"
"buffer_load_dwordx4 v20, s[96:99], s72 offen offset:0 lds\n"
"v_mfma_f32_16x16x32_bf16 v[176:179], a[64:67], v[96:99], v[176:179]\n"
"ds_read_b128 a[120:123], v7 offset:30720\n"
"v_pk_mul_f32 v[136:137], v[136:137], v[242:243] op_sel_hi:[1,0]\n"
"v_pk_mul_f32 v[138:139], v[138:139], v[242:243] op_sel_hi:[1,0]\n"
"v_mfma_f32_16x16x32_bf16 v[180:183], a[64:67], v[104:107], v[180:183]\n"
"v_cvt_pk_bf16_f32 v128, v128, v129\n"
"v_cvt_pk_bf16_f32 v129, v130, v131\n"
"v_cvt_pk_bf16_f32 v130, v136, v137\n"
"v_mfma_f32_16x16x32_bf16 v[176:179], a[68:71], v[100:103], v[176:179]\n"
"ds_read_b128 a[124:127], v7 offset:31744\n"
"s_waitcnt lgkmcnt(8)\n"
"buffer_load_dwordx4 v21, s[96:99], s72 offen offset:1024 lds\n"
"s_add_u32 s60, s60, 32768\n"
"s_cmp_eq_u32 s60, 163840\n"
"s_cselect_b32 s60, 0, s60\n"
"s_add_u32 s72, s72, 128\n"
"v_mfma_f32_16x16x32_bf16 v[180:183], a[68:71], v[108:111], v[180:183]\n"
"v_cvt_pk_bf16_f32 v131, v138, v139\n"
"v_pk_mul_f32 v[132:133], v[132:133], v[244:245] op_sel_hi:[1,0]\n"
"v_pk_mul_f32 v[134:135], v[134:135], v[244:245] op_sel_hi:[1,0]\n"
"v_mfma_f32_16x16x32_bf16 v[184:187], a[72:75], v[96:99], v[184:187]\n"
"v_pk_mul_f32 v[140:141], v[140:141], v[244:245] op_sel_hi:[1,0]\n"
"v_pk_mul_f32 v[142:143], v[142:143], v[244:245] op_sel_hi:[1,0]\n"
"v_cvt_pk_bf16_f32 v132, v132, v133\n"
"v_mfma_f32_16x16x32_bf16 v[188:191], a[72:75], v[104:107], v[188:191]\n"
"s_add_u32 m0, s60, s62\n"
"s_nop 0\n"
"buffer_load_dwordx4 v16, s[44:47], s72 offen offset:0 lds\n"
"v_mfma_f32_16x16x32_bf16 v[184:187], a[76:79], v[100:103], v[184:187]\n"
"v_cvt_pk_bf16_f32 v133, v134, v135\n"
"v_cvt_pk_bf16_f32 v134, v140, v141\n"
"v_cvt_pk_bf16_f32 v135, v142, v143\n"
"v_mfma_f32_16x16x32_bf16 v[188:191], a[76:79], v[108:111], v[188:191]\n"
"v_pk_mul_f32 v[144:145], v[144:145], v[242:243] op_sel_hi:[1,0]\n"
"v_pk_mul_f32 v[146:147], v[146:147], v[242:243] op_sel_hi:[1,0]\n"
"v_pk_mul_f32 v[152:153], v[152:153], v[242:243] op_sel_hi:[1,0]\n"
"v_mfma_f32_16x16x32_bf16 v[192:195], a[80:83], v[96:99], v[192:195]\n"
"buffer_load_dwordx4 v17, s[44:47], s72 offen offset:1024 lds\n"
"v_pk_mul_f32 v[154:155], v[154:155], v[242:243] op_sel_hi:[1,0]\n"
"v_cvt_pk_bf16_f32 v144, v144, v145\n"
"v_mfma_f32_16x16x32_bf16 v[196:199], a[80:83], v[104:107], v[196:199]\n"
"v_cvt_pk_bf16_f32 v145, v146, v147\n"
"v_cvt_pk_bf16_f32 v146, v152, v153\n"
"v_cvt_pk_bf16_f32 v147, v154, v155\n"
"v_mfma_f32_16x16x32_bf16 v[192:195], a[84:87], v[100:103], v[192:195]\n"
"v_pk_mul_f32 v[148:149], v[148:149], v[244:245] op_sel_hi:[1,0]\n"
"v_pk_mul_f32 v[150:151], v[150:151], v[244:245] op_sel_hi:[1,0]\n"
"v_pk_mul_f32 v[156:157], v[156:157], v[244:245] op_sel_hi:[1,0]\n"
"v_mfma_f32_16x16x32_bf16 v[196:199], a[84:87], v[108:111], v[196:199]\n"
"buffer_load_dwordx4 v18, s[44:47], s72 offen offset:2048 lds\n"
"v_pk_mul_f32 v[158:159], v[158:159], v[244:245] op_sel_hi:[1,0]\n"
"v_cvt_pk_bf16_f32 v148, v148, v149\n"
"v_mfma_f32_16x16x32_bf16 v[200:203], a[88:91], v[96:99], v[200:203]\n"
"v_cvt_pk_bf16_f32 v149, v150, v151\n"
"v_cvt_pk_bf16_f32 v150, v156, v157\n"
"v_cvt_pk_bf16_f32 v151, v158, v159\n"
"v_mfma_f32_16x16x32_bf16 v[204:207], a[88:91], v[104:107], v[204:207]\n"
"v_pk_mul_f32 v[160:161], v[160:161], v[242:243] op_sel_hi:[1,0]\n"
"v_pk_mul_f32 v[162:163], v[162:163], v[242:243] op_sel_hi:[1,0]\n"
"v_pk_mul_f32 v[168:169], v[168:169], v[242:243] op_sel_hi:[1,0]\n"
"v_mfma_f32_16x16x32_bf16 v[200:203], a[92:95], v[100:103], v[200:203]\n"
"buffer_load_dwordx4 v19, s[44:47], s72 offen offset:3072 lds\n"
"v_pk_mul_f32 v[170:171], v[170:171], v[242:243] op_sel_hi:[1,0]\n"
"v_cvt_pk_bf16_f32 v160, v160, v161\n"
"v_mfma_f32_16x16x32_bf16 v[204:207], a[92:95], v[108:111], v[204:207]\n"
"v_cvt_pk_bf16_f32 v161, v162, v163\n"
"v_cvt_pk_bf16_f32 v162, v168, v169\n"
"v_cvt_pk_bf16_f32 v163, v170, v171\n"
"s_waitcnt lgkmcnt(7)\n"
"v_mfma_f32_16x16x32_bf16 v[208:211], a[96:99], v[96:99], v[208:211]\n"
"v_pk_mul_f32 v[164:165], v[164:165], v[244:245] op_sel_hi:[1,0]\n"
"v_pk_mul_f32 v[166:167], v[166:167], v[244:245] op_sel_hi:[1,0]\n"
"v_pk_mul_f32 v[172:173], v[172:173], v[244:245] op_sel_hi:[1,0]\n"
"v_mfma_f32_16x16x32_bf16 v[212:215], a[96:99], v[104:107], v[212:215]\n"
"s_add_u32 m0, m0, 0x1000\n"
"s_nop 0\n"
"buffer_load_dwordx4 v20, s[44:47], s72 offen offset:0 lds\n"
"s_waitcnt lgkmcnt(6)\n"
"v_mfma_f32_16x16x32_bf16 v[208:211], a[100:103], v[100:103], v[208:211]\n"
"v_pk_mul_f32 v[174:175], v[174:175], v[244:245] op_sel_hi:[1,0]\n"
"v_cvt_pk_bf16_f32 v164, v164, v165\n"
"v_cvt_pk_bf16_f32 v165, v166, v167\n"
"v_mfma_f32_16x16x32_bf16 v[212:215], a[100:103], v[108:111], v[212:215]\n"
"v_cvt_pk_bf16_f32 v166, v172, v173\n"
"v_cvt_pk_bf16_f32 v167, v174, v175\n"
"v_pk_mul_f32 v[176:177], v[176:177], v[242:243] op_sel_hi:[1,0]\n"
"s_waitcnt lgkmcnt(5)\n"
"v_mfma_f32_16x16x32_bf16 v[216:219], a[104:107], v[96:99], v[216:219]\n"
"buffer_load_dwordx4 v21, s[44:47], s72 offen offset:1024 lds\n"
"s_add_u32 s60, s60, 32768\n"
"s_cmp_eq_u32 s60, 163840\n"
"s_cselect_b32 s60, 0, s60\n"
"v_mfma_f32_16x16x32_bf16 v[220:223], a[104:107], v[104:107], v[220:223]\n"
"v_pk_mul_f32 v[178:179], v[178:179], v[242:243] op_sel_hi:[1,0]\n"
"v_pk_mul_f32 v[184:185], v[184:185], v[242:243] op_sel_hi:[1,0]\n"
"v_pk_mul_f32 v[186:187], v[186:187], v[242:243] op_sel_hi:[1,0]\n"
"s_waitcnt lgkmcnt(4)\n"
"v_mfma_f32_16x16x32_bf16 v[216:219], a[108:111], v[100:103], v[216:219]\n"
"v_cvt_pk_bf16_f32 v176, v176, v177\n"
"v_cvt_pk_bf16_f32 v177, v178, v179\n"
"v_cvt_pk_bf16_f32 v178, v184, v185\n"
"v_mfma_f32_16x16x32_bf16 v[220:223], a[108:111], v[108:111], v[220:223]\n"
"s_add_u32 m0, s60, s62\n"
"s_nop 0\n"
"buffer_load_dwordx4 v16, s[48:51], s72 offen offset:0 lds\n"
"s_waitcnt lgkmcnt(3)\n"
"v_mfma_f32_16x16x32_bf16 v[224:227], a[112:115], v[96:99], v[224:227]\n"
"v_cvt_pk_bf16_f32 v179, v186, v187\n"
"v_pk_mul_f32 v[180:181], v[180:181], v[244:245] op_sel_hi:[1,0]\n"
"v_pk_mul_f32 v[182:183], v[182:183], v[244:245] op_sel_hi:[1,0]\n"
"v_mfma_f32_16x16x32_bf16 v[228:231], a[112:115], v[104:107], v[228:231]\n"
"v_pk_mul_f32 v[188:189], v[188:189], v[244:245] op_sel_hi:[1,0]\n"
"v_pk_mul_f32 v[190:191], v[190:191], v[244:245] op_sel_hi:[1,0]\n"
"v_cvt_pk_bf16_f32 v180, v180, v181\n"
"s_waitcnt lgkmcnt(2)\n"
"v_mfma_f32_16x16x32_bf16 v[224:227], a[116:119], v[100:103], v[224:227]\n"
"buffer_load_dwordx4 v17, s[48:51], s72 offen offset:1024 lds\n"
"v_cvt_pk_bf16_f32 v181, v182, v183\n"
"v_cvt_pk_bf16_f32 v182, v188, v189\n"
"v_mfma_f32_16x16x32_bf16 v[228:231], a[116:119], v[108:111], v[228:231]\n"
"v_cvt_pk_bf16_f32 v183, v190, v191\n"
"v_pk_mul_f32 v[192:193], v[192:193], v[242:243] op_sel_hi:[1,0]\n"
"v_pk_mul_f32 v[194:195], v[194:195], v[242:243] op_sel_hi:[1,0]\n"
"s_waitcnt lgkmcnt(1)\n"
"v_mfma_f32_16x16x32_bf16 v[232:235], a[120:123], v[96:99], v[232:235]\n"
"v_pk_mul_f32 v[200:201], v[200:201], v[242:243] op_sel_hi:[1,0]\n"
"v_pk_mul_f32 v[202:203], v[202:203], v[242:243] op_sel_hi:[1,0]\n"
"v_cvt_pk_bf16_f32 v192, v192, v193\n"
"v_mfma_f32_16x16x32_bf16 v[236:239], a[120:123], v[104:107], v[236:239]\n"
"buffer_load_dwordx4 v18, s[96:99], s72 offen offset:2048 lds\n"
"v_cvt_pk_bf16_f32 v193, v194, v195\n"
"v_cvt_pk_bf16_f32 v194, v200, v201\n"
"s_waitcnt lgkmcnt(0)\n"
"v_mfma_f32_16x16x32_bf16 v[232:235], a[124:127], v[100:103], v[232:235]\n"
"v_cvt_pk_bf16_f32 v195, v202, v203\n"
"v_pk_mul_f32 v[196:197], v[196:197], v[244:245] op_sel_hi:[1,0]\n"
"v_pk_mul_f32 v[198:199], v[198:199], v[244:245] op_sel_hi:[1,0]\n"
"v_mfma_f32_16x16x32_bf16 v[236:239], a[124:127], v[108:111], v[236:239]\n"
"v_pk_mul_f32 v[204:205], v[204:205], v[244:245] op_sel_hi:[1,0]\n"
"v_pk_mul_f32 v[206:207], v[206:207], v[244:245] op_sel_hi:[1,0]\n"
"v_cvt_pk_bf16_f32 v196, v196, v197\n"
"s_nop 7\n"
"v_cvt_pk_bf16_f32 v197, v198, v199\n"
"v_cvt_pk_bf16_f32 v198, v204, v205\n"
"v_cvt_pk_bf16_f32 v199, v206, v207\n"
"v_pk_mul_f32 v[208:209], v[208:209], v[242:243] op_sel_hi:[1,0]\n"
"v_pk_mul_f32 v[210:211], v[210:211], v[242:243] op_sel_hi:[1,0]\n"
"v_pk_mul_f32 v[216:217], v[216:217], v[242:243] op_sel_hi:[1,0]\n"
"v_pk_mul_f32 v[218:219], v[218:219], v[242:243] op_sel_hi:[1,0]\n"
"v_cvt_pk_bf16_f32 v208, v208, v209\n"
"v_cvt_pk_bf16_f32 v209, v210, v211\n"
"v_cvt_pk_bf16_f32 v210, v216, v217\n"
"v_cvt_pk_bf16_f32 v211, v218, v219\n"
"v_pk_mul_f32 v[212:213], v[212:213], v[244:245] op_sel_hi:[1,0]\n"
"v_pk_mul_f32 v[214:215], v[214:215], v[244:245] op_sel_hi:[1,0]\n"
"v_pk_mul_f32 v[220:221], v[220:221], v[244:245] op_sel_hi:[1,0]\n"
"v_pk_mul_f32 v[222:223], v[222:223], v[244:245] op_sel_hi:[1,0]\n"
"v_cvt_pk_bf16_f32 v212, v212, v213\n"
"v_cvt_pk_bf16_f32 v213, v214, v215\n"
"v_cvt_pk_bf16_f32 v214, v220, v221\n"
"v_cvt_pk_bf16_f32 v215, v222, v223\n"
"v_pk_mul_f32 v[224:225], v[224:225], v[242:243] op_sel_hi:[1,0]\n"
"v_pk_mul_f32 v[226:227], v[226:227], v[242:243] op_sel_hi:[1,0]\n"
"v_pk_mul_f32 v[232:233], v[232:233], v[242:243] op_sel_hi:[1,0]\n"
"v_pk_mul_f32 v[234:235], v[234:235], v[242:243] op_sel_hi:[1,0]\n"
"v_cvt_pk_bf16_f32 v224, v224, v225\n"
"v_cvt_pk_bf16_f32 v225, v226, v227\n"
"v_cvt_pk_bf16_f32 v226, v232, v233\n"
"v_cvt_pk_bf16_f32 v227, v234, v235\n"
"v_pk_mul_f32 v[228:229], v[228:229], v[244:245] op_sel_hi:[1,0]\n"
"v_pk_mul_f32 v[230:231], v[230:231], v[244:245] op_sel_hi:[1,0]\n"
"v_pk_mul_f32 v[236:237], v[236:237], v[244:245] op_sel_hi:[1,0]\n"
"v_pk_mul_f32 v[238:239], v[238:239], v[244:245] op_sel_hi:[1,0]\n"
"v_cvt_pk_bf16_f32 v228, v228, v229\n"
"v_cvt_pk_bf16_f32 v229, v230, v231\n"
"v_cvt_pk_bf16_f32 v230, v236, v237\n"
"v_cvt_pk_bf16_f32 v231, v238, v239\n"
"LCK%=:\n"
"s_cmp_lg_u64 s[80:81], 0\n"
"s_cbranch_scc1 LSLOW%=\n"
"LEP%=:\n"
"s_cmp_eq_u32 %14, 4\n"
"s_cbranch_scc1 LZ%=\n"
"v_accvgpr_write_b32 a152, 0\n"
"v_accvgpr_write_b32 a153, 0\n"
"v_accvgpr_write_b32 a154, 0\n"
"v_accvgpr_write_b32 a155, 0\n"
"s_waitcnt vmcnt(40)\n"
"v_accvgpr_write_b32 a168, 0\n"
"v_accvgpr_write_b32 a169, 0\n"
"v_accvgpr_write_b32 a170, 0\n"
"v_accvgpr_write_b32 a171, 0\n"
"LZ%=:\n"
"v_xor_b32 v22, 0, v5\n"
"v_xor_b32 v23, 32, v5\n"
"v_xor_b32 v24, 64, v5\n"
"v_xor_b32 v25, 96, v5\n"
"v_add_u32 v26, s59, v22\n"
"v_add_u32 v27, s59, v23\n"
"v_add_u32 v28, s59, v24\n"
"v_add_u32 v29, s59, v25\n"
"v_add_u32 v30, %13, v0\n"
"v_add_u32 v31, 57344, v30\n"
"s_mov_b32 s61, -2048\n"
"s_mov_b32 s56, 6\n"
"s_mov_b32 s42, 0\n"
"v_readlane_b32 s64, v2, 40\n"
"v_readlane_b32 s65, v2, 41\n"
"v_bfe_u32 v33, v0, 4, 4\n"
"v_mov_b32 v35, 1\n"
"s_mov_b32 s94, 0\n"
"v_mov_b32 v32, s64\n"
"v_lshl_add_u32 v33, v33, 2, s65\n"
"s_cmp_eq_u32 %15, 0\n"
"s_cselect_b32 s83, 1, -1\n"
"s_waitcnt vmcnt(15)\n"
"s_barrier\n"
"ds_read_b64_tr_b16 a[0:1], v26 offset:0\n"
"ds_read_b64_tr_b16 a[2:3], v26 offset:2048\n"
"ds_read_b64_tr_b16 a[4:5], v26 offset:4096\n"
"ds_read_b64_tr_b16 a[6:7], v26 offset:8192\n"
"ds_read_b64_tr_b16 a[8:9], v26 offset:10240\n"
"ds_read_b64_tr_b16 a[10:11], v26 offset:12288\n"
"ds_read_b64_tr_b16 a[12:13], v26 offset:16384\n"
"ds_read_b64_tr_b16 a[14:15], v26 offset:18432\n"
"ds_read_b64_tr_b16 a[16:17], v26 offset:20480\n"
"ds_read_b64_tr_b16 a[18:19], v26 offset:24576\n"
"ds_read_b64_tr_b16 a[20:21], v26 offset:26624\n"
"ds_read_b64_tr_b16 a[22:23], v26 offset:28672\n"
"ds_read_b64_tr_b16 a[24:25], v27 offset:0\n"
"ds_read_b64_tr_b16 a[26:27], v27 offset:2048\n"
"ds_read_b64_tr_b16 a[28:29], v27 offset:4096\n"
"ds_read_b64_tr_b16 a[30:31], v27 offset:8192\n"
"ds_read_b64_tr_b16 a[32:33], v27 offset:10240\n"
"ds_read_b64_tr_b16 a[34:35], v27 offset:12288\n"
"ds_read_b64_tr_b16 a[36:37], v27 offset:16384\n"
"ds_read_b64_tr_b16 a[38:39], v27 offset:18432\n"
"ds_read_b64_tr_b16 a[40:41], v27 offset:20480\n"
"ds_read_b64_tr_b16 a[42:43], v27 offset:24576\n"
"ds_read_b64_tr_b16 a[44:45], v27 offset:26624\n"
"ds_read_b64_tr_b16 a[46:47], v27 offset:28672\n"
"s_add_u32 s59, s59, 32768\n"
"s_cmp_eq_u32 s59, 163840\n"
"s_cselect_b32 s59, 0, s59\n"
"LEI%=:\n"
"s_waitcnt lgkmcnt(15)\n"
"v_mfma_f32_16x16x32_bf16 v[48:51], a[0:3], v[112:115], 0\n"
"ds_read_b64_tr_b16 a[48:49], v28 offset:0\n"
"v_add_u32 v26, s59, v22\n"
"v_mfma_f32_16x16x32_bf16 v[52:55], a[0:3], v[116:119], 0\n"
"ds_read_b64_tr_b16 a[50:51], v28 offset:2048\n"
"v_add_u32 v27, s59, v23\n"
"v_mfma_f32_16x16x32_bf16 v[48:51], a[4:7], v[128:131], v[48:51]\n"
"ds_read_b64_tr_b16 a[52:53], v28 offset:4096\n"
"v_mfma_f32_16x16x32_bf16 v[52:55], a[4:7], v[132:135], v[52:55]\n"
"ds_read_b64_tr_b16 a[54:55], v28 offset:8192\n"
"buffer_load_dwordx4 v19, s[96:99], s72 offen offset:3072 lds\n"
"v_mfma_f32_16x16x32_bf16 v[48:51], a[8:11], v[144:147], v[48:51]\n"
"ds_read_b64_tr_b16 a[56:57], v28 offset:10240\n"
"v_cvt_pk_bf16_f32 v90, v72, v73\n"
"v_mfma_f32_16x16x32_bf16 v[52:55], a[8:11], v[148:151], v[52:55]\n"
"ds_read_b64_tr_b16 a[58:59], v28 offset:12288\n"
"v_cvt_pk_bf16_f32 v91, v74, v75\n"
"v_mfma_f32_16x16x32_bf16 v[48:51], a[12:15], v[160:163], v[48:51]\n"
"ds_read_b64_tr_b16 a[60:61], v28 offset:16384\n"
"s_waitcnt lgkmcnt(15)\n"
"v_mfma_f32_16x16x32_bf16 v[52:55], a[12:15], v[164:167], v[52:55]\n"
"ds_read_b64_tr_b16 a[62:63], v28 offset:18432\n"
"v_cvt_pk_bf16_f32 v94, v76, v77\n"
"v_mfma_f32_16x16x32_bf16 v[48:51], a[16:19], v[176:179], v[48:51]\n"
"ds_read_b64_tr_b16 a[64:65], v28 offset:20480\n"
"s_add_u32 m0, m0, 0x1000\n"
"v_mfma_f32_16x16x32_bf16 v[52:55], a[16:19], v[180:183], v[52:55]\n"
"ds_read_b64_tr_b16 a[66:67], v28 offset:24576\n"
"v_cvt_pk_bf16_f32 v95, v78, v79\n"
"v_mfma_f32_16x16x32_bf16 v[48:51], a[20:23], v[192:195], v[48:51]\n"
"ds_read_b64_tr_b16 a[68:69], v28 offset:26624\n"
"buffer_store_dwordx4 v[88:91], v30, s[40:43], s61 offen offset:1024 sc0 sc1\n"
"v_mfma_f32_16x16x32_bf16 v[52:55], a[20:23], v[196:199], v[52:55]\n"
"ds_read_b64_tr_b16 a[70:71], v28 offset:28672\n"
"buffer_load_dwordx4 v20, s[96:99], s72 offen offset:0 lds\n"
"v_mfma_f32_16x16x32_bf16 v[56:59], a[24:27], v[112:115], 0\n"
"ds_read_b64_tr_b16 a[72:73], v29 offset:0\n"
"s_add_u32 s60, s60, 32768\n"
"v_mfma_f32_16x16x32_bf16 v[60:63], a[24:27], v[116:119], 0\n"
"ds_read_b64_tr_b16 a[74:75], v29 offset:2048\n"
"s_cmp_eq_u32 s60, 163840\n"
"v_mfma_f32_16x16x32_bf16 v[56:59], a[28:31], v[128:131], v[56:59]\n"
"ds_read_b64_tr_b16 a[76:77], v29 offset:4096\n"
"s_waitcnt lgkmcnt(15)\n"
"v_mfma_f32_16x16x32_bf16 v[60:63], a[28:31], v[132:135], v[60:63]\n"
"ds_read_b64_tr_b16 a[78:79], v29 offset:8192\n"
"s_cselect_b32 s60, 0, s60\n"
"v_mfma_f32_16x16x32_bf16 v[56:59], a[32:35], v[144:147], v[56:59]\n"
"ds_read_b64_tr_b16 a[80:81], v29 offset:10240\n"
"buffer_store_dwordx4 v[92:95], v31, s[40:43], s61 offen offset:1024 sc0 sc1\n"
"v_mfma_f32_16x16x32_bf16 v[60:63], a[32:35], v[148:151], v[60:63]\n"
"ds_read_b64_tr_b16 a[82:83], v29 offset:12288\n"
"s_mov_b32 s42, s71\n"
"v_mfma_f32_16x16x32_bf16 v[56:59], a[36:39], v[160:163], v[56:59]\n"
"ds_read_b64_tr_b16 a[84:85], v29 offset:16384\n"
"s_add_u32 s61, s61, 2048\n"
"v_mfma_f32_16x16x32_bf16 v[60:63], a[36:39], v[164:167], v[60:63]\n"
"ds_read_b64_tr_b16 a[86:87], v29 offset:18432\n"
"buffer_load_dwordx4 v21, s[96:99], s72 offen offset:1024 lds\n"
"v_mfma_f32_16x16x32_bf16 v[56:59], a[40:43], v[176:179], v[56:59]\n"
"ds_read_b64_tr_b16 a[88:89], v29 offset:20480\n"
"s_add_u32 s72, s72, 128\n"
"v_mfma_f32_16x16x32_bf16 v[60:63], a[40:43], v[180:183], v[60:63]\n"
"ds_read_b64_tr_b16 a[90:91], v29 offset:24576\n"
"s_add_u32 m0, s60, s62\n"
"v_mfma_f32_16x16x32_bf16 v[56:59], a[44:47], v[192:195], v[56:59]\n"
"ds_read_b64_tr_b16 a[92:93], v29 offset:26624\n"
"s_waitcnt lgkmcnt(15)\n"
"v_mfma_f32_16x16x32_bf16 v[60:63], a[44:47], v[196:199], v[60:63]\n"
"ds_read_b64_tr_b16 a[94:95], v29 offset:28672\n"
"s_waitcnt vmcnt(14)\n"
"s_barrier\n"
"v_mfma_f32_16x16x32_bf16 v[64:67], a[48:51], v[112:115], 0\n"
"ds_read_b64_tr_b16 a[0:1], v26 offset:0\n"
"s_cmp_lt_u32 s72, 896\n"
"v_mfma_f32_16x16x32_bf16 v[68:71], a[48:51], v[116:119], 0\n"
"ds_read_b64_tr_b16 a[2:3], v26 offset:2048\n"
"v_add_u32 v28, s59, v24\n"
"v_mfma_f32_16x16x32_bf16 v[64:67], a[52:55], v[128:131], v[64:67]\n"
"ds_read_b64_tr_b16 a[4:5], v26 offset:4096\n"
"s_cselect_b32 s46, s46, 0\n"
"v_mfma_f32_16x16x32_bf16 v[68:71], a[52:55], v[132:135], v[68:71]\n"
"ds_read_b64_tr_b16 a[6:7], v26 offset:8192\n"
"buffer_load_dwordx4 v16, s[44:47], s72 offen offset:0 lds\n"
"v_mfma_f32_16x16x32_bf16 v[64:67], a[56:59], v[144:147], v[64:67]\n"
"ds_read_b64_tr_b16 a[8:9], v26 offset:10240\n"
"s_cselect_b32 s50, s50, 0\n"
"v_mfma_f32_16x16x32_bf16 v[68:71], a[56:59], v[148:151], v[68:71]\n"
"ds_read_b64_tr_b16 a[10:11], v26 offset:12288\n"
"s_cselect_b32 s98, s98, 0\n"
"v_mfma_f32_16x16x32_bf16 v[64:67], a[60:63], v[160:163], v[64:67]\n"
"ds_read_b64_tr_b16 a[12:13], v26 offset:16384\n"
"s_waitcnt lgkmcnt(15)\n"
"v_mfma_f32_16x16x32_bf16 v[68:71], a[60:63], v[164:167], v[68:71]\n"
"ds_read_b64_tr_b16 a[14:15], v26 offset:18432\n"
"s_cmp_eq_u32 s56, s83\n"
"v_mfma_f32_16x16x32_bf16 v[64:67], a[64:67], v[176:179], v[64:67]\n"
"ds_read_b64_tr_b16 a[16:17], v26 offset:20480\n"
"s_cselect_b32 s94, s71, 0\n"
"v_mfma_f32_16x16x32_bf16 v[68:71], a[64:67], v[180:183], v[68:71]\n"
"ds_read_b64_tr_b16 a[18:19], v26 offset:24576\n"
"buffer_store_dword v35, v32, s[92:95], 0 offen sc0 sc1\n"
"v_mfma_f32_16x16x32_bf16 v[64:67], a[68:71], v[192:195], v[64:67]\n"
"ds_read_b64_tr_b16 a[20:21], v26 offset:26624\n"
"v_mfma_f32_16x16x32_bf16 v[68:71], a[68:71], v[196:199], v[68:71]\n"
"ds_read_b64_tr_b16 a[22:23], v26 offset:28672\n"
"buffer_load_dwordx4 v17, s[44:47], s72 offen offset:1024 lds\n"
"v_mfma_f32_16x16x32_bf16 v[72:75], a[72:75], v[112:115], 0\n"
"ds_read_b64_tr_b16 a[24:25], v27 offset:0\n"
"v_add_u32 v29, s59, v25\n"
"v_mfma_f32_16x16x32_bf16 v[76:79], a[72:75], v[116:119], 0\n"
"ds_read_b64_tr_b16 a[26:27], v27 offset:2048\n"
"s_add_u32 s59, s59, 32768\n"
"v_mfma_f32_16x16x32_bf16 v[72:75], a[76:79], v[128:131], v[72:75]\n"
"ds_read_b64_tr_b16 a[28:29], v27 offset:4096\n"
"s_waitcnt lgkmcnt(15)\n"
"v_mfma_f32_16x16x32_bf16 v[76:79], a[76:79], v[132:135], v[76:79]\n"
"ds_read_b64_tr_b16 a[30:31], v27 offset:8192\n"
"s_cmp_eq_u32 s59, 163840\n"
"v_mfma_f32_16x16x32_bf16 v[72:75], a[80:83], v[144:147], v[72:75]\n"
"ds_read_b64_tr_b16 a[32:33], v27 offset:10240\n"
"s_cselect_b32 s59, 0, s59\n"
"v_mfma_f32_16x16x32_bf16 v[76:79], a[80:83], v[148:151], v[76:79]\n"
"ds_read_b64_tr_b16 a[34:35], v27 offset:12288\n"
"v_mfma_f32_16x16x32_bf16 v[72:75], a[84:87], v[160:163], v[72:75]\n"
"ds_read_b64_tr_b16 a[36:37], v27 offset:16384\n"
"v_mfma_f32_16x16x32_bf16 v[76:79], a[84:87], v[164:167], v[76:79]\n"
"ds_read_b64_tr_b16 a[38:39], v27 offset:18432\n"
"buffer_load_dwordx4 v18, s[44:47], s72 offen offset:2048 lds\n"
"v_mfma_f32_16x16x32_bf16 v[72:75], a[88:91], v[176:179], v[72:75]\n"
"ds_read_b64_tr_b16 a[40:41], v27 offset:20480\n"
"v_mfma_f32_16x16x32_bf16 v[76:79], a[88:91], v[180:183], v[76:79]\n"
"ds_read_b64_tr_b16 a[42:43], v27 offset:24576\n"
"v_mfma_f32_16x16x32_bf16 v[72:75], a[92:95], v[192:195], v[72:75]\n"
"ds_read_b64_tr_b16 a[44:45], v27 offset:26624\n"
"s_waitcnt lgkmcnt(15)\n"
"v_mfma_f32_16x16x32_bf16 v[76:79], a[92:95], v[196:199], v[76:79]\n"
"ds_read_b64_tr_b16 a[46:47], v27 offset:28672\n"
"v_mfma_f32_16x16x32_bf16 v[48:51], a[0:3], v[208:211], v[48:51]\n"
"ds_read_b64_tr_b16 a[48:49], v28 offset:0\n"
"v_add_u32 v26, s59, v22\n"
"v_mfma_f32_16x16x32_bf16 v[52:55], a[0:3], v[212:215], v[52:55]\n"
"ds_read_b64_tr_b16 a[50:51], v28 offset:2048\n"
"v_add_u32 v27, s59, v23\n"
"v_mfma_f32_16x16x32_bf16 v[48:51], a[4:7], v[224:227], v[48:51]\n"
"ds_read_b64_tr_b16 a[52:53], v28 offset:4096\n"
"v_mfma_f32_16x16x32_bf16 v[52:55], a[4:7], v[228:231], v[52:55]\n"
"ds_read_b64_tr_b16 a[54:55], v28 offset:8192\n"
"buffer_load_dwordx4 v19, s[44:47], s72 offen offset:3072 lds\n"
"v_mfma_f32_16x16x32_bf16 v[48:51], a[8:11], a[140:143], v[48:51]\n"
"ds_read_b64_tr_b16 a[56:57], v28 offset:10240\n"
"v_mfma_f32_16x16x32_bf16 v[52:55], a[8:11], a[156:159], v[52:55]\n"
"ds_read_b64_tr_b16 a[58:59], v28 offset:12288\n"
"v_mfma_f32_16x16x32_bf16 v[48:51], a[12:15], a[144:147], v[48:51]\n"
"ds_read_b64_tr_b16 a[60:61], v28 offset:16384\n"
"s_waitcnt lgkmcnt(15)\n"
"v_mfma_f32_16x16x32_bf16 v[52:55], a[12:15], a[160:163], v[52:55]\n"
"ds_read_b64_tr_b16 a[62:63], v28 offset:18432\n"
"s_waitcnt vmcnt(17)\n"
"buffer_load_dword v34, v33, s[52:55], 0 offen sc0 sc1\n"
"v_mfma_f32_16x16x32_bf16 v[48:51], a[16:19], a[148:151], v[48:51]\n"
"ds_read_b64_tr_b16 a[64:65], v28 offset:20480\n"
"s_add_u32 m0, m0, 0x1000\n"
"v_mfma_f32_16x16x32_bf16 v[52:55], a[16:19], a[164:167], v[52:55]\n"
"ds_read_b64_tr_b16 a[66:67], v28 offset:24576\n"
"v_mfma_f32_16x16x32_bf16 v[48:51], a[20:23], a[152:155], v[48:51]\n"
"ds_read_b64_tr_b16 a[68:69], v28 offset:26624\n"
"v_mfma_f32_16x16x32_bf16 v[52:55], a[20:23], a[168:171], v[52:55]\n"
"ds_read_b64_tr_b16 a[70:71], v28 offset:28672\n"
"buffer_load_dwordx4 v20, s[44:47], s72 offen offset:0 lds\n"
"v_mfma_f32_16x16x32_bf16 v[56:59], a[24:27], v[208:211], v[56:59]\n"
"ds_read_b64_tr_b16 a[72:73], v29 offset:0\n"
"s_add_u32 s60, s60, 32768\n"
"v_mfma_f32_16x16x32_bf16 v[60:63], a[24:27], v[212:215], v[60:63]\n"
"ds_read_b64_tr_b16 a[74:75], v29 offset:2048\n"
"s_cmp_eq_u32 s60, 163840\n"
"v_mfma_f32_16x16x32_bf16 v[56:59], a[28:31], v[224:227], v[56:59]\n"
"ds_read_b64_tr_b16 a[76:77], v29 offset:4096\n"
"s_waitcnt lgkmcnt(15)\n"
"v_mfma_f32_16x16x32_bf16 v[60:63], a[28:31], v[228:231], v[60:63]\n"
"ds_read_b64_tr_b16 a[78:79], v29 offset:8192\n"
"s_cselect_b32 s60, 0, s60\n"
"v_mfma_f32_16x16x32_bf16 v[56:59], a[32:35], a[140:143], v[56:59]\n"
"ds_read_b64_tr_b16 a[80:81], v29 offset:10240\n"
"v_cvt_pk_bf16_f32 v80, v48, v49\n"
"v_mfma_f32_16x16x32_bf16 v[60:63], a[32:35], a[156:159], v[60:63]\n"
"ds_read_b64_tr_b16 a[82:83], v29 offset:12288\n"
"v_cvt_pk_bf16_f32 v81, v50, v51\n"
"v_mfma_f32_16x16x32_bf16 v[56:59], a[36:39], a[144:147], v[56:59]\n"
"ds_read_b64_tr_b16 a[84:85], v29 offset:16384\n"
"v_cvt_pk_bf16_f32 v84, v52, v53\n"
"v_mfma_f32_16x16x32_bf16 v[60:63], a[36:39], a[160:163], v[60:63]\n"
"ds_read_b64_tr_b16 a[86:87], v29 offset:18432\n"
"buffer_load_dwordx4 v21, s[44:47], s72 offen offset:1024 lds\n"
"v_mfma_f32_16x16x32_bf16 v[56:59], a[40:43], a[148:151], v[56:59]\n"
"ds_read_b64_tr_b16 a[88:89], v29 offset:20480\n"
"v_cvt_pk_bf16_f32 v85, v54, v55\n"
"v_mfma_f32_16x16x32_bf16 v[60:63], a[40:43], a[164:167], v[60:63]\n"
"ds_read_b64_tr_b16 a[90:91], v29 offset:24576\n"
"s_add_u32 m0, s60, s62\n"
"v_mfma_f32_16x16x32_bf16 v[56:59], a[44:47], a[152:155], v[56:59]\n"
"ds_read_b64_tr_b16 a[92:93], v29 offset:26624\n"
"s_waitcnt lgkmcnt(15)\n"
"v_mfma_f32_16x16x32_bf16 v[60:63], a[44:47], a[168:171], v[60:63]\n"
"ds_read_b64_tr_b16 a[94:95], v29 offset:28672\n"
"s_waitcnt vmcnt(16)\n"
"s_barrier\n"
"v_mfma_f32_16x16x32_bf16 v[64:67], a[48:51], v[208:211], v[64:67]\n"
"ds_read_b64_tr_b16 a[0:1], v26 offset:0\n"
"v_mfma_f32_16x16x32_bf16 v[68:71], a[48:51], v[212:215], v[68:71]\n"
"ds_read_b64_tr_b16 a[2:3], v26 offset:2048\n"
"v_add_u32 v28, s59, v24\n"
"v_mfma_f32_16x16x32_bf16 v[64:67], a[52:55], v[224:227], v[64:67]\n"
"ds_read_b64_tr_b16 a[4:5], v26 offset:4096\n"
"v_mfma_f32_16x16x32_bf16 v[68:71], a[52:55], v[228:231], v[68:71]\n"
"ds_read_b64_tr_b16 a[6:7], v26 offset:8192\n"
"buffer_load_dwordx4 v16, s[48:51], s72 offen offset:0 lds\n"
"v_mfma_f32_16x16x32_bf16 v[64:67], a[56:59], a[140:143], v[64:67]\n"
"ds_read_b64_tr_b16 a[8:9], v26 offset:10240\n"
"v_cvt_pk_bf16_f32 v82, v56, v57\n"
"v_mfma_f32_16x16x32_bf16 v[68:71], a[56:59], a[156:159], v[68:71]\n"
"ds_read_b64_tr_b16 a[10:11], v26 offset:12288\n"
"v_cvt_pk_bf16_f32 v83, v58, v59\n"
"v_mfma_f32_16x16x32_bf16 v[64:67], a[60:63], a[144:147], v[64:67]\n"
"ds_read_b64_tr_b16 a[12:13], v26 offset:16384\n"
"s_waitcnt lgkmcnt(15)\n"
"v_mfma_f32_16x16x32_bf16 v[68:71], a[60:63], a[160:163], v[68:71]\n"
"ds_read_b64_tr_b16 a[14:15], v26 offset:18432\n"
"v_cvt_pk_bf16_f32 v86, v60, v61\n"
"v_mfma_f32_16x16x32_bf16 v[64:67], a[64:67], a[148:151], v[64:67]\n"
"ds_read_b64_tr_b16 a[16:17], v26 offset:20480\n"
"v_cvt_pk_bf16_f32 v87, v62, v63\n"
"v_mfma_f32_16x16x32_bf16 v[68:71], a[64:67], a[164:167], v[68:71]\n"
"ds_read_b64_tr_b16 a[18:19], v26 offset:24576\n"
"buffer_store_dwordx4 v[80:83], v30, s[40:43], s61 offen offset:0 sc0 sc1\n"
"v_mfma_f32_16x16x32_bf16 v[64:67], a[68:71], a[152:155], v[64:67]\n"
"ds_read_b64_tr_b16 a[20:21], v26 offset:26624\n"
"v_mfma_f32_16x16x32_bf16 v[68:71], a[68:71], a[168:171], v[68:71]\n"
"ds_read_b64_tr_b16 a[22:23], v26 offset:28672\n"
"buffer_load_dwordx4 v17, s[48:51], s72 offen offset:1024 lds\n"
"v_mfma_f32_16x16x32_bf16 v[72:75], a[72:75], v[208:211], v[72:75]\n"
"ds_read_b64_tr_b16 a[24:25], v27 offset:0\n"
"v_add_u32 v29, s59, v25\n"
"v_mfma_f32_16x16x32_bf16 v[76:79], a[72:75], v[212:215], v[76:79]\n"
"ds_read_b64_tr_b16 a[26:27], v27 offset:2048\n"
"s_add_u32 s59, s59, 32768\n"
"v_mfma_f32_16x16x32_bf16 v[72:75], a[76:79], v[224:227], v[72:75]\n"
"ds_read_b64_tr_b16 a[28:29], v27 offset:4096\n"
"s_waitcnt lgkmcnt(15)\n"
"v_mfma_f32_16x16x32_bf16 v[76:79], a[76:79], v[228:231], v[76:79]\n"
"ds_read_b64_tr_b16 a[30:31], v27 offset:8192\n"
"s_cmp_eq_u32 s59, 163840\n"
"v_mfma_f32_16x16x32_bf16 v[72:75], a[80:83], a[140:143], v[72:75]\n"
"ds_read_b64_tr_b16 a[32:33], v27 offset:10240\n"
"s_cselect_b32 s59, 0, s59\n"
"v_mfma_f32_16x16x32_bf16 v[76:79], a[80:83], a[156:159], v[76:79]\n"
"ds_read_b64_tr_b16 a[34:35], v27 offset:12288\n"
"buffer_store_dwordx4 v[84:87], v31, s[40:43], s61 offen offset:0 sc0 sc1\n"
"v_mfma_f32_16x16x32_bf16 v[72:75], a[84:87], a[144:147], v[72:75]\n"
"ds_read_b64_tr_b16 a[36:37], v27 offset:16384\n"
"v_cvt_pk_bf16_f32 v88, v64, v65\n"
"v_mfma_f32_16x16x32_bf16 v[76:79], a[84:87], a[160:163], v[76:79]\n"
"ds_read_b64_tr_b16 a[38:39], v27 offset:18432\n"
"buffer_load_dwordx4 v18, s[96:99], s72 offen offset:2048 lds\n"
"v_mfma_f32_16x16x32_bf16 v[72:75], a[88:91], a[148:151], v[72:75]\n"
"ds_read_b64_tr_b16 a[40:41], v27 offset:20480\n"
"v_cvt_pk_bf16_f32 v89, v66, v67\n"
"v_mfma_f32_16x16x32_bf16 v[76:79], a[88:91], a[164:167], v[76:79]\n"
"ds_read_b64_tr_b16 a[42:43], v27 offset:24576\n"
"v_cvt_pk_bf16_f32 v92, v68, v69\n"
"v_mfma_f32_16x16x32_bf16 v[72:75], a[92:95], a[152:155], v[72:75]\n"
"ds_read_b64_tr_b16 a[44:45], v27 offset:26624\n"
"s_waitcnt lgkmcnt(15)\n"
"v_mfma_f32_16x16x32_bf16 v[76:79], a[92:95], a[168:171], v[76:79]\n"
"ds_read_b64_tr_b16 a[46:47], v27 offset:28672\n"
"v_cvt_pk_bf16_f32 v93, v70, v71\n"
"s_sub_u32 s56, s56, 1\n"
"s_cbranch_scc0 LEI%=\n"
"s_nop 7\n"
"v_cvt_pk_bf16_f32 v90, v72, v73\n"
"v_cvt_pk_bf16_f32 v91, v74, v75\n"
"v_cvt_pk_bf16_f32 v94, v76, v77\n"
"v_cvt_pk_bf16_f32 v95, v78, v79\n"
"buffer_store_dwordx4 v[88:91], v30, s[40:43], s61 offen offset:1024 sc0 sc1\n"
"buffer_store_dwordx4 v[92:95], v31, s[40:43], s61 offen offset:1024 sc0 sc1\n"
"s_mov_b64 s[56:57], %18\n"
"s_and_b32 s57, s57, 0xffff\n"
"s_mov_b32 s58, 1835008\n"
"s_mov_b32 s59, 0x20000\n"
"s_mov_b64 s[60:61], %19\n"
"s_and_b32 s61, s61, 0xffff\n"
"s_mov_b64 s[62:63], s[58:59]\n"
"s_mov_b64 s[44:45], s[52:53]\n"
"s_mov_b64 s[46:47], s[54:55]\n"
"v_readlane_b32 s66, v2, 46\n"
"v_readlane_b32 s67, v2, 47\n"
"v_readlane_b32 s68, v2, 48\n"
"v_readlane_b32 s69, v2, 49\n"
"v_bfe_u32 v16, v0, 4, 4\n"
"v_mul_u32_u24 v17, 896, v16\n"
"v_lshrrev_b32 v18, 8, v0\n"
"v_lshl_add_u32 v17, v18, 3, v17\n"
"s_waitcnt vmcnt(9)\n"
"v_cmp_ne_u32 vcc, 1, v34\n"
"s_nop 4\n"
"s_cbranch_vccz LRA%=\n"
"s_mov_b32 s73, 0x40000\n"
"LRPA%=:\n"
"buffer_load_dword v34, v33, s[52:55], 0 offen sc0 sc1\n"
"s_waitcnt vmcnt(0)\n"
"v_cmp_ne_u32 vcc, 1, v34\n"
"s_nop 4\n"
"s_cbranch_vccz LRA%=\n"
"s_sleep 2\n"
"s_sub_u32 s73, s73, 1\n"
"s_cbranch_scc0 LRPA%=\n"
"LRA%=:\n"
"buffer_load_dwordx4 v[112:115], v0, s[44:47], s66 offen sc0 sc1\n"
"s_add_u32 s66, s66, 1835008\n"
"buffer_load_dwordx4 v[116:119], v0, s[44:47], s66 offen sc0 sc1\n"
"s_add_u32 s66, s66, 1835008\n"
"buffer_load_dwordx4 v[120:123], v0, s[44:47], s66 offen sc0 sc1\n"
"s_add_u32 s66, s66, 1835008\n"
"buffer_load_dwordx4 v[124:127], v0, s[44:47], s66 offen sc0 sc1\n"
"s_add_u32 s66, s66, 1835008\n"
"buffer_load_dwordx4 v[128:131], v0, s[44:47], s66 offen sc0 sc1\n"
"s_add_u32 s66, s66, 1835008\n"
"buffer_load_dwordx4 v[132:135], v0, s[44:47], s66 offen sc0 sc1\n"
"s_add_u32 s66, s66, 1835008\n"
"buffer_load_dwordx4 v[136:139], v0, s[44:47], s66 offen sc0 sc1\n"
"s_add_u32 s66, s66, 1835008\n"
"buffer_load_dwordx4 v[140:143], v0, s[44:47], s66 offen sc0 sc1\n"
"s_add_u32 s66, s66, 1835008\n"
"buffer_load_dwordx4 v[144:147], v0, s[44:47], s66 offen sc0 sc1\n"
"s_add_u32 s66, s66, 1835008\n"
"buffer_load_dwordx4 v[148:151], v0, s[44:47], s66 offen sc0 sc1\n"
"s_add_u32 s66, s66, 1835008\n"
"buffer_load_dwordx4 v[152:155], v0, s[44:47], s66 offen sc0 sc1\n"
"s_add_u32 s66, s66, 1835008\n"
"buffer_load_dwordx4 v[156:159], v0, s[44:47], s66 offen sc0 sc1\n"
"s_add_u32 s66, s66, 1835008\n"
"buffer_load_dwordx4 v[160:163], v0, s[44:47], s66 offen sc0 sc1\n"
"s_add_u32 s66, s66, 1835008\n"
"buffer_load_dwordx4 v[164:167], v0, s[44:47], s66 offen sc0 sc1\n"
"s_add_u32 s66, s66, 1835008\n"
"buffer_load_dwordx4 v[168:171], v0, s[44:47], s66 offen sc0 sc1\n"
"s_add_u32 s66, s66, 1835008\n"
"buffer_load_dwordx4 v[172:175], v0, s[44:47], s66 offen sc0 sc1\n"
"s_add_u32 s66, s66, 1835008\n"
"buffer_load_dwordx2 v[36:37], v17, s[56:59], s67 offen offset:0\n"
"buffer_load_dwordx2 v[38:39], v17, s[56:59], s67 offen offset:32\n"
"buffer_load_dwordx2 v[40:41], v17, s[56:59], s69 offen offset:0\n"
"buffer_load_dwordx2 v[42:43], v17, s[56:59], s69 offen offset:32\n"
"s_waitcnt vmcnt(20)\n"
"s_barrier\n"
"s_cmp_eq_u32 %15, 0\n"
"s_cbranch_scc0 LRW%=\n"
"buffer_store_dword v35, v32, s[52:55], 0 offen offset:128 sc0 sc1\n"
"LRW%=:\n"
"s_waitcnt vmcnt(0)\n"
"v_mfma_f32_16x16x32_bf16 v[56:59], a[240:243], v[36:39], 0\n"
"v_mfma_f32_16x16x32_bf16 v[60:63], a[244:247], v[36:39], 0\n"
"v_mfma_f32_16x16x32_bf16 v[48:51], a[240:243], v[112:115], 0\n"
"v_mfma_f32_16x16x32_bf16 v[52:55], a[244:247], v[112:115], 0\n"
"v_mfma_f32_16x16x32_bf16 v[56:59], a[240:243], v[116:119], v[56:59]\n"
"v_mfma_f32_16x16x32_bf16 v[60:63], a[244:247], v[116:119], v[60:63]\n"
"v_mfma_f32_16x16x32_bf16 v[48:51], a[240:243], v[120:123], v[48:51]\n"
"v_mfma_f32_16x16x32_bf16 v[52:55], a[244:247], v[120:123], v[52:55]\n"
"v_mfma_f32_16x16x32_bf16 v[56:59], a[240:243], v[124:127], v[56:59]\n"
"v_mfma_f32_16x16x32_bf16 v[60:63], a[244:247], v[124:127], v[60:63]\n"
"v_mfma_f32_16x16x32_bf16 v[48:51], a[240:243], v[128:131], v[48:51]\n"
"v_mfma_f32_16x16x32_bf16 v[52:55], a[244:247], v[128:131], v[52:55]\n"
"v_mfma_f32_16x16x32_bf16 v[56:59], a[240:243], v[132:135], v[56:59]\n"
"v_mfma_f32_16x16x32_bf16 v[60:63], a[244:247], v[132:135], v[60:63]\n"
"v_mfma_f32_16x16x32_bf16 v[48:51], a[240:243], v[136:139], v[48:51]\n"
"v_mfma_f32_16x16x32_bf16 v[52:55], a[244:247], v[136:139], v[52:55]\n"
"v_mfma_f32_16x16x32_bf16 v[56:59], a[240:243], v[140:143], v[56:59]\n"
"v_mfma_f32_16x16x32_bf16 v[60:63], a[244:247], v[140:143], v[60:63]\n"
"v_mfma_f32_16x16x32_bf16 v[48:51], a[240:243], v[144:147], v[48:51]\n"
"v_mfma_f32_16x16x32_bf16 v[52:55], a[244:247], v[144:147], v[52:55]\n"
"v_mfma_f32_16x16x32_bf16 v[56:59], a[240:243], v[148:151], v[56:59]\n"
"v_mfma_f32_16x16x32_bf16 v[60:63], a[244:247], v[148:151], v[60:63]\n"
"v_mfma_f32_16x16x32_bf16 v[48:51], a[240:243], v[152:155], v[48:51]\n"
"v_mfma_f32_16x16x32_bf16 v[52:55], a[244:247], v[152:155], v[52:55]\n"
"v_mfma_f32_16x16x32_bf16 v[56:59], a[240:243], v[156:159], v[56:59]\n"
"v_mfma_f32_16x16x32_bf16 v[60:63], a[244:247], v[156:159], v[60:63]\n"
"v_mfma_f32_16x16x32_bf16 v[48:51], a[240:243], v[160:163], v[48:51]\n"
"v_mfma_f32_16x16x32_bf16 v[52:55], a[244:247], v[160:163], v[52:55]\n"
"v_mfma_f32_16x16x32_bf16 v[56:59], a[240:243], v[164:167], v[56:59]\n"
"v_mfma_f32_16x16x32_bf16 v[60:63], a[244:247], v[164:167], v[60:63]\n"
"v_mfma_f32_16x16x32_bf16 v[48:51], a[240:243], v[168:171], v[48:51]\n"
"v_mfma_f32_16x16x32_bf16 v[52:55], a[244:247], v[168:171], v[52:55]\n"
"v_mfma_f32_16x16x32_bf16 v[56:59], a[240:243], v[172:175], v[56:59]\n"
"v_mfma_f32_16x16x32_bf16 v[60:63], a[244:247], v[172:175], v[60:63]\n"
"s_nop 7\n"
"v_pk_add_f32 v[48:49], v[48:49], v[56:57]\n"
"v_pk_add_f32 v[50:51], v[50:51], v[58:59]\n"
"v_pk_add_f32 v[52:53], v[52:53], v[60:61]\n"
"v_pk_add_f32 v[54:55], v[54:55], v[62:63]\n"
"v_cvt_pk_bf16_f32 v64, v48, v49\n"
"v_cvt_pk_bf16_f32 v65, v50, v51\n"
"v_cvt_pk_bf16_f32 v66, v52, v53\n"
"v_cvt_pk_bf16_f32 v67, v54, v55\n"
"buffer_store_dwordx2 v[64:65], v17, s[60:63], s67 offen offset:0\n"
"buffer_store_dwordx2 v[66:67], v17, s[60:63], s67 offen offset:32\n"
"s_cmp_eq_u32 %15, 3\n"
"s_cbranch_scc1 LRE%=\n"
"v_add_u32 v33, 128, v33\n"
"s_mov_b32 s73, 0x40000\n"
"LRSA%=:\n"
"buffer_load_dword v34, v33, s[52:55], 0 offen sc0 sc1\n"
"s_waitcnt vmcnt(0)\n"
"v_cmp_ne_u32 vcc, 1, v34\n"
"s_nop 4\n"
"s_cbranch_vccz LRG%=\n"
"s_sleep 2\n"
"s_sub_u32 s73, s73, 1\n"
"s_cbranch_scc0 LRSA%=\n"
"LRG%=:\n"
"buffer_load_dwordx4 v[176:179], v0, s[44:47], s68 offen sc0 sc1\n"
"s_add_u32 s68, s68, 1835008\n"
"buffer_load_dwordx4 v[180:183], v0, s[44:47], s68 offen sc0 sc1\n"
"s_add_u32 s68, s68, 1835008\n"
"buffer_load_dwordx4 v[184:187], v0, s[44:47], s68 offen sc0 sc1\n"
"s_add_u32 s68, s68, 1835008\n"
"buffer_load_dwordx4 v[188:191], v0, s[44:47], s68 offen sc0 sc1\n"
"s_add_u32 s68, s68, 1835008\n"
"buffer_load_dwordx4 v[192:195], v0, s[44:47], s68 offen sc0 sc1\n"
"s_add_u32 s68, s68, 1835008\n"
"buffer_load_dwordx4 v[196:199], v0, s[44:47], s68 offen sc0 sc1\n"
"s_add_u32 s68, s68, 1835008\n"
"buffer_load_dwordx4 v[200:203], v0, s[44:47], s68 offen sc0 sc1\n"
"s_add_u32 s68, s68, 1835008\n"
"buffer_load_dwordx4 v[204:207], v0, s[44:47], s68 offen sc0 sc1\n"
"s_add_u32 s68, s68, 1835008\n"
"buffer_load_dwordx4 v[208:211], v0, s[44:47], s68 offen sc0 sc1\n"
"s_add_u32 s68, s68, 1835008\n"
"buffer_load_dwordx4 v[212:215], v0, s[44:47], s68 offen sc0 sc1\n"
"s_add_u32 s68, s68, 1835008\n"
"buffer_load_dwordx4 v[216:219], v0, s[44:47], s68 offen sc0 sc1\n"
"s_add_u32 s68, s68, 1835008\n"
"buffer_load_dwordx4 v[220:223], v0, s[44:47], s68 offen sc0 sc1\n"
"s_add_u32 s68, s68, 1835008\n"
"buffer_load_dwordx4 v[224:227], v0, s[44:47], s68 offen sc0 sc1\n"
"s_add_u32 s68, s68, 1835008\n"
"buffer_load_dwordx4 v[228:231], v0, s[44:47], s68 offen sc0 sc1\n"
"s_add_u32 s68, s68, 1835008\n"
"buffer_load_dwordx4 v[232:235], v0, s[44:47], s68 offen sc0 sc1\n"
"s_add_u32 s68, s68, 1835008\n"
"buffer_load_dwordx4 v[236:239], v0, s[44:47], s68 offen sc0 sc1\n"
"s_add_u32 s68, s68, 1835008\n"
"v_mfma_f32_16x16x32_bf16 v[56:59], a[240:243], v[40:43], 0\n"
"v_mfma_f32_16x16x32_bf16 v[60:63], a[244:247], v[40:43], 0\n"
"s_waitcnt vmcnt(15)\n"
"v_mfma_f32_16x16x32_bf16 v[48:51], a[240:243], v[176:179], 0\n"
"v_mfma_f32_16x16x32_bf16 v[52:55], a[244:247], v[176:179], 0\n"
"s_waitcnt vmcnt(14)\n"
"v_mfma_f32_16x16x32_bf16 v[56:59], a[240:243], v[180:183], v[56:59]\n"
"v_mfma_f32_16x16x32_bf16 v[60:63], a[244:247], v[180:183], v[60:63]\n"
"s_waitcnt vmcnt(13)\n"
"v_mfma_f32_16x16x32_bf16 v[48:51], a[240:243], v[184:187], v[48:51]\n"
"v_mfma_f32_16x16x32_bf16 v[52:55], a[244:247], v[184:187], v[52:55]\n"
"s_waitcnt vmcnt(12)\n"
"v_mfma_f32_16x16x32_bf16 v[56:59], a[240:243], v[188:191], v[56:59]\n"
"v_mfma_f32_16x16x32_bf16 v[60:63], a[244:247], v[188:191], v[60:63]\n"
"s_waitcnt vmcnt(11)\n"
"v_mfma_f32_16x16x32_bf16 v[48:51], a[240:243], v[192:195], v[48:51]\n"
"v_mfma_f32_16x16x32_bf16 v[52:55], a[244:247], v[192:195], v[52:55]\n"
"s_waitcnt vmcnt(10)\n"
"v_mfma_f32_16x16x32_bf16 v[56:59], a[240:243], v[196:199], v[56:59]\n"
"v_mfma_f32_16x16x32_bf16 v[60:63], a[244:247], v[196:199], v[60:63]\n"
"s_waitcnt vmcnt(9)\n"
"v_mfma_f32_16x16x32_bf16 v[48:51], a[240:243], v[200:203], v[48:51]\n"
"v_mfma_f32_16x16x32_bf16 v[52:55], a[244:247], v[200:203], v[52:55]\n"
"s_waitcnt vmcnt(8)\n"
"v_mfma_f32_16x16x32_bf16 v[56:59], a[240:243], v[204:207], v[56:59]\n"
"v_mfma_f32_16x16x32_bf16 v[60:63], a[244:247], v[204:207], v[60:63]\n"
"s_waitcnt vmcnt(7)\n"
"v_mfma_f32_16x16x32_bf16 v[48:51], a[240:243], v[208:211], v[48:51]\n"
"v_mfma_f32_16x16x32_bf16 v[52:55], a[244:247], v[208:211], v[52:55]\n"
"s_waitcnt vmcnt(6)\n"
"v_mfma_f32_16x16x32_bf16 v[56:59], a[240:243], v[212:215], v[56:59]\n"
"v_mfma_f32_16x16x32_bf16 v[60:63], a[244:247], v[212:215], v[60:63]\n"
"s_waitcnt vmcnt(5)\n"
"v_mfma_f32_16x16x32_bf16 v[48:51], a[240:243], v[216:219], v[48:51]\n"
"v_mfma_f32_16x16x32_bf16 v[52:55], a[244:247], v[216:219], v[52:55]\n"
"s_waitcnt vmcnt(4)\n"
"v_mfma_f32_16x16x32_bf16 v[56:59], a[240:243], v[220:223], v[56:59]\n"
"v_mfma_f32_16x16x32_bf16 v[60:63], a[244:247], v[220:223], v[60:63]\n"
"s_waitcnt vmcnt(3)\n"
"v_mfma_f32_16x16x32_bf16 v[48:51], a[240:243], v[224:227], v[48:51]\n"
"v_mfma_f32_16x16x32_bf16 v[52:55], a[244:247], v[224:227], v[52:55]\n"
"s_waitcnt vmcnt(2)\n"
"v_mfma_f32_16x16x32_bf16 v[56:59], a[240:243], v[228:231], v[56:59]\n"
"v_mfma_f32_16x16x32_bf16 v[60:63], a[244:247], v[228:231], v[60:63]\n"
"s_waitcnt vmcnt(1)\n"
"v_mfma_f32_16x16x32_bf16 v[48:51], a[240:243], v[232:235], v[48:51]\n"
"v_mfma_f32_16x16x32_bf16 v[52:55], a[244:247], v[232:235], v[52:55]\n"
"s_waitcnt vmcnt(0)\n"
"v_mfma_f32_16x16x32_bf16 v[56:59], a[240:243], v[236:239], v[56:59]\n"
"v_mfma_f32_16x16x32_bf16 v[60:63], a[244:247], v[236:239], v[60:63]\n"
"s_nop 7\n"
"v_pk_add_f32 v[48:49], v[48:49], v[56:57]\n"
"v_pk_add_f32 v[50:51], v[50:51], v[58:59]\n"
"v_pk_add_f32 v[52:53], v[52:53], v[60:61]\n"
"v_pk_add_f32 v[54:55], v[54:55], v[62:63]\n"
"v_cvt_pk_bf16_f32 v64, v48, v49\n"
"v_cvt_pk_bf16_f32 v65, v50, v51\n"
"v_cvt_pk_bf16_f32 v66, v52, v53\n"
"v_cvt_pk_bf16_f32 v67, v54, v55\n"
"buffer_store_dwordx2 v[64:65], v17, s[60:63], s69 offen offset:0\n"
"buffer_store_dwordx2 v[66:67], v17, s[60:63], s69 offen offset:32\n"
"LRE%=:\n"
"LK2E:\n"
  :: "s"(ws),"s"(w0),"s"(wa),"s"(wb),"s"(n1),"s"(n2),"s"(qoff1),"s"(qoff2),"s"(rel1),"s"(rel2),"s"(obo),"s"(flo),"s"(hoff),"s"(poff),"s"(nfs),"s"(w),"s"(dbgo),"s"(pf),"s"(x),"s"(out),
     "{v0}"(lane*16),"{v1}"(lane*16+w*8192),"{v2}"(tab),"{v3}"(mb),"{v4}"(ed),"{v5}"(et) : "v6","v7","v8","v9","v10","v11","v12","v13","v14","v15","v16","v17","v18","v19","v20","v21","v22","v23","v24","v25","v26","v27","v28","v29","v30","v31","v32","v33","v34","v35","v36","v37","v38","v39","v40","v41","v42","v43","v44","v45","v46","v47","v48","v49","v50","v51","v52","v53","v54","v55","v56","v57","v58","v59","v60","v61","v62","v63","v64","v65","v66","v67","v68","v69","v70","v71","v72","v73","v74","v75","v76","v77","v78","v79","v80","v81","v82","v83","v84","v85","v86","v87","v88","v89","v90","v91","v92","v93","v94","v95","v96","v97","v98","v99","v100","v101","v102","v103","v104","v105","v106","v107","v108","v109","v110","v111","v112","v113","v114","v115","v116","v117","v118","v119","v120","v121","v122","v123","v124","v125","v126","v127","v128","v129","v130","v131","v132","v133","v134","v135","v136","v137","v138","v139","v140","v141","v142","v143","v144","v145","v146","v147","v148","v149","v150","v151","v152","v153","v154","v155","v156","v157","v158","v159","v160","v161","v162","v163","v164","v165","v166","v167","v168","v169","v170","v171","v172","v173","v174","v175","v176","v177","v178","v179","v180","v181","v182","v183","v184","v185","v186","v187","v188","v189","v190","v191","v192","v193","v194","v195","v196","v197","v198","v199","v200","v201","v202","v203","v204","v205","v206","v207","v208","v209","v210","v211","v212","v213","v214","v215","v216","v217","v218","v219","v220","v221","v222","v223","v224","v225","v226","v227","v228","v229","v230","v231","v232","v233","v234","v235","v236","v237","v238","v239","v240","v241","v242","v243","v244","v245","v246","v247","v248","v249","v250","v251","v252","v253","v254","v255","a0","a1","a2","a3","a4","a5","a6","a7","a8","a9","a10","a11","a12","a13","a14","a15","a16","a17","a18","a19","a20","a21","a22","a23","a24","a25","a26","a27","a28","a29","a30","a31","a32","a33","a34","a35","a36","a37","a38","a39","a40","a41","a42","a43","a44","a45","a46","a47","a48","a49","a50","a51","a52","a53","a54","a55","a56","a57","a58","a59","a60","a61","a62","a63","a64","a65","a66","a67","a68","a69","a70","a71","a72","a73","a74","a75","a76","a77","a78","a79","a80","a81","a82","a83","a84","a85","a86","a87","a88","a89","a90","a91","a92","a93","a94","a95","a96","a97","a98","a99","a100","a101","a102","a103","a104","a105","a106","a107","a108","a109","a110","a111","a112","a113","a114","a115","a116","a117","a118","a119","a120","a121","a122","a123","a124","a125","a126","a127","a128","a129","a130","a131","a132","a133","a134","a135","a136","a137","a138","a139","a140","a141","a142","a143","a144","a145","a146","a147","a148","a149","a150","a151","a152","a153","a154","a155","a156","a157","a158","a159","a160","a161","a162","a163","a164","a165","a166","a167","a168","a169","a170","a171","a172","a173","a174","a175","a176","a177","a178","a179","a180","a181","a182","a183","a184","a185","a186","a187","a188","a189","a190","a191","a192","a193","a194","a195","a196","a197","a198","a199","a200","a201","a202","a203","a204","a205","a206","a207","a208","a209","a210","a211","a212","a213","a214","a215","a216","a217","a218","a219","a220","a221","a222","a223","a224","a225","a226","a227","a228","a229","a230","a231","a232","a233","a234","a235","a236","a237","a238","a239","a240","a241","a242","a243","a244","a245","a246","a247","a248","a249","a250","a251","a252","a253","a254","a255","s40","s41","s42","s43","s44","s45","s46","s47","s48","s49","s50","s51","s52","s53","s54","s55","s56","s57","s58","s59","s60","s61","s62","s63","s64","s65","s66","s67","s68","s69","s70","s71","s72","s73","s74","s75","s76","s77","s78","s79","s80","s81","s82","s83","s84","s85","s86","s87","s88","s89","s90","s91","s92","s93","s94","s95","s96","s97","s98","s99","vcc","scc","memory");
}

extern "C" void kernel_launch(void* const* d_in, const int* in_sizes, int n_in, void* d_out, int out_size, void* d_ws, size_t ws_size, hipStream_t stream) {
  (void)in_sizes; (void)n_in; (void)out_size; (void)ws_size;
  (void)hipFuncSetAttribute((const void*)k1_proj, hipFuncAttributeMaxDynamicSharedMemorySize, 163840);
  (void)hipFuncSetAttribute((const void*)k2_attn, hipFuncAttributeMaxDynamicSharedMemorySize, 163840);
  hipLaunchKernelGGL(k1_proj, dim3(256), dim3(256), 163840, stream, d_in[0], d_in[1], d_in[2], d_in[3], d_in[4], d_in[7], d_ws);
  hipLaunchKernelGGL(k2_attn, dim3(256), dim3(256), 163840, stream, (const char*)d_in[5], (const char*)d_in[8], (char*)d_ws, (const char*)d_in[0], (char*)d_out);
}
```
